```python
import torch

def _softmax(x, axis=-1):
    return torch.softmax(x, dim=axis)

def _gelu(x):
    return torch.nn.functional.gelu(x, approximate='tanh')

def _silu(x):
    return torch.nn.functional.silu(x)

def _sigmoid(x):
    return torch.sigmoid(x)

def _layernorm(x, axis=-1, eps=1e-05):
    m = x.mean(dim=axis, keepdim=True)
    v = x.var(dim=axis, keepdim=True, unbiased=False)
    return (x - m) / torch.sqrt(v + eps)

def _scatter_add(tgt, idx, src, axis=0):
    return tgt.clone().index_add_(axis, idx, src)

def _segment_sum(src, seg, n):
    out = torch.zeros((n,) + src.shape[1:], dtype=src.dtype)
    return out.index_add_(0, seg, src)

def _pad(x, pw):
    flat = tuple((v for pair in reversed(pw) for v in pair))
    return torch.nn.functional.pad(x, flat)
kernel_name = 'standalone__tree_speculative_verify_tree_attention_over_candidate_token_tree__decode'

def setup_inputs():

    _w = lambda a: (lambda t: t.to(torch.bfloat16) if t.is_floating_point() else t)(torch.as_tensor(a))
    B, Hq, Hkv, T, C, D = (1, 32, 32, 1, 4096, 128)
    tree_mask = torch.zeros((B, T, T), dtype=float)
    for b in range(B):
        parent = torch.zeros(T, dtype=torch.int64)
        for i in range(1, T):
            parent[i] = int(torch.randint(0, i, (1,)).item())
        for i in range(T):
            j = i
            tree_mask[b, i, j] = 1.0
            while j > 0:
                j = parent[j]
                tree_mask[b, i, j] = 1.0
    return {'q': _w((lambda *s: torch.randn(*s, dtype=torch.bfloat16) * 0.8)(B, Hq, T, D)), 'k_cache': _w((lambda *s: torch.randn(*s, dtype=torch.bfloat16) * 0.8)(B, Hkv, C, D)), 'v_cache': _w((lambda *s: torch.randn(*s, dtype=torch.bfloat16) * 0.05)(B, Hkv, C, D)), 'k_tree': _w((lambda *s: torch.randn(*s, dtype=torch.bfloat16) * 0.8)(B, Hkv, T, D)), 'v_tree': _w((lambda *s: torch.randn(*s, dtype=torch.bfloat16) * 0.05)(B, Hkv, T, D)), 'tree_mask': _w(tree_mask)}

def reference(q, k_cache, v_cache, k_tree, v_tree, tree_mask):
    B, Hq, T, D = q.shape
    Hkv, C = (k_cache.shape[1], k_cache.shape[2])
    rep = Hq // Hkv
    kc = (lambda x, n, axis: torch.repeat_interleave(x, n, dim=axis))(k_cache, rep, axis=1)
    vc = (lambda x, n, axis: torch.repeat_interleave(x, n, dim=axis))(v_cache, rep, axis=1)
    kt = (lambda x, n, axis: torch.repeat_interleave(x, n, dim=axis))(k_tree, rep, axis=1)
    vt = (lambda x, n, axis: torch.repeat_interleave(x, n, dim=axis))(v_tree, rep, axis=1)
    k = torch.concatenate([kc, kt], axis=2)
    v = torch.concatenate([vc, vt], axis=2)
    scale = D ** (-0.5)
    scores = torch.einsum('bhqd,bhkd->bhqk', q, k) * scale
    ctx_mask = torch.ones((B, 1, T, C), dtype=scores.dtype)
    tm = tree_mask[:, None, :, :].clone()
    full_mask = torch.concatenate([ctx_mask, tm], axis=-1)
    scores = scores + (1.0 - full_mask) * -1000000000.0
    p = _softmax(scores, axis=-1)
    return torch.einsum('bhqk,bhkd->bhqd', p, v)
```

```cpp
#define WAVES 16
#define SPLITS 8
#define KV_NT 1
#include <hip/hip_runtime.h>
#include <hip/hip_bf16.h>
#include <stdint.h>

#define EXTRA_PRE 0
#define EXTRA_POST 0

constexpr int H = 32, C = 4096, D = 128;
constexpr int KEYS_WG = C / SPLITS;
constexpr int KEYS_WAVE = KEYS_WG / WAVES;
constexpr int NT = KEYS_WAVE / 4;
constexpr float SCALE = 0.088388347648318440f, LOG2E = 1.4426950408889634f;
constexpr float C2 = SCALE * LOG2E;

using bf16x2 = __attribute__((ext_vector_type(2))) __bf16;
using bf16x8 = __attribute__((ext_vector_type(8))) __bf16;
using u32x4 = __attribute__((ext_vector_type(4))) unsigned;
using f32x4 = __attribute__((ext_vector_type(4))) float;
#define GAS __attribute__((address_space(1)))
typedef GAS unsigned gu32;
typedef GAS unsigned long long gu64;
#define RLX_AGENT __ATOMIC_RELAXED, __HIP_MEMORY_SCOPE_AGENT

__device__ __forceinline__ float dot2(unsigned k, unsigned q, float acc) {
  return __builtin_amdgcn_fdot2_f32_bf16(__builtin_bit_cast(bf16x2, k), __builtin_bit_cast(bf16x2, q), acc, false);
}
template <int CTRL> __device__ __forceinline__ float dpp(float x) {
  return __builtin_bit_cast(float, __builtin_amdgcn_mov_dpp(__builtin_bit_cast(int, x), CTRL, 0xf, 0xf, true));
}
__device__ __forceinline__ float row_sum16(float x) {
  x += dpp<0x128>(x); x += dpp<0x124>(x); x += dpp<0x122>(x); x += dpp<0x121>(x);
  return x;
}
__device__ __forceinline__ float xrow_max(float x) {
  auto s = __builtin_amdgcn_permlane16_swap(__float_as_uint(x), __float_as_uint(x), false, false);
  x = fmaxf(__uint_as_float(s[0]), __uint_as_float(s[1]));
  auto t = __builtin_amdgcn_permlane32_swap(__float_as_uint(x), __float_as_uint(x), false, false);
  return fmaxf(__uint_as_float(t[0]), __uint_as_float(t[1]));
}
__device__ __forceinline__ float xrow_sum(float x) {
  auto s = __builtin_amdgcn_permlane16_swap(__float_as_uint(x), __float_as_uint(x), false, false);
  x = __uint_as_float(s[0]) + __uint_as_float(s[1]);
  auto t = __builtin_amdgcn_permlane32_swap(__float_as_uint(x), __float_as_uint(x), false, false);
  return __uint_as_float(t[0]) + __uint_as_float(t[1]);
}
__device__ __forceinline__ u32x4 ldkv(const u32x4* p) {
  return __builtin_nontemporal_load(p);
}
__device__ __forceinline__ float rl(float x, int i) { return __uint_as_float(__builtin_amdgcn_readlane(__float_as_uint(x), i)); }

constexpr size_t WS_GRAN = 8192, GREC = 256;

#define STAMP(i)

__global__ void __launch_bounds__(WAVES * 64) attn_fused(const u32x4* __restrict__ q, const u32x4* __restrict__ K,
                                                         const u32x4* __restrict__ V, const u32x4* __restrict__ kt,
                                                         const unsigned short* __restrict__ vt, const unsigned short* __restrict__ mask,
                                                         unsigned char* ws, __hip_bfloat16* __restrict__ out) {
  __shared__ __attribute__((aligned(16))) float lds[WAVES * 128 + 2 * WAVES];
  const int wg = blockIdx.x, head = wg / SPLITS, split = wg % SPLITS;
  const int tid = threadIdx.x, lane = tid & 63, wave = __builtin_amdgcn_readfirstlane(tid >> 6);
  const int c = lane & 15;
  STAMP(0);
  const u32x4 qv = q[head * 16 + c];
  gu32* ep = (gu32*)(ws + head * 256);
  const unsigned tag = __hip_atomic_load(ep, RLX_AGENT) + 1u;
  const u32x4 ktv = kt[head * 16 + c];
  const unsigned short vtv = vt[head * D + (tid & 127)];
  const unsigned short mk = mask[0];
  const size_t base = ((size_t)head * C + split * KEYS_WG + wave * KEYS_WAVE) * 16 + lane;
  u32x4 k[NT], v[NT];
#pragma unroll
  for (int t = 0; t < NT; ++t) k[t] = ldkv(K + base + t * 64);
#pragma unroll
  for (int t = 0; t < NT; ++t) v[t] = ldkv(V + base + t * 64);
  __builtin_amdgcn_sched_barrier(0);
  float st;
  {
    float d = dot2(ktv[0], qv[0], 0.f);
    d = dot2(ktv[1], qv[1], d); d = dot2(ktv[2], qv[2], d); d = dot2(ktv[3], qv[3], d);
    st = (row_sum16(d) * SCALE + (1.0f - __uint_as_float((unsigned)mk << 16)) * -1000000000.0f) * LOG2E;
  }
  float x[NT];
#pragma unroll
  for (int t = 0; t < NT; ++t) {
    float d = dot2(k[t][0], qv[0], 0.f);
    d = dot2(k[t][1], qv[1], d); d = dot2(k[t][2], qv[2], d); d = dot2(k[t][3], qv[3], d);
    x[t] = row_sum16(d) * C2;
  }
  STAMP(1);
  float m = x[0];
#pragma unroll
  for (int t = 1; t < NT; ++t) m = fmaxf(m, x[t]);
  m = xrow_max(m);
  float* lm = lds + WAVES * 128;
  if (lane == 0) lm[wave] = m;
  __syncthreads();
  STAMP(2);
  float M = lm[0];
#pragma unroll
  for (int w = 1; w < WAVES; ++w) M = fmaxf(M, lm[w]);
  const int n = c & 7;
  const unsigned lomask = (c >= 8) ? 0xffff0000u : 0u;
  unsigned sel[4];
#pragma unroll
  for (int r = 0; r < 4; ++r) sel[r] = ((n >> 1) == r) ? ((n & 1) ? 0x03020c0cu : 0x0c0c0302u) : 0x0c0c0c0cu;
  float l = 0.f;
  u32x4 b[NT];
#pragma unroll
  for (int t = 0; t < NT; ++t) {
    const float p = __builtin_amdgcn_exp2f(x[t] - M);
    l += p;
    const unsigned pu = __float_as_uint(p);
    const unsigned s = __float_as_uint(p - __uint_as_float(pu & lomask));
#pragma unroll
    for (int r = 0; r < 4; ++r) b[t][r] = __builtin_amdgcn_perm(s, s, sel[r]);
  }
  l = xrow_sum(l);
  if (lane == 0) lm[WAVES + wave] = l;
  __builtin_amdgcn_sched_barrier(0);
  f32x4 acc = {0.f, 0.f, 0.f, 0.f};
#pragma unroll
  for (int t = 0; t < NT; ++t)
    acc = __builtin_amdgcn_mfma_f32_16x16x32_bf16(__builtin_bit_cast(bf16x8, v[t]), __builtin_bit_cast(bf16x8, b[t]), acc, 0, 0, 0);
#pragma unroll
  for (int r = 0; r < 4; ++r) acc[r] += dpp<0x128>(acc[r]);
  if (c < 8) {
    const int g = lane >> 4;
#pragma unroll
    for (int r = 0; r < 4; ++r) lds[wave * 128 + 8 * (4 * g + r) + n] = acc[r];
  }
  STAMP(3);
  __syncthreads();
  STAMP(4);
  if (wave >= 2) return;
  float o = 0.f, L = 0.f;
#pragma unroll
  for (int w = 0; w < WAVES; ++w) { o += lds[w * 128 + tid]; L += lm[WAVES + w]; }
  gu64* gr = (gu64*)(ws + WS_GRAN) + (size_t)head * SPLITS * GREC;
  if (split != 0) {
    gu64* rec = gr + split * GREC;
    __hip_atomic_store(rec + tid, ((unsigned long long)tag << 32) | __float_as_uint(o), RLX_AGENT);
    if (tid < 2) __hip_atomic_store(rec + 128 + tid, ((unsigned long long)tag << 32) | __float_as_uint(tid ? L : M), RLX_AGENT);
    STAMP(5);
    return;
  }
  float os[SPLITS], ml;
  const int mls = 1 + ((lane >> 1) % (SPLITS - 1));
  for (unsigned spins = 0;; ++spins) {
    bool ok = true;
    unsigned long long g0 = __hip_atomic_load(gr + mls * GREC + 128 + (lane & 1), RLX_AGENT);
#pragma unroll
    for (int s = 1; s < SPLITS; ++s) {
      const unsigned long long g = __hip_atomic_load(gr + s * GREC + tid, RLX_AGENT);
      os[s] = __uint_as_float((unsigned)g); ok &= (unsigned)(g >> 32) == tag;
    }
    ml = __uint_as_float((unsigned)g0); ok &= (unsigned)(g0 >> 32) == tag;
    if (__all(ok) || spins > (1u << 20)) break;
    __builtin_amdgcn_s_sleep(1);
  }
  STAMP(5);
  float MM = fmaxf(M, st);
#pragma unroll
  for (int s = 1; s < SPLITS; ++s) MM = fmaxf(MM, rl(ml, 2 * (s - 1)));
  const float wt = __builtin_amdgcn_exp2f(st - MM), w0 = __builtin_amdgcn_exp2f(M - MM);
  float LL = fmaf(w0, L, wt), oo = fmaf(w0, o, wt * __uint_as_float((unsigned)vtv << 16));
#pragma unroll
  for (int s = 1; s < SPLITS; ++s) {
    const float w = __builtin_amdgcn_exp2f(rl(ml, 2 * (s - 1)) - MM);
    LL = fmaf(w, rl(ml, 2 * (s - 1) + 1), LL); oo = fmaf(w, os[s], oo);
  }
  out[head * D + tid] = __float2bfloat16(oo / LL);
  if (tid == 0) __hip_atomic_store(ep, tag, RLX_AGENT);
  STAMP(6);
}

__global__ void nop_kernel() {}

extern "C" void kernel_launch(void* const* d_in, const int* in_sizes, int n_in, void* d_out, int out_size, void* d_ws,
                              size_t ws_size, hipStream_t stream) {
  for (int i = 0; i < EXTRA_PRE; ++i) nop_kernel<<<1, 64, 0, stream>>>();
  attn_fused<<<H * SPLITS, WAVES * 64, 0, stream>>>((const u32x4*)d_in[0], (const u32x4*)d_in[1], (const u32x4*)d_in[2],
                                                    (const u32x4*)d_in[3], (const unsigned short*)d_in[4], (const unsigned short*)d_in[5],
                                                    (unsigned char*)d_ws, (__hip_bfloat16*)d_out);
  for (int i = 0; i < EXTRA_POST; ++i) nop_kernel<<<1, 64, 0, stream>>>();
}
```

```cpp
#define AMP 6
#define AMPK 1
#include <hip/hip_runtime.h>
#include <hip/hip_bf16.h>
#include <stdint.h>

#define WAVES 8
#define SPLITS 8
#define KV_NT 1
#define EXTRA_PRE 0
#define EXTRA_POST 0

constexpr int H = 32, C = 4096, D = 128;
constexpr int KEYS_WG = C / SPLITS;
constexpr int KEYS_WAVE = KEYS_WG / WAVES;
constexpr int NT = KEYS_WAVE / 4;
constexpr float SCALE = 0.088388347648318440f, LOG2E = 1.4426950408889634f;
constexpr float C2 = SCALE * LOG2E;

using bf16x2 = __attribute__((ext_vector_type(2))) __bf16;
using bf16x8 = __attribute__((ext_vector_type(8))) __bf16;
using u32x4 = __attribute__((ext_vector_type(4))) unsigned;
using f32x4 = __attribute__((ext_vector_type(4))) float;
#define GAS __attribute__((address_space(1)))
typedef GAS unsigned gu32;
typedef GAS unsigned long long gu64;
#define RLX_AGENT __ATOMIC_RELAXED, __HIP_MEMORY_SCOPE_AGENT

__device__ __forceinline__ float dot2(unsigned k, unsigned q, float acc) {
  return __builtin_amdgcn_fdot2_f32_bf16(__builtin_bit_cast(bf16x2, k), __builtin_bit_cast(bf16x2, q), acc, false);
}
template <int CTRL> __device__ __forceinline__ float dpp(float x) {
  return __builtin_bit_cast(float, __builtin_amdgcn_mov_dpp(__builtin_bit_cast(int, x), CTRL, 0xf, 0xf, true));
}
__device__ __forceinline__ float row_sum16(float x) {
  x += dpp<0x128>(x); x += dpp<0x124>(x); x += dpp<0x122>(x); x += dpp<0x121>(x);
  return x;
}
__device__ __forceinline__ float xrow_max(float x) {
  auto s = __builtin_amdgcn_permlane16_swap(__float_as_uint(x), __float_as_uint(x), false, false);
  x = fmaxf(__uint_as_float(s[0]), __uint_as_float(s[1]));
  auto t = __builtin_amdgcn_permlane32_swap(__float_as_uint(x), __float_as_uint(x), false, false);
  return fmaxf(__uint_as_float(t[0]), __uint_as_float(t[1]));
}
__device__ __forceinline__ float xrow_sum(float x) {
  auto s = __builtin_amdgcn_permlane16_swap(__float_as_uint(x), __float_as_uint(x), false, false);
  x = __uint_as_float(s[0]) + __uint_as_float(s[1]);
  auto t = __builtin_amdgcn_permlane32_swap(__float_as_uint(x), __float_as_uint(x), false, false);
  return __uint_as_float(t[0]) + __uint_as_float(t[1]);
}
__device__ __forceinline__ u32x4 ldkv(const u32x4* p) {
  return __builtin_nontemporal_load(p);
}
__device__ __forceinline__ float rl(float x, int i) { return __uint_as_float(__builtin_amdgcn_readlane(__float_as_uint(x), i)); }

constexpr size_t WS_GRAN = 8192, GREC = 256;

#define RT(x) do { __builtin_amdgcn_sched_barrier(0); asm volatile("s_memrealtime %0\n\ts_waitcnt lgkmcnt(0)" : "=s"(x) :: "memory"); __builtin_amdgcn_sched_barrier(0); } while (0)
#define STAMP(i)

#define AMPEND() do { if (AMP) { RT(te); unsigned long long dl = AMP == 1 ? te - ta : AMP == 2 ? tb - ta : AMP == 3 ? tc - tb : AMP == 4 ? td - tc : AMP == 5 ? te - td : 500; \
   unsigned long long tgt = te + dl * AMPK, now; do { RT(now); } while (now < tgt); } } while (0)
__global__ void __launch_bounds__(WAVES * 64) attn_fused(const u32x4* __restrict__ q, const u32x4* __restrict__ K,
                                                         const u32x4* __restrict__ V, const u32x4* __restrict__ kt,
                                                         const unsigned short* __restrict__ vt, const unsigned short* __restrict__ mask,
                                                         unsigned char* ws, __hip_bfloat16* __restrict__ out) {
  __shared__ __attribute__((aligned(16))) float lds[WAVES * 128 + 2 * WAVES];
  unsigned long long ta = 0, tb = 0, tc = 0, td = 0, te = 0;
  if (AMP) RT(ta);
  const int wg = blockIdx.x, head = wg / SPLITS, split = wg % SPLITS;
  const int tid = threadIdx.x, lane = tid & 63, wave = __builtin_amdgcn_readfirstlane(tid >> 6);
  const int c = lane & 15;
  STAMP(0);
  if (AMP) { asm volatile("" :: "s"(q), "s"(K), "s"(V), "s"(ws)); RT(tb); }
  const u32x4 qv = q[head * 16 + c];
  gu32* ep = (gu32*)(ws + head * 256);
  const unsigned tag = __hip_atomic_load(ep, RLX_AGENT) + 1u;
  const u32x4 ktv = kt[head * 16 + c];
  const unsigned short vtv = vt[head * D + (tid & 127)];
  const unsigned short mk = mask[0];
  const size_t base = ((size_t)head * C + split * KEYS_WG + wave * KEYS_WAVE) * 16 + lane;
  u32x4 k[NT], v[NT];
#pragma unroll
  for (int t = 0; t < NT; ++t) k[t] = ldkv(K + base + t * 64);
#pragma unroll
  for (int t = 0; t < NT; ++t) v[t] = ldkv(V + base + t * 64);
  __builtin_amdgcn_sched_barrier(0);
  if (AMP) { asm volatile("" :: "v"(k[0])); RT(tc); }
  float st;
  {
    float d = dot2(ktv[0], qv[0], 0.f);
    d = dot2(ktv[1], qv[1], d); d = dot2(ktv[2], qv[2], d); d = dot2(ktv[3], qv[3], d);
    st = (row_sum16(d) * SCALE + (1.0f - __uint_as_float((unsigned)mk << 16)) * -1000000000.0f) * LOG2E;
  }
  float x[NT];
#pragma unroll
  for (int t = 0; t < NT; ++t) {
    float d = dot2(k[t][0], qv[0], 0.f);
    d = dot2(k[t][1], qv[1], d); d = dot2(k[t][2], qv[2], d); d = dot2(k[t][3], qv[3], d);
    x[t] = row_sum16(d) * C2;
  }
  STAMP(1);
  float m = x[0];
#pragma unroll
  for (int t = 1; t < NT; ++t) m = fmaxf(m, x[t]);
  m = xrow_max(m);
  float* lm = lds + WAVES * 128;
  if (lane == 0) lm[wave] = m;
  __syncthreads();
  STAMP(2);
  float M = lm[0];
#pragma unroll
  for (int w = 1; w < WAVES; ++w) M = fmaxf(M, lm[w]);
  const int n = c & 7;
  const unsigned lomask = (c >= 8) ? 0xffff0000u : 0u;
  unsigned sel[4];
#pragma unroll
  for (int r = 0; r < 4; ++r) sel[r] = ((n >> 1) == r) ? ((n & 1) ? 0x03020c0cu : 0x0c0c0302u) : 0x0c0c0c0cu;
  float l = 0.f;
  u32x4 b[NT];
#pragma unroll
  for (int t = 0; t < NT; ++t) {
    const float p = __builtin_amdgcn_exp2f(x[t] - M);
    l += p;
    const unsigned pu = __float_as_uint(p);
    const unsigned s = __float_as_uint(p - __uint_as_float(pu & lomask));
#pragma unroll
    for (int r = 0; r < 4; ++r) b[t][r] = __builtin_amdgcn_perm(s, s, sel[r]);
  }
  l = xrow_sum(l);
  if (lane == 0) lm[WAVES + wave] = l;
  __builtin_amdgcn_sched_barrier(0);
  f32x4 acc = {0.f, 0.f, 0.f, 0.f};
#pragma unroll
  for (int t = 0; t < NT; ++t)
    acc = __builtin_amdgcn_mfma_f32_16x16x32_bf16(__builtin_bit_cast(bf16x8, v[t]), __builtin_bit_cast(bf16x8, b[t]), acc, 0, 0, 0);
#pragma unroll
  for (int r = 0; r < 4; ++r) acc[r] += dpp<0x128>(acc[r]);
  if (c < 8) {
    const int g = lane >> 4;
#pragma unroll
    for (int r = 0; r < 4; ++r) lds[wave * 128 + 8 * (4 * g + r) + n] = acc[r];
  }
  STAMP(3);
  if (AMP) RT(td);
  __syncthreads();
  STAMP(4);
  if (wave >= 2) return;
  float o = 0.f, L = 0.f;
#pragma unroll
  for (int w = 0; w < WAVES; ++w) { o += lds[w * 128 + tid]; L += lm[WAVES + w]; }
  gu64* gr = (gu64*)(ws + WS_GRAN) + (size_t)head * SPLITS * GREC;
  if (split != 0) {
    gu64* rec = gr + split * GREC;
    __hip_atomic_store(rec + tid, ((unsigned long long)tag << 32) | __float_as_uint(o), RLX_AGENT);
    if (tid < 2) __hip_atomic_store(rec + 128 + tid, ((unsigned long long)tag << 32) | __float_as_uint(tid ? L : M), RLX_AGENT);
    STAMP(5);
    return;
  }
  float os[SPLITS], ml;
  const int mls = 1 + ((lane >> 1) % (SPLITS - 1));
  for (unsigned spins = 0;; ++spins) {
    bool ok = true;
    unsigned long long g0 = __hip_atomic_load(gr + mls * GREC + 128 + (lane & 1), RLX_AGENT);
#pragma unroll
    for (int s = 1; s < SPLITS; ++s) {
      const unsigned long long g = __hip_atomic_load(gr + s * GREC + tid, RLX_AGENT);
      os[s] = __uint_as_float((unsigned)g); ok &= (unsigned)(g >> 32) == tag;
    }
    ml = __uint_as_float((unsigned)g0); ok &= (unsigned)(g0 >> 32) == tag;
    if (__all(ok) || spins > (1u << 20)) break;
    __builtin_amdgcn_s_sleep(1);
  }
  STAMP(5);
  float MM = fmaxf(M, st);
#pragma unroll
  for (int s = 1; s < SPLITS; ++s) MM = fmaxf(MM, rl(ml, 2 * (s - 1)));
  const float wt = __builtin_amdgcn_exp2f(st - MM), w0 = __builtin_amdgcn_exp2f(M - MM);
  float LL = fmaf(w0, L, wt), oo = fmaf(w0, o, wt * __uint_as_float((unsigned)vtv << 16));
#pragma unroll
  for (int s = 1; s < SPLITS; ++s) {
    const float w = __builtin_amdgcn_exp2f(rl(ml, 2 * (s - 1)) - MM);
    LL = fmaf(w, rl(ml, 2 * (s - 1) + 1), LL); oo = fmaf(w, os[s], oo);
  }
  out[head * D + tid] = __float2bfloat16(oo / LL);
  if (tid == 0) __hip_atomic_store(ep, tag, RLX_AGENT);
  STAMP(6);
  AMPEND();
}

__global__ void nop_kernel() {}

extern "C" void kernel_launch(void* const* d_in, const int* in_sizes, int n_in, void* d_out, int out_size, void* d_ws,
                              size_t ws_size, hipStream_t stream) {
  for (int i = 0; i < EXTRA_PRE; ++i) nop_kernel<<<1, 64, 0, stream>>>();
  attn_fused<<<H * SPLITS, WAVES * 64, 0, stream>>>((const u32x4*)d_in[0], (const u32x4*)d_in[1], (const u32x4*)d_in[2],
                                                    (const u32x4*)d_in[3], (const unsigned short*)d_in[4], (const unsigned short*)d_in[5],
                                                    (unsigned char*)d_ws, (__hip_bfloat16*)d_out);
  for (int i = 0; i < EXTRA_POST; ++i) nop_kernel<<<1, 64, 0, stream>>>();
}
```

```cpp
#define XP 5
#include <hip/hip_runtime.h>
#include <hip/hip_bf16.h>
#include <stdint.h>

#define WAVES 8
#define SPLITS 8
#define KV_NT 1
#define EXTRA_PRE 0
#define EXTRA_POST 0

constexpr int H = 32, C = 4096, D = 128;
constexpr int KEYS_WG = C / SPLITS;
constexpr int KEYS_WAVE = KEYS_WG / WAVES;
constexpr int NT = KEYS_WAVE / 4;
constexpr float SCALE = 0.088388347648318440f, LOG2E = 1.4426950408889634f;
constexpr float C2 = SCALE * LOG2E;

using bf16x2 = __attribute__((ext_vector_type(2))) __bf16;
using bf16x8 = __attribute__((ext_vector_type(8))) __bf16;
using u32x4 = __attribute__((ext_vector_type(4))) unsigned;
using f32x4 = __attribute__((ext_vector_type(4))) float;
#define GAS __attribute__((address_space(1)))
typedef GAS unsigned gu32;
typedef GAS unsigned long long gu64;
#define RLX_AGENT __ATOMIC_RELAXED, __HIP_MEMORY_SCOPE_AGENT

__device__ __forceinline__ float dot2(unsigned k, unsigned q, float acc) {
  return __builtin_amdgcn_fdot2_f32_bf16(__builtin_bit_cast(bf16x2, k), __builtin_bit_cast(bf16x2, q), acc, false);
}
template <int CTRL> __device__ __forceinline__ float dpp(float x) {
  return __builtin_bit_cast(float, __builtin_amdgcn_mov_dpp(__builtin_bit_cast(int, x), CTRL, 0xf, 0xf, true));
}
__device__ __forceinline__ float row_sum16(float x) {
  x += dpp<0x128>(x); x += dpp<0x124>(x); x += dpp<0x122>(x); x += dpp<0x121>(x);
  return x;
}
__device__ __forceinline__ float xrow_max(float x) {
  auto s = __builtin_amdgcn_permlane16_swap(__float_as_uint(x), __float_as_uint(x), false, false);
  x = fmaxf(__uint_as_float(s[0]), __uint_as_float(s[1]));
  auto t = __builtin_amdgcn_permlane32_swap(__float_as_uint(x), __float_as_uint(x), false, false);
  return fmaxf(__uint_as_float(t[0]), __uint_as_float(t[1]));
}
__device__ __forceinline__ float xrow_sum(float x) {
  auto s = __builtin_amdgcn_permlane16_swap(__float_as_uint(x), __float_as_uint(x), false, false);
  x = __uint_as_float(s[0]) + __uint_as_float(s[1]);
  auto t = __builtin_amdgcn_permlane32_swap(__float_as_uint(x), __float_as_uint(x), false, false);
  return __uint_as_float(t[0]) + __uint_as_float(t[1]);
}
__device__ __forceinline__ u32x4 ldkv(const u32x4* p) {
  return __builtin_nontemporal_load(p);
}
__device__ __forceinline__ float rl(float x, int i) { return __uint_as_float(__builtin_amdgcn_readlane(__float_as_uint(x), i)); }

constexpr size_t WS_GRAN = 8192, GREC = 256;

#define XK 1
#define AMP 0
#define AMPK 1
#define RT(x) do { __builtin_amdgcn_sched_barrier(0); asm volatile("s_memrealtime %0\n\ts_waitcnt lgkmcnt(0)" : "=s"(x) :: "memory"); __builtin_amdgcn_sched_barrier(0); } while (0)
#define STAMP(i)

#define AMPEND() do { if (AMP) { RT(te); unsigned long long dl = AMP == 1 ? te - ta : AMP == 2 ? tb - ta : AMP == 3 ? tc - tb : AMP == 4 ? td - tc : AMP == 5 ? te - td : AMP == 7 ? x1d : AMP == 8 ? 0 : 500; \
   unsigned long long tgt = te + dl * AMPK, now; do { RT(now); } while (now < tgt); } } while (0)
__global__ void __launch_bounds__(WAVES * 64) attn_fused(const u32x4* __restrict__ q, const u32x4* __restrict__ K,
                                                         const u32x4* __restrict__ V, const u32x4* __restrict__ kt,
                                                         const unsigned short* __restrict__ vt, const unsigned short* __restrict__ mask,
                                                         unsigned char* ws, __hip_bfloat16* __restrict__ out) {
  __shared__ __attribute__((aligned(16))) float lds[WAVES * 128 + 2 * WAVES];
  unsigned long long ta = 0, tb = 0, tc = 0, td = 0, te = 0;
  if (AMP) RT(ta);
  const int wg = blockIdx.x, head = wg / SPLITS, split = wg % SPLITS;
  const int tid = threadIdx.x, lane = tid & 63, wave = __builtin_amdgcn_readfirstlane(tid >> 6);
  const int c = lane & 15;
  STAMP(0);
  if (XP == 3 && wg < 8 && wave == 0) asm volatile("buffer_wbl2 sc0 sc1" ::: "memory");
  if (AMP) { asm volatile("" :: "s"(q), "s"(K), "s"(V), "s"(ws)); RT(tb); }
  const u32x4 qv = q[head * 16 + c];
  gu32* ep = (gu32*)(ws + head * 256);
  const unsigned tag = __hip_atomic_load(ep, RLX_AGENT) + 1u;
  const u32x4 ktv = kt[head * 16 + c];
  const unsigned short vtv = vt[head * D + (tid & 127)];
  const unsigned short mk = mask[0];
  const size_t base = ((size_t)head * C + split * KEYS_WG + wave * KEYS_WAVE) * 16 + lane;
  u32x4 k[NT], v[NT];
#pragma unroll
  for (int t = 0; t < NT; ++t) k[t] = ldkv(K + base + t * 64);
#pragma unroll
  for (int t = 0; t < NT; ++t) v[t] = ldkv(V + base + t * 64);
  __builtin_amdgcn_sched_barrier(0);
  if (AMP) { asm volatile("" :: "v"(k[0])); RT(tc); }
  float st;
  {
    float d = dot2(ktv[0], qv[0], 0.f);
    d = dot2(ktv[1], qv[1], d); d = dot2(ktv[2], qv[2], d); d = dot2(ktv[3], qv[3], d);
    st = (row_sum16(d) * SCALE + (1.0f - __uint_as_float((unsigned)mk << 16)) * -1000000000.0f) * LOG2E;
  }
  float x[NT];
#pragma unroll
  for (int t = 0; t < NT; ++t) {
    float d = dot2(k[t][0], qv[0], 0.f);
    d = dot2(k[t][1], qv[1], d); d = dot2(k[t][2], qv[2], d); d = dot2(k[t][3], qv[3], d);
    x[t] = row_sum16(d) * C2;
  }
  STAMP(1);
  float m = x[0];
#pragma unroll
  for (int t = 1; t < NT; ++t) m = fmaxf(m, x[t]);
  m = xrow_max(m);
  float* lm = lds + WAVES * 128;
  if (lane == 0) lm[wave] = m;
  __syncthreads();
  STAMP(2);
  float M = lm[0];
#pragma unroll
  for (int w = 1; w < WAVES; ++w) M = fmaxf(M, lm[w]);
  const int n = c & 7;
  const unsigned lomask = (c >= 8) ? 0xffff0000u : 0u;
  unsigned sel[4];
#pragma unroll
  for (int r = 0; r < 4; ++r) sel[r] = ((n >> 1) == r) ? ((n & 1) ? 0x03020c0cu : 0x0c0c0302u) : 0x0c0c0c0cu;
  float l = 0.f;
  u32x4 b[NT];
#pragma unroll
  for (int t = 0; t < NT; ++t) {
    const float p = __builtin_amdgcn_exp2f(x[t] - M);
    l += p;
    const unsigned pu = __float_as_uint(p);
    const unsigned s = __float_as_uint(p - __uint_as_float(pu & lomask));
#pragma unroll
    for (int r = 0; r < 4; ++r) b[t][r] = __builtin_amdgcn_perm(s, s, sel[r]);
  }
  l = xrow_sum(l);
  if (lane == 0) lm[WAVES + wave] = l;
  __builtin_amdgcn_sched_barrier(0);
  f32x4 acc = {0.f, 0.f, 0.f, 0.f};
#pragma unroll
  for (int t = 0; t < NT; ++t)
    acc = __builtin_amdgcn_mfma_f32_16x16x32_bf16(__builtin_bit_cast(bf16x8, v[t]), __builtin_bit_cast(bf16x8, b[t]), acc, 0, 0, 0);
#pragma unroll
  for (int r = 0; r < 4; ++r) acc[r] += dpp<0x128>(acc[r]);
  if (c < 8) {
    const int g = lane >> 4;
#pragma unroll
    for (int r = 0; r < 4; ++r) lds[wave * 128 + 8 * (4 * g + r) + n] = acc[r];
  }
  STAMP(3);
  if (AMP) RT(td);
  __syncthreads();
  STAMP(4);
  if (wave >= 2) return;
  float o = 0.f, L = 0.f;
#pragma unroll
  for (int w = 0; w < WAVES; ++w) { o += lds[w * 128 + tid]; L += lm[WAVES + w]; }
  gu64* gr = (gu64*)(ws + WS_GRAN) + (size_t)head * SPLITS * GREC;
  if (split != 0) {
    gu64* rec = gr + split * GREC;
    __hip_atomic_store(rec + tid, ((unsigned long long)tag << 32) | __float_as_uint(o), RLX_AGENT);
    if (tid < 2) __hip_atomic_store(rec + 128 + tid, ((unsigned long long)tag << 32) | __float_as_uint(tid ? L : M), RLX_AGENT);
    STAMP(5);
    return;
  }
  float os[SPLITS], ml;
  const int mls = 1 + ((lane >> 1) % (SPLITS - 1));
  for (unsigned spins = 0;; ++spins) {
    bool ok = true;
    unsigned long long g0 = __hip_atomic_load(gr + mls * GREC + 128 + (lane & 1), RLX_AGENT);
#pragma unroll
    for (int s = 1; s < SPLITS; ++s) {
      const unsigned long long g = __hip_atomic_load(gr + s * GREC + tid, RLX_AGENT);
      os[s] = __uint_as_float((unsigned)g); ok &= (unsigned)(g >> 32) == tag;
    }
    ml = __uint_as_float((unsigned)g0); ok &= (unsigned)(g0 >> 32) == tag;
    if (__all(ok) || spins > (1u << 20)) break;
    __builtin_amdgcn_s_sleep(1);
  }
  STAMP(5);
  float MM = fmaxf(M, st);
#pragma unroll
  for (int s = 1; s < SPLITS; ++s) MM = fmaxf(MM, rl(ml, 2 * (s - 1)));
  const float wt = __builtin_amdgcn_exp2f(st - MM), w0 = __builtin_amdgcn_exp2f(M - MM);
  float LL = fmaf(w0, L, wt), oo = fmaf(w0, o, wt * __uint_as_float((unsigned)vtv << 16));
#pragma unroll
  for (int s = 1; s < SPLITS; ++s) {
    const float w = __builtin_amdgcn_exp2f(rl(ml, 2 * (s - 1)) - MM);
    LL = fmaf(w, rl(ml, 2 * (s - 1) + 1), LL); oo = fmaf(w, os[s], oo);
  }
  out[head * D + tid] = __float2bfloat16(oo / LL);
  if (tid == 0) __hip_atomic_store(ep, tag, RLX_AGENT);
  STAMP(6);
  unsigned long long x1d = 0;
  if (AMP == 7) { unsigned long long z = __hip_atomic_load((gu64*)(ws + (1 << 20)), RLX_AGENT);
    unsigned lo = __builtin_amdgcn_readfirstlane((unsigned)z), hi = __builtin_amdgcn_readfirstlane((unsigned)(z >> 32));
    x1d = ta - (((unsigned long long)hi << 32) | lo); if (x1d > 3000) x1d = 3000; }
  if (AMP == 8) { unsigned long long z; RT(z); if (tid == 0) __hip_atomic_store((gu64*)(ws + (1 << 20)) + head * 32, z, RLX_AGENT); }
  AMPEND();
}

__global__ void nop_kernel() {}
__global__ void stamp_kernel(unsigned char* ws) { unsigned long long t; RT(t); if (threadIdx.x == 0) __hip_atomic_store((gu64*)(ws + (1 << 20)), t, RLX_AGENT); }
__global__ void amp_kernel(unsigned char* ws) {
  unsigned long long t; RT(t);
  unsigned long long m = __hip_atomic_load((gu64*)(ws + (1 << 20)) + (threadIdx.x & 31) * 32, RLX_AGENT);
  for (int o = 16; o >= 1; o >>= 1) { unsigned long long y = __shfl_xor(m, o); m = y > m ? y : m; }
  unsigned lo = __builtin_amdgcn_readfirstlane((unsigned)m), hi = __builtin_amdgcn_readfirstlane((unsigned)(m >> 32));
  unsigned long long mm = ((unsigned long long)hi << 32) | lo, dl = t - mm; if (dl > 3000) dl = 3000;
  unsigned long long now, t2; RT(t2); unsigned long long tgt = t2 + dl * XK; do { RT(now); } while (now < tgt);
}
__global__ void __launch_bounds__(512) geom_kernel(float* p) {
  __shared__ float l[1040];
  asm volatile("v_mov_b32 v145, 0" ::: "v145");
  if (p == nullptr) l[threadIdx.x] = 1.f;
  asm volatile("" :: "s"(l));
}

extern "C" void kernel_launch(void* const* d_in, const int* in_sizes, int n_in, void* d_out, int out_size, void* d_ws,
                              size_t ws_size, hipStream_t stream) {
  for (int i = 0; i < EXTRA_PRE; ++i) nop_kernel<<<1, 64, 0, stream>>>();
  if (XP == 1) stamp_kernel<<<1, 64, 0, stream>>>((unsigned char*)d_ws);
  if (XP == 6) geom_kernel<<<256, 512, 0, stream>>>((float*)d_ws);
  attn_fused<<<H * SPLITS, WAVES * 64, 0, stream>>>((const u32x4*)d_in[0], (const u32x4*)d_in[1], (const u32x4*)d_in[2],
                                                    (const u32x4*)d_in[3], (const unsigned short*)d_in[4], (const unsigned short*)d_in[5],
                                                    (unsigned char*)d_ws, (__hip_bfloat16*)d_out);
  for (int i = 0; i < EXTRA_POST; ++i) nop_kernel<<<1, 64, 0, stream>>>();
  if (XP == 2) amp_kernel<<<1, 64, 0, stream>>>((unsigned char*)d_ws);
  if (XP == 5) geom_kernel<<<256, 512, 0, stream>>>((float*)d_ws);
}
```

```cpp
#define WAVES 16
#define AUX 2
#define PAT 0
#define PRE 2
#include <hip/hip_runtime.h>
#include <hip/hip_bf16.h>
#include <stdint.h>

#define HMAP 0
#define AMP 0
#define AMPK 1

constexpr int H = 32, C = 4096, D = 128, SPLITS = 8;
constexpr int NT = C / SPLITS / WAVES / 4;
constexpr float SCALE = 0.088388347648318440f, LOG2E = 1.4426950408889634f;
constexpr float C2 = SCALE * LOG2E;

using bf16x2 = __attribute__((ext_vector_type(2))) __bf16;
using bf16x8 = __attribute__((ext_vector_type(8))) __bf16;
using u32x4 = __attribute__((ext_vector_type(4))) unsigned;
using f32x4 = __attribute__((ext_vector_type(4))) float;
using f32x2 = __attribute__((ext_vector_type(2))) float;
#define GAS __attribute__((address_space(1)))
typedef GAS unsigned gu32;
#define RLX_AGENT __ATOMIC_RELAXED, __HIP_MEMORY_SCOPE_AGENT

__device__ __forceinline__ float dot2(unsigned k, unsigned q, float acc) {
  return __builtin_amdgcn_fdot2_f32_bf16(__builtin_bit_cast(bf16x2, k), __builtin_bit_cast(bf16x2, q), acc, false);
}
template <int CTRL> __device__ __forceinline__ float dpp(float x) {
  return __builtin_bit_cast(float, __builtin_amdgcn_mov_dpp(__builtin_bit_cast(int, x), CTRL, 0xf, 0xf, true));
}
__device__ __forceinline__ float row_sum16(float x) {
  x += dpp<0x128>(x); x += dpp<0x124>(x); x += dpp<0x122>(x); x += dpp<0x121>(x);
  return x;
}
__device__ __forceinline__ float row_max16(float x) {
  x = fmaxf(x, dpp<0x128>(x)); x = fmaxf(x, dpp<0x124>(x)); x = fmaxf(x, dpp<0x122>(x)); x = fmaxf(x, dpp<0x121>(x));
  return x;
}
__device__ __forceinline__ float xrow_max(float x) {
  auto s = __builtin_amdgcn_permlane16_swap(__float_as_uint(x), __float_as_uint(x), false, false);
  x = fmaxf(__uint_as_float(s[0]), __uint_as_float(s[1]));
  auto t = __builtin_amdgcn_permlane32_swap(__float_as_uint(x), __float_as_uint(x), false, false);
  return fmaxf(__uint_as_float(t[0]), __uint_as_float(t[1]));
}
__device__ __forceinline__ float xrow_sum(float x) {
  auto s = __builtin_amdgcn_permlane16_swap(__float_as_uint(x), __float_as_uint(x), false, false);
  x = __uint_as_float(s[0]) + __uint_as_float(s[1]);
  auto t = __builtin_amdgcn_permlane32_swap(__float_as_uint(x), __float_as_uint(x), false, false);
  return __uint_as_float(t[0]) + __uint_as_float(t[1]);
}
__device__ __forceinline__ float bf2f(unsigned short h) { return __uint_as_float((unsigned)h << 16); }

constexpr unsigned WS_REC = 8192, REC = 2048, WS_BYTES = WS_REC + H * SPLITS * REC;

#define RT(x) do { __builtin_amdgcn_sched_barrier(0); asm volatile("s_memrealtime %0\n\ts_waitcnt lgkmcnt(0)" : "=s"(x) :: "memory"); __builtin_amdgcn_sched_barrier(0); } while (0)
#define STAMP(i)

__global__ void __launch_bounds__(WAVES * 64) attn_fused(const void* __restrict__ K, const void* __restrict__ V,
                                                         const u32x4* __restrict__ q, unsigned char* ws,
                                                         const u32x4* __restrict__ kt, const unsigned short* __restrict__ vt,
                                                         const unsigned short* __restrict__ mask, __hip_bfloat16* __restrict__ out) {
  __shared__ __attribute__((aligned(16))) float lds[WAVES * 128 + 2 * WAVES];
  unsigned long long ta = 0, te = 0;
  if (AMP) RT(ta);
  const int wg = blockIdx.x;
  const int head = HMAP ? (wg & 31) : (wg >> 3), split = HMAP ? (wg >> 5) : (wg & 7);
  const int tid = threadIdx.x, lane = tid & 63, wave = __builtin_amdgcn_readfirstlane(tid >> 6);
  const int c = lane & 15, g = lane >> 4;
  STAMP(0);
  const auto rk = __builtin_amdgcn_make_buffer_rsrc((void*)K, 0, H * C * D * 2, 0x00020000);
  const auto rv = __builtin_amdgcn_make_buffer_rsrc((void*)V, 0, H * C * D * 2, 0x00020000);
  constexpr int SW = PAT == 0 ? NT : PAT == 1 ? 1 : SPLITS, ST = PAT == 0 ? 1 : PAT == 1 ? WAVES : SPLITS * WAVES;
  const unsigned so = (unsigned)(head * 1024 + (PAT == 2 ? split : split * 128) + wave * SW) * 1024u;
  const unsigned vo = lane * 16u;
  u32x4 k[NT], v[NT];
#pragma unroll
  for (int t = 0; t < NT; ++t) k[t] = __builtin_amdgcn_raw_buffer_load_b128(rk, vo, so + t * ST * 1024, AUX);
  const u32x4 qv = q[head * 16 + c];
  const int d0 = 32 * g + 16 * (c >> 3) + (c & 7);
  const unsigned nw = wave == 0 ? WS_BYTES : 0u, nt_ = (wave == 0 && split == 0) ? H * D * 2 + 16 : 0u;
  const auto rw = __builtin_amdgcn_make_buffer_rsrc((void*)ws, 0, nw, 0x00020000);
  const auto rkt = __builtin_amdgcn_make_buffer_rsrc((void*)kt, 0, nt_, 0x00020000);
  const auto rvt = __builtin_amdgcn_make_buffer_rsrc((void*)vt, 0, nt_, 0x00020000);
  const auto rmk = __builtin_amdgcn_make_buffer_rsrc((void*)mask, 0, nt_ ? 16u : 0u, 0x00020000);
  unsigned epoch; u32x4 ktv; unsigned short vt0, vt1, mk;
#define SMALL_LOADS() do { epoch = __builtin_amdgcn_raw_buffer_load_b32(rw, 0, head * 256, 16); \
    ktv = __builtin_amdgcn_raw_buffer_load_b128(rkt, c * 16, head * 256, 0); \
    vt0 = __builtin_amdgcn_raw_buffer_load_b16(rvt, d0 * 2, head * 256, 0); \
    vt1 = __builtin_amdgcn_raw_buffer_load_b16(rvt, d0 * 2 + 16, head * 256, 0); \
    mk = __builtin_amdgcn_raw_buffer_load_b16(rmk, 0, 0, 0); } while (0)
  SMALL_LOADS();
#pragma unroll
  for (int t = 0; t < PRE; ++t) v[t] = __builtin_amdgcn_raw_buffer_load_b128(rv, vo, so + t * ST * 1024, AUX);
  __builtin_amdgcn_sched_barrier(0);
  float x[NT];
#pragma unroll
  for (int t = 0; t < NT; ++t) {
    float d = dot2(k[t][0], qv[0], 0.f);
    d = dot2(k[t][1], qv[1], d); d = dot2(k[t][2], qv[2], d); d = dot2(k[t][3], qv[3], d);
    x[t] = row_sum16(d) * C2;
  }
  STAMP(1);
  float m = x[0];
#pragma unroll
  for (int t = 1; t < NT; ++t) m = fmaxf(m, x[t]);
  m = xrow_max(m);
  const float M = m;
  float* lm = lds + WAVES * 128;
  const int n = c & 7;
  const unsigned lomask = (c >= 8) ? 0xffff0000u : 0u;
  unsigned sel[4];
#pragma unroll
  for (int r = 0; r < 4; ++r) sel[r] = ((n >> 1) == r) ? ((n & 1) ? 0x03020c0cu : 0x0c0c0302u) : 0x0c0c0c0cu;
  float l = 0.f;
  u32x4 b[NT];
#pragma unroll
  for (int t = 0; t < NT; ++t) {
    const float p = __builtin_amdgcn_exp2f(x[t] - M);
    l += p;
    const unsigned pu = __float_as_uint(p);
    const unsigned s = __float_as_uint(p - __uint_as_float(pu & lomask));
#pragma unroll
    for (int r = 0; r < 4; ++r) b[t][r] = __builtin_amdgcn_perm(s, s, sel[r]);
  }
  l = xrow_sum(l);
  if (lane == 0) *reinterpret_cast<f32x2*>(lm + 2 * wave) = f32x2{M, l};
  float mx, vtf0 = bf2f(vt0), vtf1 = bf2f(vt1);
  {
    float d = dot2(ktv[0], qv[0], 0.f);
    d = dot2(ktv[1], qv[1], d); d = dot2(ktv[2], qv[2], d); d = dot2(ktv[3], qv[3], d);
    const float st = (row_sum16(d) * SCALE + (1.0f - bf2f(mk)) * -1000000000.0f) * LOG2E;
    mx = c == 8 ? st : -INFINITY;
  }
  unsigned tag = epoch + 1u;
  asm volatile("" : "+v"(mx), "+v"(vtf0), "+v"(vtf1), "+v"(tag));
  STAMP(2);
  __builtin_amdgcn_sched_barrier(0);
#pragma unroll
  for (int t = PRE; t < NT; ++t) v[t] = __builtin_amdgcn_raw_buffer_load_b128(rv, vo, so + t * ST * 1024, AUX);
  __builtin_amdgcn_sched_barrier(0);
  f32x4 acc = {0.f, 0.f, 0.f, 0.f};
#pragma unroll
  for (int t = 0; t < NT; ++t)
    acc = __builtin_amdgcn_mfma_f32_16x16x32_bf16(__builtin_bit_cast(bf16x8, v[t]), __builtin_bit_cast(bf16x8, b[t]), acc, 0, 0, 0);
#pragma unroll
  for (int r = 0; r < 4; ++r) acc[r] += dpp<0x128>(acc[r]);
  f32x2 o2 = {c < 8 ? acc[0] : acc[2], c < 8 ? acc[1] : acc[3]};
  *reinterpret_cast<f32x2*>(lds + wave * 128 + lane * 2) = o2;
  STAMP(3);
  __syncthreads();
  if (wave != 0) return;
  STAMP(4);
  const f32x2 ml = *reinterpret_cast<const f32x2*>(lm + 2 * (c & (WAVES - 1)));
  f32x2 ow[WAVES];
#pragma unroll
  for (int w = 0; w < WAVES; ++w) ow[w] = *reinterpret_cast<const f32x2*>(lds + w * 128 + lane * 2);
  const float MW = row_max16(ml[0]);
  const float f = __builtin_amdgcn_exp2f(ml[0] - MW);
  const float L = row_sum16(f * ml[1]) * (WAVES / 16.0f);
  f32x2 o = ow[0] * dpp<0x150>(f);
#define MRG(W) if (W < WAVES) { const float fw = dpp<0x150 + W>(f); o[0] = fmaf(fw, ow[W % WAVES][0], o[0]); o[1] = fmaf(fw, ow[W % WAVES][1], o[1]); }
  MRG(1) MRG(2) MRG(3) MRG(4) MRG(5) MRG(6) MRG(7) MRG(8) MRG(9) MRG(10) MRG(11) MRG(12) MRG(13) MRG(14) MRG(15)
#undef MRG
  const unsigned rec0 = WS_REC + head * SPLITS * REC;
  if (split != 0) {
    const u32x4 gv = {__float_as_uint(o[0]), tag, __float_as_uint(o[1]), tag};
    __builtin_amdgcn_raw_buffer_store_b128(gv, rw, vo, rec0 + split * REC, 16);
    if (lane == 0) {
      const u32x4 gm = {__float_as_uint(MW), tag, __float_as_uint(L), tag};
      __builtin_amdgcn_raw_buffer_store_b128(gm, rw, 1024, rec0 + split * REC, 16);
    }
    STAMP(5);
    return;
  }
  float lx = c == 0 ? L : 1.0f;
  mx = c == 0 ? MW : mx;
  asm volatile("" : "+v"(lx), "+v"(mx));
  u32x4 gs[SPLITS], gm;
  const unsigned mlo = (unsigned)(((c & 7) == 0 ? 1 : (c & 7)) * REC + 1024);
  for (unsigned spins = 0;; ++spins) {
    asm volatile("" ::: "memory");
    gm = __builtin_amdgcn_raw_buffer_load_b128(rw, mlo, rec0, 16);
#pragma unroll
    for (int s = 1; s < SPLITS; ++s) gs[s] = __builtin_amdgcn_raw_buffer_load_b128(rw, vo, rec0 + s * REC, 16);
    unsigned bad = (gm[1] ^ tag) | (gm[3] ^ tag);
#pragma unroll
    for (int s = 1; s < SPLITS; ++s) bad |= (gs[s][1] ^ tag) | (gs[s][3] ^ tag);
    if (__all(bad == 0) || spins > (1u << 20)) break;
  }
  STAMP(5);
  const bool oth = (unsigned)(c - 1) < 7u;
  const float mc = oth ? __uint_as_float(gm[0]) : mx;
  const float lc = oth ? __uint_as_float(gm[2]) : lx;
  const float wc = __builtin_amdgcn_exp2f(mc - row_max16(mc));
  const float LL = row_sum16(wc * lc);
  float o0 = dpp<0x150>(wc) * o[0], o1 = dpp<0x150>(wc) * o[1];
#define ACC(S) o0 = fmaf(dpp<0x150 + S>(wc), __uint_as_float(gs[S][0]), o0); o1 = fmaf(dpp<0x150 + S>(wc), __uint_as_float(gs[S][2]), o1);
  ACC(1) ACC(2) ACC(3) ACC(4) ACC(5) ACC(6) ACC(7)
#undef ACC
  o0 = fmaf(dpp<0x158>(wc), vtf0, o0); o1 = fmaf(dpp<0x158>(wc), vtf1, o1);
  const float inv = __builtin_amdgcn_rcpf(LL);
  out[head * D + d0] = __float2bfloat16(o0 * inv);
  out[head * D + d0 + 8] = __float2bfloat16(o1 * inv);
  if (lane == 0) __builtin_amdgcn_raw_buffer_store_b32(tag, rw, 0, head * 256, 16);
  STAMP(6);
  if (AMP) { RT(te); unsigned long long tgt = te + (te - ta) * AMPK, now; do { RT(now); } while (now < tgt); }
}

extern "C" void kernel_launch(void* const* d_in, const int* in_sizes, int n_in, void* d_out, int out_size, void* d_ws,
                              size_t ws_size, hipStream_t stream) {
  attn_fused<<<H * SPLITS, WAVES * 64, 0, stream>>>(d_in[1], d_in[2], (const u32x4*)d_in[0], (unsigned char*)d_ws,
                                                    (const u32x4*)d_in[3], (const unsigned short*)d_in[4],
                                                    (const unsigned short*)d_in[5], (__hip_bfloat16*)d_out);
}
```

```cpp
#define WAVES 8 AUX=2 PAT=0 PRE=4
#define AMP 7
#define AMPK 1
#include <hip/hip_runtime.h>
#include <hip/hip_bf16.h>
#include <stdint.h>

#define AUX 2
#define PAT 1
#define HMAP 0
#define PRE 4

constexpr int H = 32, C = 4096, D = 128, SPLITS = 8;
constexpr int NT = C / SPLITS / WAVES / 4;
constexpr float SCALE = 0.088388347648318440f, LOG2E = 1.4426950408889634f;
constexpr float C2 = SCALE * LOG2E;

using bf16x2 = __attribute__((ext_vector_type(2))) __bf16;
using bf16x8 = __attribute__((ext_vector_type(8))) __bf16;
using u32x4 = __attribute__((ext_vector_type(4))) unsigned;
using f32x4 = __attribute__((ext_vector_type(4))) float;
using f32x2 = __attribute__((ext_vector_type(2))) float;
#define GAS __attribute__((address_space(1)))
typedef GAS unsigned gu32;
#define RLX_AGENT __ATOMIC_RELAXED, __HIP_MEMORY_SCOPE_AGENT

__device__ __forceinline__ float dot2(unsigned k, unsigned q, float acc) {
  return __builtin_amdgcn_fdot2_f32_bf16(__builtin_bit_cast(bf16x2, k), __builtin_bit_cast(bf16x2, q), acc, false);
}
template <int CTRL> __device__ __forceinline__ float dpp(float x) {
  return __builtin_bit_cast(float, __builtin_amdgcn_mov_dpp(__builtin_bit_cast(int, x), CTRL, 0xf, 0xf, true));
}
__device__ __forceinline__ float row_sum16(float x) {
  x += dpp<0x128>(x); x += dpp<0x124>(x); x += dpp<0x122>(x); x += dpp<0x121>(x);
  return x;
}
__device__ __forceinline__ float row_max16(float x) {
  x = fmaxf(x, dpp<0x128>(x)); x = fmaxf(x, dpp<0x124>(x)); x = fmaxf(x, dpp<0x122>(x)); x = fmaxf(x, dpp<0x121>(x));
  return x;
}
__device__ __forceinline__ float xrow_max(float x) {
  auto s = __builtin_amdgcn_permlane16_swap(__float_as_uint(x), __float_as_uint(x), false, false);
  x = fmaxf(__uint_as_float(s[0]), __uint_as_float(s[1]));
  auto t = __builtin_amdgcn_permlane32_swap(__float_as_uint(x), __float_as_uint(x), false, false);
  return fmaxf(__uint_as_float(t[0]), __uint_as_float(t[1]));
}
__device__ __forceinline__ float xrow_sum(float x) {
  auto s = __builtin_amdgcn_permlane16_swap(__float_as_uint(x), __float_as_uint(x), false, false);
  x = __uint_as_float(s[0]) + __uint_as_float(s[1]);
  auto t = __builtin_amdgcn_permlane32_swap(__float_as_uint(x), __float_as_uint(x), false, false);
  return __uint_as_float(t[0]) + __uint_as_float(t[1]);
}
__device__ __forceinline__ float bf2f(unsigned short h) { return __uint_as_float((unsigned)h << 16); }

constexpr unsigned WS_REC = 8192, REC = 2048, WS_BYTES = WS_REC + H * SPLITS * REC;

#define AMPEND() do { if (AMP) { RT(te); const unsigned long long dl = AMP == 1 ? te - ta : AMP == 2 ? te - t5 : AMP == 3 ? t5 - t4 : AMP == 4 ? ti - tb : AMP == 5 ? t4 - ta : AMP == 6 ? t4 - t3 : 0; \
  unsigned long long tgt = te + dl * AMPK, now; do { RT(now); } while (now < tgt); } } while (0)
#define RT(x) do { __builtin_amdgcn_sched_barrier(0); asm volatile("s_memrealtime %0\n\ts_waitcnt lgkmcnt(0)" : "=s"(x) :: "memory"); __builtin_amdgcn_sched_barrier(0); } while (0)
#define STAMP(i)

__global__ void __launch_bounds__(WAVES * 64) attn_fused(const void* __restrict__ K, const void* __restrict__ V,
                                                         const u32x4* __restrict__ q, unsigned char* ws,
                                                         const u32x4* __restrict__ kt, const unsigned short* __restrict__ vt,
                                                         const unsigned short* __restrict__ mask, __hip_bfloat16* __restrict__ out) {
  __shared__ __attribute__((aligned(16))) float lds[WAVES * 128 + 2 * WAVES];
  unsigned long long ta = 0, tb = 0, ti = 0, t3 = 0, t4 = 0, t5 = 0, te = 0;
  if (AMP) RT(ta);
  const int wg = blockIdx.x;
  const int head = HMAP ? (wg & 31) : (wg >> 3), split = HMAP ? (wg >> 5) : (wg & 7);
  const int tid = threadIdx.x, lane = tid & 63, wave = __builtin_amdgcn_readfirstlane(tid >> 6);
  const int c = lane & 15, g = lane >> 4;
  STAMP(0);
  const auto rk = __builtin_amdgcn_make_buffer_rsrc((void*)K, 0, H * C * D * 2, 0x00020000);
  const auto rv = __builtin_amdgcn_make_buffer_rsrc((void*)V, 0, H * C * D * 2, 0x00020000);
  constexpr int SW = PAT == 0 ? NT : PAT == 1 ? 1 : SPLITS, ST = PAT == 0 ? 1 : PAT == 1 ? WAVES : SPLITS * WAVES;
  const unsigned so = (unsigned)(head * 1024 + (PAT == 2 ? split : split * 128) + wave * SW) * 1024u;
  const unsigned vo = lane * 16u;
  u32x4 k[NT], v[NT];
  if (AMP == 4) { asm volatile("" :: "s"(K), "s"(V), "s"(q)); RT(tb); }
#pragma unroll
  for (int t = 0; t < NT; ++t) k[t] = __builtin_amdgcn_raw_buffer_load_b128(rk, vo, so + t * ST * 1024, AUX);
  if (AMP == 4) RT(ti);
  const u32x4 qv = q[head * 16 + c];
  const int d0 = 32 * g + 16 * (c >> 3) + (c & 7);
  const unsigned nw = wave == 0 ? WS_BYTES : 0u, nt_ = (wave == 0 && split == 0) ? H * D * 2 + 16 : 0u;
  const auto rw = __builtin_amdgcn_make_buffer_rsrc((void*)ws, 0, nw, 0x00020000);
  const auto rkt = __builtin_amdgcn_make_buffer_rsrc((void*)kt, 0, nt_, 0x00020000);
  const auto rvt = __builtin_amdgcn_make_buffer_rsrc((void*)vt, 0, nt_, 0x00020000);
  const auto rmk = __builtin_amdgcn_make_buffer_rsrc((void*)mask, 0, nt_ ? 16u : 0u, 0x00020000);
  unsigned epoch; u32x4 ktv; unsigned short vt0, vt1, mk;
#define SMALL_LOADS() do { epoch = __builtin_amdgcn_raw_buffer_load_b32(rw, 0, head * 256, 16); \
    ktv = __builtin_amdgcn_raw_buffer_load_b128(rkt, c * 16, head * 256, 0); \
    vt0 = __builtin_amdgcn_raw_buffer_load_b16(rvt, d0 * 2, head * 256, 0); \
    vt1 = __builtin_amdgcn_raw_buffer_load_b16(rvt, d0 * 2 + 16, head * 256, 0); \
    mk = __builtin_amdgcn_raw_buffer_load_b16(rmk, 0, 0, 0); } while (0)
  SMALL_LOADS();
#pragma unroll
  for (int t = 0; t < PRE; ++t) v[t] = __builtin_amdgcn_raw_buffer_load_b128(rv, vo, so + t * ST * 1024, AUX);
  __builtin_amdgcn_sched_barrier(0);
  float x[NT];
#pragma unroll
  for (int t = 0; t < NT; ++t) {
    float d = dot2(k[t][0], qv[0], 0.f);
    d = dot2(k[t][1], qv[1], d); d = dot2(k[t][2], qv[2], d); d = dot2(k[t][3], qv[3], d);
    x[t] = row_sum16(d) * C2;
  }
  STAMP(1);
  float m = x[0];
#pragma unroll
  for (int t = 1; t < NT; ++t) m = fmaxf(m, x[t]);
  m = xrow_max(m);
  const float M = m;
  float* lm = lds + WAVES * 128;
  const int n = c & 7;
  const unsigned lomask = (c >= 8) ? 0xffff0000u : 0u;
  unsigned sel[4];
#pragma unroll
  for (int r = 0; r < 4; ++r) sel[r] = ((n >> 1) == r) ? ((n & 1) ? 0x03020c0cu : 0x0c0c0302u) : 0x0c0c0c0cu;
  float l = 0.f;
  u32x4 b[NT];
#pragma unroll
  for (int t = 0; t < NT; ++t) {
    const float p = __builtin_amdgcn_exp2f(x[t] - M);
    l += p;
    const unsigned pu = __float_as_uint(p);
    const unsigned s = __float_as_uint(p - __uint_as_float(pu & lomask));
#pragma unroll
    for (int r = 0; r < 4; ++r) b[t][r] = __builtin_amdgcn_perm(s, s, sel[r]);
  }
  l = xrow_sum(l);
  if (lane == 0) *reinterpret_cast<f32x2*>(lm + 2 * wave) = f32x2{M, l};
  float mx, vtf0 = bf2f(vt0), vtf1 = bf2f(vt1);
  {
    float d = dot2(ktv[0], qv[0], 0.f);
    d = dot2(ktv[1], qv[1], d); d = dot2(ktv[2], qv[2], d); d = dot2(ktv[3], qv[3], d);
    const float st = (row_sum16(d) * SCALE + (1.0f - bf2f(mk)) * -1000000000.0f) * LOG2E;
    mx = c == 8 ? st : -INFINITY;
  }
  unsigned tag = epoch + 1u;
  asm volatile("" : "+v"(mx), "+v"(vtf0), "+v"(vtf1), "+v"(tag));
  STAMP(2);
  __builtin_amdgcn_sched_barrier(0);
#pragma unroll
  for (int t = PRE; t < NT; ++t) v[t] = __builtin_amdgcn_raw_buffer_load_b128(rv, vo, so + t * ST * 1024, AUX);
  __builtin_amdgcn_sched_barrier(0);
  f32x4 acc = {0.f, 0.f, 0.f, 0.f};
#pragma unroll
  for (int t = 0; t < NT; ++t)
    acc = __builtin_amdgcn_mfma_f32_16x16x32_bf16(__builtin_bit_cast(bf16x8, v[t]), __builtin_bit_cast(bf16x8, b[t]), acc, 0, 0, 0);
#pragma unroll
  for (int r = 0; r < 4; ++r) acc[r] += dpp<0x128>(acc[r]);
  f32x2 o2 = {c < 8 ? acc[0] : acc[2], c < 8 ? acc[1] : acc[3]};
  *reinterpret_cast<f32x2*>(lds + wave * 128 + lane * 2) = o2;
  if (AMP == 6) RT(t3);
  __syncthreads();
  if (wave != 0) return;
  if (AMP == 3 || AMP == 5 || AMP == 6) RT(t4);
  const f32x2 ml = *reinterpret_cast<const f32x2*>(lm + 2 * (c & (WAVES - 1)));
  f32x2 ow[WAVES];
#pragma unroll
  for (int w = 0; w < WAVES; ++w) ow[w] = *reinterpret_cast<const f32x2*>(lds + w * 128 + lane * 2);
  const float MW = row_max16(ml[0]);
  const float f = __builtin_amdgcn_exp2f(ml[0] - MW);
  const float L = row_sum16(f * ml[1]) * (WAVES / 16.0f);
  f32x2 o = ow[0] * dpp<0x150>(f);
#define MRG(W) if (W < WAVES) { const float fw = dpp<0x150 + W>(f); o[0] = fmaf(fw, ow[W % WAVES][0], o[0]); o[1] = fmaf(fw, ow[W % WAVES][1], o[1]); }
  MRG(1) MRG(2) MRG(3) MRG(4) MRG(5) MRG(6) MRG(7) MRG(8) MRG(9) MRG(10) MRG(11) MRG(12) MRG(13) MRG(14) MRG(15)
#undef MRG
  const unsigned rec0 = WS_REC + head * SPLITS * REC;
  if (split != 0) {
    const u32x4 gv = {__float_as_uint(o[0]), tag, __float_as_uint(o[1]), tag};
    __builtin_amdgcn_raw_buffer_store_b128(gv, rw, vo, rec0 + split * REC, 16);
    if (lane == 0) {
      const u32x4 gm = {__float_as_uint(MW), tag, __float_as_uint(L), tag};
      __builtin_amdgcn_raw_buffer_store_b128(gm, rw, 1024, rec0 + split * REC, 16);
    }
    if (AMP == 3) RT(t5);
    if (AMP >= 3) AMPEND();
    return;
  }
  float lx = c == 0 ? L : 1.0f;
  mx = c == 0 ? MW : mx;
  asm volatile("" : "+v"(lx), "+v"(mx));
  u32x4 gs[SPLITS], gm;
  const unsigned mlo = (unsigned)(((c & 7) == 0 ? 1 : (c & 7)) * REC + 1024);
  for (unsigned spins = 0;; ++spins) {
    asm volatile("" ::: "memory");
    gm = __builtin_amdgcn_raw_buffer_load_b128(rw, mlo, rec0, 16);
#pragma unroll
    for (int s = 1; s < SPLITS; ++s) gs[s] = __builtin_amdgcn_raw_buffer_load_b128(rw, vo, rec0 + s * REC, 16);
    unsigned bad = (gm[1] ^ tag) | (gm[3] ^ tag);
#pragma unroll
    for (int s = 1; s < SPLITS; ++s) bad |= (gs[s][1] ^ tag) | (gs[s][3] ^ tag);
    if (__all(bad == 0) || spins > (1u << 20)) break;
  }
  if (AMP == 2) RT(t5);
  if (AMP == 3) t5 = t4;
  const bool oth = (unsigned)(c - 1) < 7u;
  const float mc = oth ? __uint_as_float(gm[0]) : mx;
  const float lc = oth ? __uint_as_float(gm[2]) : lx;
  const float wc = __builtin_amdgcn_exp2f(mc - row_max16(mc));
  const float LL = row_sum16(wc * lc);
  float o0 = dpp<0x150>(wc) * o[0], o1 = dpp<0x150>(wc) * o[1];
#define ACC(S) o0 = fmaf(dpp<0x150 + S>(wc), __uint_as_float(gs[S][0]), o0); o1 = fmaf(dpp<0x150 + S>(wc), __uint_as_float(gs[S][2]), o1);
  ACC(1) ACC(2) ACC(3) ACC(4) ACC(5) ACC(6) ACC(7)
#undef ACC
  o0 = fmaf(dpp<0x158>(wc), vtf0, o0); o1 = fmaf(dpp<0x158>(wc), vtf1, o1);
  const float inv = __builtin_amdgcn_rcpf(LL);
  out[head * D + d0] = __float2bfloat16(o0 * inv);
  out[head * D + d0 + 8] = __float2bfloat16(o1 * inv);
  if (lane == 0) __builtin_amdgcn_raw_buffer_store_b32(tag, rw, 0, head * 256, 16);
  STAMP(6);
  AMPEND();
}

extern "C" void kernel_launch(void* const* d_in, const int* in_sizes, int n_in, void* d_out, int out_size, void* d_ws,
                              size_t ws_size, hipStream_t stream) {
  attn_fused<<<H * SPLITS, WAVES * 64, 0, stream>>>(d_in[1], d_in[2], (const u32x4*)d_in[0], (unsigned char*)d_ws,
                                                    (const u32x4*)d_in[3], (const unsigned short*)d_in[4],
                                                    (const unsigned short*)d_in[5], (__hip_bfloat16*)d_out);
}
```

```cpp
#define AUX 2 PAT=0 WAVES=8 PRE=4
#define NUMA 1
#define HSTRV 20480
#include <hip/hip_runtime.h>
#include <hip/hip_bf16.h>
#include <stdint.h>

#define WAVES 8
#define PAT 1
#define HMAP 0
#define PRE 4
#define AMP 0
#define AMPK 1

constexpr int H = 32, C = 4096, D = 128, SPLITS = 8;
constexpr int NT = C / SPLITS / WAVES / 4;
constexpr float SCALE = 0.088388347648318440f, LOG2E = 1.4426950408889634f;
constexpr float C2 = SCALE * LOG2E;

using bf16x2 = __attribute__((ext_vector_type(2))) __bf16;
using bf16x8 = __attribute__((ext_vector_type(8))) __bf16;
using u32x4 = __attribute__((ext_vector_type(4))) unsigned;
using f32x4 = __attribute__((ext_vector_type(4))) float;
using u32x2 = __attribute__((ext_vector_type(2))) unsigned;
typedef __attribute__((address_space(1))) unsigned long long gu64;
using f32x2 = __attribute__((ext_vector_type(2))) float;
#define GAS __attribute__((address_space(1)))
typedef GAS unsigned gu32;
#define RLX_AGENT __ATOMIC_RELAXED, __HIP_MEMORY_SCOPE_AGENT

__device__ __forceinline__ float dot2(unsigned k, unsigned q, float acc) {
  return __builtin_amdgcn_fdot2_f32_bf16(__builtin_bit_cast(bf16x2, k), __builtin_bit_cast(bf16x2, q), acc, false);
}
template <int CTRL> __device__ __forceinline__ float dpp(float x) {
  return __builtin_bit_cast(float, __builtin_amdgcn_mov_dpp(__builtin_bit_cast(int, x), CTRL, 0xf, 0xf, true));
}
__device__ __forceinline__ float row_sum16(float x) {
  x += dpp<0x128>(x); x += dpp<0x124>(x); x += dpp<0x122>(x); x += dpp<0x121>(x);
  return x;
}
__device__ __forceinline__ float row_max16(float x) {
  asm volatile("s_nop 1\n\tv_max_f32_dpp %0, %0, %0 row_ror:8 row_mask:0xf bank_mask:0xf\n\ts_nop 1\n\t"
               "v_max_f32_dpp %0, %0, %0 row_ror:4 row_mask:0xf bank_mask:0xf\n\ts_nop 1\n\t"
               "v_max_f32_dpp %0, %0, %0 row_ror:2 row_mask:0xf bank_mask:0xf\n\ts_nop 1\n\t"
               "v_max_f32_dpp %0, %0, %0 row_ror:1 row_mask:0xf bank_mask:0xf" : "+v"(x));
  return x;
}
__device__ __forceinline__ float xrow_max(float x) {
  auto s = __builtin_amdgcn_permlane16_swap(__float_as_uint(x), __float_as_uint(x), false, false);
  x = fmaxf(__uint_as_float(s[0]), __uint_as_float(s[1]));
  auto t = __builtin_amdgcn_permlane32_swap(__float_as_uint(x), __float_as_uint(x), false, false);
  return fmaxf(__uint_as_float(t[0]), __uint_as_float(t[1]));
}
__device__ __forceinline__ float xrow_sum(float x) {
  auto s = __builtin_amdgcn_permlane16_swap(__float_as_uint(x), __float_as_uint(x), false, false);
  x = __uint_as_float(s[0]) + __uint_as_float(s[1]);
  auto t = __builtin_amdgcn_permlane32_swap(__float_as_uint(x), __float_as_uint(x), false, false);
  return __uint_as_float(t[0]) + __uint_as_float(t[1]);
}
__device__ __forceinline__ float bf2f(unsigned short h) { return __uint_as_float((unsigned)h << 16); }

constexpr unsigned HSTR = 256, RSTR = HSTRV, CPY = 16384, REC = 640, WS_BYTES = CPY + H * RSTR;

#define AMPEND() do { if (AMP) { RT(te); const unsigned long long dl = AMP == 1 ? te - ta : AMP == 2 ? te - t5 : AMP == 3 ? t5 - t4 : AMP == 4 ? ti - tb : AMP == 5 ? t4 - ta : AMP == 6 ? t4 - t3 : 0; \
  unsigned long long tgt = te + dl * AMPK, now; do { RT(now); } while (now < tgt); } } while (0)
#define RT(x) do { __builtin_amdgcn_sched_barrier(0); asm volatile("s_memrealtime %0\n\ts_waitcnt lgkmcnt(0)" : "=s"(x) :: "memory"); __builtin_amdgcn_sched_barrier(0); } while (0)
#define DB0()
#define DB1()
#define DBS(x)
#define DBEND(sw, last)

__global__ void __launch_bounds__(WAVES * 64) attn_fused(const void* __restrict__ K, const void* __restrict__ V,
                                                         const u32x4* __restrict__ q, unsigned char* ws,
                                                         const u32x4* __restrict__ kt, const unsigned short* __restrict__ vt,
                                                         const unsigned short* __restrict__ mask, __hip_bfloat16* __restrict__ out) {
  __shared__ __attribute__((aligned(16))) float lds[WAVES * 128 + 2 * WAVES];
  unsigned long long ta = 0, tb = 0, ti = 0, t3 = 0, t4 = 0, t5 = 0, te = 0;
  if (AMP) RT(ta);
  const int wg = blockIdx.x;
  const int head = HMAP ? (wg & 31) : (wg >> 3), split = HMAP ? (wg >> 5) : (wg & 7);
  const int tid = threadIdx.x, lane = tid & 63, wave = __builtin_amdgcn_readfirstlane(tid >> 6);
  const int c = lane & 15, g = lane >> 4;
  DB0();
  const auto rk = __builtin_amdgcn_make_buffer_rsrc((void*)K, 0, H * C * D * 2, 0x00020000);
  const auto rv = __builtin_amdgcn_make_buffer_rsrc((void*)V, 0, H * C * D * 2, 0x00020000);
  constexpr int SW = PAT == 0 ? NT : PAT == 1 ? 1 : SPLITS, ST = PAT == 0 ? 1 : PAT == 1 ? WAVES : SPLITS * WAVES;
  const unsigned so = (unsigned)(head * 1024 + (PAT == 2 ? split : split * 128) + wave * SW) * 1024u;
  const unsigned vo = lane * 16u;
  u32x4 k[NT], v[NT];
  asm volatile("" :: "s"(out));
  if (AMP == 4) { asm volatile("" :: "s"(K), "s"(V), "s"(q)); RT(tb); }
#pragma unroll
  for (int t = 0; t < NT; ++t) k[t] = __builtin_amdgcn_raw_buffer_load_b128(rk, vo, so + t * ST * 1024, AUX);
  if (AMP == 4) RT(ti);
  const u32x4 qv = q[head * 16 + c];
  const int d0 = 32 * g + 16 * (c >> 3) + (c & 7);
  const unsigned nw = wave == 0 ? WS_BYTES : 0u, nt_ = wave == 0 ? H * D * 2 + 16 : 0u;
  const auto rw = __builtin_amdgcn_make_buffer_rsrc((void*)ws, 0, nw, 0x00020000);
  const auto rkt = __builtin_amdgcn_make_buffer_rsrc((void*)kt, 0, nt_, 0x00020000);
  const auto rvt = __builtin_amdgcn_make_buffer_rsrc((void*)vt, 0, nt_, 0x00020000);
  const auto rmk = __builtin_amdgcn_make_buffer_rsrc((void*)mask, 0, nt_ ? 16u : 0u, 0x00020000);
  unsigned epoch; u32x2 w0; u32x4 ktv; unsigned short vt0, vt1, mk;
#define SMALL_LOADS() do { epoch = __builtin_amdgcn_raw_buffer_load_b32(rw, 0, head * HSTR, 16); \
    w0 = __builtin_amdgcn_raw_buffer_load_b64(rw, 128, head * HSTR, 16); \
    ktv = __builtin_amdgcn_raw_buffer_load_b128(rkt, c * 16, head * 256, 0); \
    vt0 = __builtin_amdgcn_raw_buffer_load_b16(rvt, d0 * 2, head * 256, 0); \
    vt1 = __builtin_amdgcn_raw_buffer_load_b16(rvt, d0 * 2 + 16, head * 256, 0); \
    mk = __builtin_amdgcn_raw_buffer_load_b16(rmk, 0, 0, 0); } while (0)
  SMALL_LOADS();
#pragma unroll
  for (int t = 0; t < PRE; ++t) v[t] = __builtin_amdgcn_raw_buffer_load_b128(rv, vo, so + t * ST * 1024, AUX);
  __builtin_amdgcn_sched_barrier(0);
  float x[NT];
#pragma unroll
  for (int t = 0; t < NT; ++t) {
    float d = dot2(k[t][0], qv[0], 0.f);
    d = dot2(k[t][1], qv[1], d); d = dot2(k[t][2], qv[2], d); d = dot2(k[t][3], qv[3], d);
    x[t] = row_sum16(d) * C2;
  }
  float m = x[0];
#pragma unroll
  for (int t = 1; t < NT; ++t) m = fmaxf(m, x[t]);
  m = xrow_max(m);
  const float M = m;
  float* lm = lds + WAVES * 128;
  const int n = c & 7;
  const unsigned lomask = (c >= 8) ? 0xffff0000u : 0u;
  unsigned sel[4];
#pragma unroll
  for (int r = 0; r < 4; ++r) sel[r] = ((n >> 1) == r) ? ((n & 1) ? 0x03020c0cu : 0x0c0c0302u) : 0x0c0c0c0cu;
  float l = 0.f;
  u32x4 b[NT];
#pragma unroll
  for (int t = 0; t < NT; ++t) {
    const float p = __builtin_amdgcn_exp2f(x[t] - M);
    l += p;
    const unsigned pu = __float_as_uint(p);
    const unsigned s = __float_as_uint(p - __uint_as_float(pu & lomask));
#pragma unroll
    for (int r = 0; r < 4; ++r) b[t][r] = __builtin_amdgcn_perm(s, s, sel[r]);
  }
  l = xrow_sum(l);
  if (lane == 0) *reinterpret_cast<f32x2*>(lm + 2 * wave) = f32x2{M, l};
  float mx, vtf0 = bf2f(vt0), vtf1 = bf2f(vt1);
  {
    float d = dot2(ktv[0], qv[0], 0.f);
    d = dot2(ktv[1], qv[1], d); d = dot2(ktv[2], qv[2], d); d = dot2(ktv[3], qv[3], d);
    const float st = (row_sum16(d) * SCALE + (1.0f - bf2f(mk)) * -1000000000.0f) * LOG2E;
    mx = c == 8 ? st : -INFINITY;
  }
  unsigned tag = epoch + 1u;
  asm volatile("" : "+v"(mx), "+v"(vtf0), "+v"(vtf1), "+v"(tag));
  gu64* cnt = (gu64*)(ws + head * HSTR + 128);
  if (tid == 0 && w0[1] != tag) {
    unsigned long long e = ((unsigned long long)w0[1] << 32) | w0[0];
    __hip_atomic_compare_exchange_strong(cnt, &e, (unsigned long long)tag << 32, __ATOMIC_RELAXED, __ATOMIC_RELAXED, __HIP_MEMORY_SCOPE_AGENT);
  }
  __builtin_amdgcn_sched_barrier(0);
#pragma unroll
  for (int t = PRE; t < NT; ++t) v[t] = __builtin_amdgcn_raw_buffer_load_b128(rv, vo, so + t * ST * 1024, AUX);
  __builtin_amdgcn_sched_barrier(0);
  __syncthreads();
  const f32x2 ml = *reinterpret_cast<const f32x2*>(lm + 2 * (c & (WAVES - 1)));
  const float MW = __uint_as_float(__float_as_uint(row_max16(ml[0])) & 0xffffff00u);
  float L = row_sum16(__builtin_amdgcn_exp2f(ml[0] - MW) * ml[1]) * (WAVES / 16.0f);
  float fw = __builtin_amdgcn_exp2f(M - MW);
  L = __uint_as_float(__float_as_uint(L) & 0xffffff00u);
  float lx = c == split ? L : 1.0f;
  mx = c == split ? MW : mx;
  unsigned tg = tag & 0xffu;
  u32x2 gml = {__float_as_uint(MW) | tg, __float_as_uint(L) | tg};
  const bool oth = c < 8 && c != split;
  unsigned keepml = oth ? 0xffffff00u : 0u;
  const unsigned hb = head * RSTR + CPY;
  const unsigned long long wa = (unsigned long long)ws + hb + 2048;
  const unsigned near = NUMA ? ((((unsigned)(wa >> 13) ^ (unsigned)(wa >> 21) ^ ((unsigned)__builtin_amdgcn_s_getreg(20 | (3 << 11)) >> 2)) & 1u) << 13) : 0u;
  unsigned ca = hb + near, cb = hb + (near ^ 8192u), mine = hb + split * REC;
  unsigned vo8 = lane * 8u, vo8h = vo8 + 4 * REC, mlo = (c & 7) * REC + 512u + g * 8u, lo8 = lane * 8u;
  unsigned ato = lane == 0 ? head * HSTR + 128u : WS_BYTES, mo = lane < 16 ? vo8 + 512u : WS_BYTES;
  unsigned kbml = keepml >> 24;
  mlo = oth ? mlo : WS_BYTES;
  int old = 1;
  unsigned ks[SPLITS];
#pragma unroll
  for (int s = 0; s < SPLITS; ++s) { ks[s] = s == split ? 0u : 0xffffff00u; asm volatile("" : "+s"(ks[s])); }
  __hip_bfloat16* op = out + head * D + d0;
  asm volatile("" : "+v"(fw), "+v"(lx), "+v"(mx), "+v"(gml), "+v"(keepml), "+v"(vo8), "+v"(vo8h), "+v"(mlo), "+v"(ato), "+v"(mo), "+v"(tg), "+v"(kbml), "+v"(old), "+v"(op), "+v"(lo8));
  asm volatile("" : "+s"(ca), "+s"(cb), "+s"(mine));
  __builtin_amdgcn_sched_barrier(0);
  f32x4 acc = {0.f, 0.f, 0.f, 0.f};
#pragma unroll
  for (int t = 0; t < NT; ++t)
    acc = __builtin_amdgcn_mfma_f32_16x16x32_bf16(__builtin_bit_cast(bf16x8, v[t]), __builtin_bit_cast(bf16x8, b[t]), acc, 0, 0, 0);
#pragma unroll
  for (int r = 0; r < 4; ++r) acc[r] += dpp<0x128>(acc[r]);
  f32x2 o2 = {c < 8 ? acc[0] : acc[2], c < 8 ? acc[1] : acc[3]};
  o2 *= fw;
  *reinterpret_cast<f32x2*>(lds + wave * 128 + lane * 2) = o2;
  if (AMP == 6) RT(t3);
  __syncthreads();
  if (wave != 0) return;
  DB1();
  u32x2 ga[SPLITS], gb[SPLITS], ma, mb;
#define SWEEP(G, MLV, BASE) do { MLV = __builtin_amdgcn_raw_buffer_load_b64(rw, mlo, BASE, 16); \
    _Pragma("unroll") for (int s = 0; s < SPLITS; ++s) G[s] = __builtin_amdgcn_raw_buffer_load_b64(rw, (s < 4 ? vo8 : vo8h) + (s & 3) * REC, (BASE) + (~ks[s] & 0x40000000u), 16); } while (0)
  old = __builtin_amdgcn_raw_ptr_buffer_atomic_add_i32(old, rw, ato, 0, 0);
  __builtin_amdgcn_sched_barrier(0);
  f32x2 ow[WAVES];
#pragma unroll
  for (int w = 0; w < WAVES; ++w) ow[w] = *reinterpret_cast<const f32x2*>(reinterpret_cast<const char*>(lds) + lo8 + w * 512);
#pragma unroll
  for (int st = 1; st < WAVES; st *= 2)
#pragma unroll
    for (int w = 0; w < WAVES; w += 2 * st) ow[w] += ow[w + st];
  unsigned oc0 = __float_as_uint(ow[0][0]) & 0xffffff00u, oc1 = __float_as_uint(ow[0][1]) & 0xffffff00u;
  {
    const u32x2 gv = {oc0 | tg, oc1 | tg};
    __builtin_amdgcn_raw_buffer_store_b64(gv, rw, vo8, mine, 16);
    __builtin_amdgcn_raw_buffer_store_b64(gv, rw, vo8, mine + 8192u, 16);
    __builtin_amdgcn_raw_buffer_store_b64(gml, rw, mo, mine, 16);
    __builtin_amdgcn_raw_buffer_store_b64(gml, rw, mo, mine + 8192u, 16);
  }
  DBS(dbp);
  __builtin_amdgcn_sched_barrier(0);
  if (__builtin_amdgcn_readfirstlane(old) != SPLITS - 1) { DBEND(0u, 0u); return; }
  SWEEP(ga, ma, ca);
  __builtin_amdgcn_sched_barrier(0);
  f32x2 o;
  unsigned bad;
#define BFI(M, X, Y) (((X) & (M)) | ((Y) & ~(M)))
#define ACC(G, S) { asm volatile("" : "+v"(G[S])); const float w_ = dpp<0x150 + S>(wc); \
    bad |= ((G[S][0] ^ tg) | (G[S][1] ^ tg)) & (ks[S] >> 24); \
    const f32x2 x_ = {__uint_as_float(BFI(ks[S], G[S][0], oc0)), __uint_as_float(BFI(ks[S], G[S][1], oc1))}; \
    o = __builtin_elementwise_fma(f32x2{w_, w_}, x_, o); asm volatile("" : "+v"(o), "+v"(bad)); }
#define FINISH(G, MLV) do { asm volatile("" : "+v"(MLV)); \
    bad = ((MLV[0] ^ tg) | (MLV[1] ^ tg)) & kbml; \
    const float mc = __uint_as_float(BFI(keepml, MLV[0], __float_as_uint(mx))); \
    const float lc = __uint_as_float(BFI(keepml, MLV[1], __float_as_uint(lx))); \
    float wc = __builtin_amdgcn_exp2f(mc - row_max16(mc)); \
    wc *= __builtin_amdgcn_rcpf(row_sum16(wc * lc)); \
    const float wt = dpp<0x158>(wc); \
    o = f32x2{wt * vtf0, wt * vtf1}; \
    asm volatile("" : "+v"(o), "+v"(bad), "+v"(wc)); \
    ACC(G, 0) ACC(G, 1) ACC(G, 2) ACC(G, 3) ACC(G, 4) ACC(G, 5) ACC(G, 6) ACC(G, 7) } while (0)
  unsigned nsw = 1;
  FINISH(ga, ma);
  DBS(dba);
  if (!__all(bad == 0)) {
    for (unsigned spins = 0; !__all(bad == 0) && spins < (1u << 20); ++spins) {
      asm volatile("" ::: "memory");
      SWEEP(gb, mb, cb); ++nsw;
      FINISH(gb, mb);
    }
  }
  DBS(dbb);
  op[0] = __float2bfloat16(o[0]);
  op[8] = __float2bfloat16(o[1]);
  __builtin_amdgcn_sched_barrier(0);
  if (lane == 0) __builtin_amdgcn_raw_buffer_store_b32(tag, rw, 0, head * HSTR, 16);
  DBEND(nsw, 1u);
  AMPEND();
}

extern "C" void kernel_launch(void* const* d_in, const int* in_sizes, int n_in, void* d_out, int out_size, void* d_ws,
                              size_t ws_size, hipStream_t stream) {
  attn_fused<<<H * SPLITS, WAVES * 64, 0, stream>>>(d_in[1], d_in[2], (const u32x4*)d_in[0], (unsigned char*)d_ws,
                                                    (const u32x4*)d_in[3], (const unsigned short*)d_in[4],
                                                    (const unsigned short*)d_in[5], (__hip_bfloat16*)d_out);
}
```

```cpp
#define AUX 2 PAT=0 WAVES=8 PRE=4 NUMA=1 HSTRV=16384
#define AMP 9
#define AMPK 2
#define XSEL 6
#include <hip/hip_runtime.h>
#include <hip/hip_bf16.h>
#include <stdint.h>

#define WAVES 8
#define PAT 1
#define HMAP 0
#define PRE 4
#define NUMA 1
#define HSTRV 20480

constexpr int H = 32, C = 4096, D = 128, SPLITS = 8;
constexpr int NT = C / SPLITS / WAVES / 4;
constexpr float SCALE = 0.088388347648318440f, LOG2E = 1.4426950408889634f;
constexpr float C2 = SCALE * LOG2E;

using bf16x2 = __attribute__((ext_vector_type(2))) __bf16;
using bf16x8 = __attribute__((ext_vector_type(8))) __bf16;
using u32x4 = __attribute__((ext_vector_type(4))) unsigned;
using f32x4 = __attribute__((ext_vector_type(4))) float;
using u32x2 = __attribute__((ext_vector_type(2))) unsigned;
typedef __attribute__((address_space(1))) unsigned long long gu64;
using f32x2 = __attribute__((ext_vector_type(2))) float;
#define GAS __attribute__((address_space(1)))
typedef GAS unsigned gu32;
#define RLX_AGENT __ATOMIC_RELAXED, __HIP_MEMORY_SCOPE_AGENT

__device__ __forceinline__ float dot2(unsigned k, unsigned q, float acc) {
  return __builtin_amdgcn_fdot2_f32_bf16(__builtin_bit_cast(bf16x2, k), __builtin_bit_cast(bf16x2, q), acc, false);
}
template <int CTRL> __device__ __forceinline__ float dpp(float x) {
  return __builtin_bit_cast(float, __builtin_amdgcn_mov_dpp(__builtin_bit_cast(int, x), CTRL, 0xf, 0xf, true));
}
__device__ __forceinline__ float row_sum16(float x) {
  x += dpp<0x128>(x); x += dpp<0x124>(x); x += dpp<0x122>(x); x += dpp<0x121>(x);
  return x;
}
__device__ __forceinline__ float row_max16(float x) {
  asm volatile("s_nop 1\n\tv_max_f32_dpp %0, %0, %0 row_ror:8 row_mask:0xf bank_mask:0xf\n\ts_nop 1\n\t"
               "v_max_f32_dpp %0, %0, %0 row_ror:4 row_mask:0xf bank_mask:0xf\n\ts_nop 1\n\t"
               "v_max_f32_dpp %0, %0, %0 row_ror:2 row_mask:0xf bank_mask:0xf\n\ts_nop 1\n\t"
               "v_max_f32_dpp %0, %0, %0 row_ror:1 row_mask:0xf bank_mask:0xf" : "+v"(x));
  return x;
}
__device__ __forceinline__ float xrow_max(float x) {
  auto s = __builtin_amdgcn_permlane16_swap(__float_as_uint(x), __float_as_uint(x), false, false);
  x = fmaxf(__uint_as_float(s[0]), __uint_as_float(s[1]));
  auto t = __builtin_amdgcn_permlane32_swap(__float_as_uint(x), __float_as_uint(x), false, false);
  return fmaxf(__uint_as_float(t[0]), __uint_as_float(t[1]));
}
__device__ __forceinline__ float xrow_sum(float x) {
  auto s = __builtin_amdgcn_permlane16_swap(__float_as_uint(x), __float_as_uint(x), false, false);
  x = __uint_as_float(s[0]) + __uint_as_float(s[1]);
  auto t = __builtin_amdgcn_permlane32_swap(__float_as_uint(x), __float_as_uint(x), false, false);
  return __uint_as_float(t[0]) + __uint_as_float(t[1]);
}
__device__ __forceinline__ float bf2f(unsigned short h) { return __uint_as_float((unsigned)h << 16); }

constexpr unsigned HSTR = 256, RSTR = HSTRV, CPY = 16384, REC = 640, WS_BYTES = CPY + H * RSTR;

#define AMPEND() do { if (AMP) { RT(te); const unsigned long long dl = AMP == 1 ? te - ta : AMP == 2 ? te - t5 : AMP == 3 ? t5 - t4 : AMP == 4 ? ti - tb : AMP == 5 ? t4 - ta : AMP == 6 ? t4 - t3 : 0; \
  unsigned long long tgt = te + dl * AMPK, now; do { RT(now); } while (now < tgt); } } while (0)
#define RT(x) do { __builtin_amdgcn_sched_barrier(0); asm volatile("s_memrealtime %0\n\ts_waitcnt lgkmcnt(0)" : "=s"(x) :: "memory"); __builtin_amdgcn_sched_barrier(0); } while (0)
#define DB0()
#define DB1()
#define DBS(x)
#define DBEND(sw, last)

__global__ void __launch_bounds__(WAVES * 64) attn_fused(const void* __restrict__ K, const void* __restrict__ V,
                                                         const u32x4* __restrict__ q, unsigned char* ws,
                                                         const u32x4* __restrict__ kt, const unsigned short* __restrict__ vt,
                                                         const unsigned short* __restrict__ mask, __hip_bfloat16* __restrict__ out) {
  __shared__ __attribute__((aligned(16))) float lds[WAVES * 128 + 2 * WAVES];
  unsigned long long ta = 0, tb = 0, ti = 0, t3 = 0, t4 = 0, t5 = 0, te = 0;
  if (AMP) RT(ta);
  const int wg = blockIdx.x;
  const int head = HMAP ? (wg & 31) : (wg >> 3), split = HMAP ? (wg >> 5) : (wg & 7);
  const int tid = threadIdx.x, lane = tid & 63, wave = __builtin_amdgcn_readfirstlane(tid >> 6);
  const int c = lane & 15, g = lane >> 4;
  DB0();
  if (AMP >= 8 && tid == 0) __hip_atomic_store((gu64*)(ws + (4u << 20) + wg * 128), (ta << 4) | ((unsigned)__builtin_amdgcn_s_getreg(20 | (3 << 11)) & 7u), RLX_AGENT);
  const auto rk = __builtin_amdgcn_make_buffer_rsrc((void*)K, 0, H * C * D * 2, 0x00020000);
  const auto rv = __builtin_amdgcn_make_buffer_rsrc((void*)V, 0, H * C * D * 2, 0x00020000);
  constexpr int SW = PAT == 0 ? NT : PAT == 1 ? 1 : SPLITS, ST = PAT == 0 ? 1 : PAT == 1 ? WAVES : SPLITS * WAVES;
  const unsigned so = (unsigned)(head * 1024 + (PAT == 2 ? split : split * 128) + wave * SW) * 1024u;
  const unsigned vo = lane * 16u;
  u32x4 k[NT], v[NT];
  asm volatile("" :: "s"(out));
  if (AMP == 4) { asm volatile("" :: "s"(K), "s"(V), "s"(q)); RT(tb); }
#pragma unroll
  for (int t = 0; t < NT; ++t) k[t] = __builtin_amdgcn_raw_buffer_load_b128(rk, vo, so + t * ST * 1024, AUX);
  if (AMP == 4) RT(ti);
  const u32x4 qv = q[head * 16 + c];
  const int d0 = 32 * g + 16 * (c >> 3) + (c & 7);
  const unsigned nw = wave == 0 ? WS_BYTES : 0u, nt_ = wave == 0 ? H * D * 2 + 16 : 0u;
  const auto rw = __builtin_amdgcn_make_buffer_rsrc((void*)ws, 0, nw, 0x00020000);
  const auto rkt = __builtin_amdgcn_make_buffer_rsrc((void*)kt, 0, nt_, 0x00020000);
  const auto rvt = __builtin_amdgcn_make_buffer_rsrc((void*)vt, 0, nt_, 0x00020000);
  const auto rmk = __builtin_amdgcn_make_buffer_rsrc((void*)mask, 0, nt_ ? 16u : 0u, 0x00020000);
  unsigned epoch; u32x2 w0; u32x4 ktv; unsigned short vt0, vt1, mk;
#define SMALL_LOADS() do { epoch = __builtin_amdgcn_raw_buffer_load_b32(rw, 0, head * HSTR, 16); \
    w0 = __builtin_amdgcn_raw_buffer_load_b64(rw, 128, head * HSTR, 16); \
    ktv = __builtin_amdgcn_raw_buffer_load_b128(rkt, c * 16, head * 256, 0); \
    vt0 = __builtin_amdgcn_raw_buffer_load_b16(rvt, d0 * 2, head * 256, 0); \
    vt1 = __builtin_amdgcn_raw_buffer_load_b16(rvt, d0 * 2 + 16, head * 256, 0); \
    mk = __builtin_amdgcn_raw_buffer_load_b16(rmk, 0, 0, 0); } while (0)
  SMALL_LOADS();
#pragma unroll
  for (int t = 0; t < PRE; ++t) v[t] = __builtin_amdgcn_raw_buffer_load_b128(rv, vo, so + t * ST * 1024, AUX);
  __builtin_amdgcn_sched_barrier(0);
  float x[NT];
#pragma unroll
  for (int t = 0; t < NT; ++t) {
    float d = dot2(k[t][0], qv[0], 0.f);
    d = dot2(k[t][1], qv[1], d); d = dot2(k[t][2], qv[2], d); d = dot2(k[t][3], qv[3], d);
    x[t] = row_sum16(d) * C2;
  }
  float m = x[0];
#pragma unroll
  for (int t = 1; t < NT; ++t) m = fmaxf(m, x[t]);
  m = xrow_max(m);
  const float M = m;
  float* lm = lds + WAVES * 128;
  const int n = c & 7;
  const unsigned lomask = (c >= 8) ? 0xffff0000u : 0u;
  unsigned sel[4];
#pragma unroll
  for (int r = 0; r < 4; ++r) sel[r] = ((n >> 1) == r) ? ((n & 1) ? 0x03020c0cu : 0x0c0c0302u) : 0x0c0c0c0cu;
  float l = 0.f;
  u32x4 b[NT];
#pragma unroll
  for (int t = 0; t < NT; ++t) {
    const float p = __builtin_amdgcn_exp2f(x[t] - M);
    l += p;
    const unsigned pu = __float_as_uint(p);
    const unsigned s = __float_as_uint(p - __uint_as_float(pu & lomask));
#pragma unroll
    for (int r = 0; r < 4; ++r) b[t][r] = __builtin_amdgcn_perm(s, s, sel[r]);
  }
  l = xrow_sum(l);
  if (lane == 0) *reinterpret_cast<f32x2*>(lm + 2 * wave) = f32x2{M, l};
  float mx, vtf0 = bf2f(vt0), vtf1 = bf2f(vt1);
  {
    float d = dot2(ktv[0], qv[0], 0.f);
    d = dot2(ktv[1], qv[1], d); d = dot2(ktv[2], qv[2], d); d = dot2(ktv[3], qv[3], d);
    const float st = (row_sum16(d) * SCALE + (1.0f - bf2f(mk)) * -1000000000.0f) * LOG2E;
    mx = c == 8 ? st : -INFINITY;
  }
  unsigned tag = epoch + 1u;
  asm volatile("" : "+v"(mx), "+v"(vtf0), "+v"(vtf1), "+v"(tag));
  gu64* cnt = (gu64*)(ws + head * HSTR + 128);
  if (tid == 0 && w0[1] != tag) {
    unsigned long long e = ((unsigned long long)w0[1] << 32) | w0[0];
    __hip_atomic_compare_exchange_strong(cnt, &e, (unsigned long long)tag << 32, __ATOMIC_RELAXED, __ATOMIC_RELAXED, __HIP_MEMORY_SCOPE_AGENT);
  }
  __builtin_amdgcn_sched_barrier(0);
#pragma unroll
  for (int t = PRE; t < NT; ++t) v[t] = __builtin_amdgcn_raw_buffer_load_b128(rv, vo, so + t * ST * 1024, AUX);
  __builtin_amdgcn_sched_barrier(0);
  __syncthreads();
  const f32x2 ml = *reinterpret_cast<const f32x2*>(lm + 2 * (c & (WAVES - 1)));
  const float MW = __uint_as_float(__float_as_uint(row_max16(ml[0])) & 0xffffff00u);
  float L = row_sum16(__builtin_amdgcn_exp2f(ml[0] - MW) * ml[1]) * (WAVES / 16.0f);
  float fw = __builtin_amdgcn_exp2f(M - MW);
  L = __uint_as_float(__float_as_uint(L) & 0xffffff00u);
  float lx = c == split ? L : 1.0f;
  mx = c == split ? MW : mx;
  unsigned tg = tag & 0xffu;
  u32x2 gml = {__float_as_uint(MW) | tg, __float_as_uint(L) | tg};
  const bool oth = c < 8 && c != split;
  unsigned keepml = oth ? 0xffffff00u : 0u;
  const unsigned hb = head * RSTR + CPY;
  const unsigned long long wa = (unsigned long long)ws + hb + 2048;
  const unsigned near = NUMA ? ((((unsigned)(wa >> 13) ^ (unsigned)(wa >> 21) ^ ((unsigned)__builtin_amdgcn_s_getreg(20 | (3 << 11)) >> 2)) & 1u) << 13) : 0u;
  unsigned ca = hb + near, cb = hb + (near ^ 8192u), mine = hb + split * REC;
  unsigned vo8 = lane * 8u, vo8h = vo8 + 4 * REC, mlo = (c & 7) * REC + 512u + g * 8u, lo8 = lane * 8u;
  unsigned ato = lane == 0 ? head * HSTR + 128u : WS_BYTES, mo = lane < 16 ? vo8 + 512u : WS_BYTES;
  unsigned kbml = keepml >> 24;
  mlo = oth ? mlo : WS_BYTES;
  int old = 1;
  unsigned ks[SPLITS];
#pragma unroll
  for (int s = 0; s < SPLITS; ++s) { ks[s] = s == split ? 0u : 0xffffff00u; asm volatile("" : "+s"(ks[s])); }
  __hip_bfloat16* op = out + head * D + d0;
  asm volatile("" : "+v"(fw), "+v"(lx), "+v"(mx), "+v"(gml), "+v"(keepml), "+v"(vo8), "+v"(vo8h), "+v"(mlo), "+v"(ato), "+v"(mo), "+v"(tg), "+v"(kbml), "+v"(old), "+v"(op), "+v"(lo8));
  asm volatile("" : "+s"(ca), "+s"(cb), "+s"(mine));
  __builtin_amdgcn_sched_barrier(0);
  f32x4 acc = {0.f, 0.f, 0.f, 0.f};
#pragma unroll
  for (int t = 0; t < NT; ++t)
    acc = __builtin_amdgcn_mfma_f32_16x16x32_bf16(__builtin_bit_cast(bf16x8, v[t]), __builtin_bit_cast(bf16x8, b[t]), acc, 0, 0, 0);
#pragma unroll
  for (int r = 0; r < 4; ++r) acc[r] += dpp<0x128>(acc[r]);
  f32x2 o2 = {c < 8 ? acc[0] : acc[2], c < 8 ? acc[1] : acc[3]};
  o2 *= fw;
  *reinterpret_cast<f32x2*>(lds + wave * 128 + lane * 2) = o2;
  if (AMP == 6) RT(t3);
  __syncthreads();
  if (wave != 0) return;
  DB1();
  u32x2 ga[SPLITS], gb[SPLITS], ma, mb;
#define SWEEP(G, MLV, BASE) do { MLV = __builtin_amdgcn_raw_buffer_load_b64(rw, mlo, BASE, 16); \
    _Pragma("unroll") for (int s = 0; s < SPLITS; ++s) G[s] = __builtin_amdgcn_raw_buffer_load_b64(rw, (s < 4 ? vo8 : vo8h) + (s & 3) * REC, (BASE) + (~ks[s] & 0x40000000u), 16); } while (0)
  old = __builtin_amdgcn_raw_ptr_buffer_atomic_add_i32(old, rw, ato, 0, 0);
  __builtin_amdgcn_sched_barrier(0);
  f32x2 ow[WAVES];
#pragma unroll
  for (int w = 0; w < WAVES; ++w) ow[w] = *reinterpret_cast<const f32x2*>(reinterpret_cast<const char*>(lds) + lo8 + w * 512);
#pragma unroll
  for (int st = 1; st < WAVES; st *= 2)
#pragma unroll
    for (int w = 0; w < WAVES; w += 2 * st) ow[w] += ow[w + st];
  unsigned oc0 = __float_as_uint(ow[0][0]) & 0xffffff00u, oc1 = __float_as_uint(ow[0][1]) & 0xffffff00u;
  {
    const u32x2 gv = {oc0 | tg, oc1 | tg};
    __builtin_amdgcn_raw_buffer_store_b64(gv, rw, vo8, mine, 16);
    __builtin_amdgcn_raw_buffer_store_b64(gv, rw, vo8, mine + 8192u, 16);
    __builtin_amdgcn_raw_buffer_store_b64(gml, rw, mo, mine, 16);
    __builtin_amdgcn_raw_buffer_store_b64(gml, rw, mo, mine + 8192u, 16);
  }
  DBS(dbp);
  __builtin_amdgcn_sched_barrier(0);
  if (__builtin_amdgcn_readfirstlane(old) != SPLITS - 1) { DBEND(0u, 0u); return; }
  SWEEP(ga, ma, ca);
  __builtin_amdgcn_sched_barrier(0);
  f32x2 o;
  unsigned bad;
#define BFI(M, X, Y) (((X) & (M)) | ((Y) & ~(M)))
#define ACC(G, S) { asm volatile("" : "+v"(G[S])); const float w_ = dpp<0x150 + S>(wc); \
    bad |= ((G[S][0] ^ tg) | (G[S][1] ^ tg)) & (ks[S] >> 24); \
    const f32x2 x_ = {__uint_as_float(BFI(ks[S], G[S][0], oc0)), __uint_as_float(BFI(ks[S], G[S][1], oc1))}; \
    o = __builtin_elementwise_fma(f32x2{w_, w_}, x_, o); asm volatile("" : "+v"(o), "+v"(bad)); }
#define FINISH(G, MLV) do { asm volatile("" : "+v"(MLV)); \
    bad = ((MLV[0] ^ tg) | (MLV[1] ^ tg)) & kbml; \
    const float mc = __uint_as_float(BFI(keepml, MLV[0], __float_as_uint(mx))); \
    const float lc = __uint_as_float(BFI(keepml, MLV[1], __float_as_uint(lx))); \
    float wc = __builtin_amdgcn_exp2f(mc - row_max16(mc)); \
    wc *= __builtin_amdgcn_rcpf(row_sum16(wc * lc)); \
    const float wt = dpp<0x158>(wc); \
    o = f32x2{wt * vtf0, wt * vtf1}; \
    asm volatile("" : "+v"(o), "+v"(bad), "+v"(wc)); \
    ACC(G, 0) ACC(G, 1) ACC(G, 2) ACC(G, 3) ACC(G, 4) ACC(G, 5) ACC(G, 6) ACC(G, 7) } while (0)
  unsigned nsw = 1;
  FINISH(ga, ma);
  DBS(dba);
  if (!__all(bad == 0)) {
    for (unsigned spins = 0; !__all(bad == 0) && spins < (1u << 20); ++spins) {
      asm volatile("" ::: "memory");
      SWEEP(gb, mb, cb); ++nsw;
      FINISH(gb, mb);
    }
  }
  DBS(dbb);
  op[0] = __float2bfloat16(o[0]);
  op[8] = __float2bfloat16(o[1]);
  __builtin_amdgcn_sched_barrier(0);
  if (lane == 0) __builtin_amdgcn_raw_buffer_store_b32(tag, rw, 0, head * HSTR, 16);
  DBEND(nsw, 1u);
  if (AMP >= 8) {
    unsigned long long z = __hip_atomic_load((gu64*)(ws + (4u << 20) + (head * 8 + (lane & 7)) * 128), RLX_AGENT);
    unsigned e = (unsigned)(z >> 4), x = (unsigned)z & 7u;
    unsigned ref = __builtin_amdgcn_readlane(e, 0);
    int rel = (int)(e - ref);
    int mn = 1 << 30, mxv = -(1 << 30), sel = 0, nlate = 0;
    for (int i = 0; i < 8; ++i) { int r = __builtin_amdgcn_readlane(rel, i); mn = r < mn ? r : mn; mxv = r > mxv ? r : mxv; }
    for (int i = 0; i < 8; ++i) { int r = __builtin_amdgcn_readlane(rel, i); unsigned xi = __builtin_amdgcn_readlane(x, i); if (xi == XSEL) sel = r - mn; if (r - mn > 200) ++nlate; }
    RT(te);
    long long dl = AMP == 8 ? mxv - mn : AMP == 9 ? sel : AMP == 10 ? nlate * 100 : AMP == 11 ? (long long)(int)((unsigned)te - ref) - mn : (long long)__builtin_amdgcn_readlane(x, 0) * 100;
    if (dl < 0) dl = 0; if (dl > 3000) dl = 3000;
    unsigned long long tgt = te + dl * AMPK, now; do { RT(now); } while (now < tgt);
  }
}

extern "C" void kernel_launch(void* const* d_in, const int* in_sizes, int n_in, void* d_out, int out_size, void* d_ws,
                              size_t ws_size, hipStream_t stream) {
  attn_fused<<<H * SPLITS, WAVES * 64, 0, stream>>>(d_in[1], d_in[2], (const u32x4*)d_in[0], (unsigned char*)d_ws,
                                                    (const u32x4*)d_in[3], (const unsigned short*)d_in[4],
                                                    (const unsigned short*)d_in[5], (__hip_bfloat16*)d_out);
}
```

```cpp
#define AUX 2 PAT=0 WAVES=8 PRE=4 NUMA=0 KILL=1 HSTRV=16384 EARLYV=2
#define AMP 4
#define AMPK 10
#include <hip/hip_runtime.h>
#include <hip/hip_bf16.h>
#include <stdint.h>

#define WAVES 8
#define PAT 1
#define HMAP 0
#define PRE 4
#define NUMA 1
#define HSTRV 20480
#define EARLYV 0
#define KBAR 0
#define PRIO 0
#define KILL 0
#define SAUX 16

constexpr int H = 32, C = 4096, D = 128, SPLITS = 8;
constexpr int NT = C / SPLITS / WAVES / 4;
constexpr float SCALE = 0.088388347648318440f, LOG2E = 1.4426950408889634f;
constexpr float C2 = SCALE * LOG2E;

using bf16x2 = __attribute__((ext_vector_type(2))) __bf16;
using bf16x8 = __attribute__((ext_vector_type(8))) __bf16;
using u32x4 = __attribute__((ext_vector_type(4))) unsigned;
using f32x4 = __attribute__((ext_vector_type(4))) float;
using u32x2 = __attribute__((ext_vector_type(2))) unsigned;
typedef __attribute__((address_space(1))) unsigned long long gu64;
using f32x2 = __attribute__((ext_vector_type(2))) float;
#define GAS __attribute__((address_space(1)))
typedef GAS unsigned gu32;
#define RLX_AGENT __ATOMIC_RELAXED, __HIP_MEMORY_SCOPE_AGENT

__device__ __forceinline__ float dot2(unsigned k, unsigned q, float acc) {
  return __builtin_amdgcn_fdot2_f32_bf16(__builtin_bit_cast(bf16x2, k), __builtin_bit_cast(bf16x2, q), acc, false);
}
template <int CTRL> __device__ __forceinline__ float dpp(float x) {
  return __builtin_bit_cast(float, __builtin_amdgcn_mov_dpp(__builtin_bit_cast(int, x), CTRL, 0xf, 0xf, true));
}
__device__ __forceinline__ float row_sum16(float x) {
  x += dpp<0x128>(x); x += dpp<0x124>(x); x += dpp<0x122>(x); x += dpp<0x121>(x);
  return x;
}
__device__ __forceinline__ float row_max16(float x) {
  asm volatile("s_nop 1\n\tv_max_f32_dpp %0, %0, %0 row_ror:8 row_mask:0xf bank_mask:0xf\n\ts_nop 1\n\t"
               "v_max_f32_dpp %0, %0, %0 row_ror:4 row_mask:0xf bank_mask:0xf\n\ts_nop 1\n\t"
               "v_max_f32_dpp %0, %0, %0 row_ror:2 row_mask:0xf bank_mask:0xf\n\ts_nop 1\n\t"
               "v_max_f32_dpp %0, %0, %0 row_ror:1 row_mask:0xf bank_mask:0xf" : "+v"(x));
  return x;
}
__device__ __forceinline__ float xrow_max(float x) {
  auto s = __builtin_amdgcn_permlane16_swap(__float_as_uint(x), __float_as_uint(x), false, false);
  x = fmaxf(__uint_as_float(s[0]), __uint_as_float(s[1]));
  auto t = __builtin_amdgcn_permlane32_swap(__float_as_uint(x), __float_as_uint(x), false, false);
  return fmaxf(__uint_as_float(t[0]), __uint_as_float(t[1]));
}
__device__ __forceinline__ float xrow_sum(float x) {
  auto s = __builtin_amdgcn_permlane16_swap(__float_as_uint(x), __float_as_uint(x), false, false);
  x = __uint_as_float(s[0]) + __uint_as_float(s[1]);
  auto t = __builtin_amdgcn_permlane32_swap(__float_as_uint(x), __float_as_uint(x), false, false);
  return __uint_as_float(t[0]) + __uint_as_float(t[1]);
}
__device__ __forceinline__ float bf2f(unsigned short h) { return __uint_as_float((unsigned)h << 16); }

constexpr unsigned HSTR = 256, RSTR = HSTRV, CPY = 16384, REC = 640, WS_BYTES = CPY + H * RSTR;

#define AMPEND() do { if (AMP) { RT(te); const unsigned long long dl = AMP == 1 ? te - ta : AMP == 2 ? te - t5 : AMP == 3 ? t5 - t4 : AMP == 4 ? tb - ta : AMP == 5 ? t4 - ta : AMP == 6 ? ti - tb : 0; \
  unsigned long long tgt = te + dl * AMPK, now; do { RT(now); } while (now < tgt); } } while (0)
#define RT(x) do { __builtin_amdgcn_sched_barrier(0); asm volatile("s_memrealtime %0\n\ts_waitcnt lgkmcnt(0)" : "=s"(x) :: "memory"); __builtin_amdgcn_sched_barrier(0); } while (0)
#define DB0()
#define DB1()
#define DBS(x)
#define DBEND(sw, last)

__global__ void __launch_bounds__(WAVES * 64) attn_fused(const void* __restrict__ K, const void* __restrict__ V,
                                                         const u32x4* __restrict__ q, unsigned char* ws,
                                                         const u32x4* __restrict__ kt, const unsigned short* __restrict__ vt,
                                                         const unsigned short* __restrict__ mask, __hip_bfloat16* __restrict__ out) {
  __shared__ __attribute__((aligned(16))) float lds[WAVES * 128 + 2 * WAVES];
  unsigned long long ta = 0, tb = 0, ti = 0, t3 = 0, t4 = 0, t5 = 0, te = 0;
  if (AMP) RT(ta);
  const int wg = blockIdx.x;
  const int head = HMAP == 0 ? (wg >> 3) : HMAP == 1 ? (wg & 31) : HMAP == 2 ? ((wg & 7) * 4 + ((wg >> 3) & 3)) : (((wg & 3) << 3) | ((wg >> 3) & 7)), split = HMAP == 0 ? (wg & 7) : HMAP <= 2 ? (wg >> 5) : (((wg >> 2) & 1) | ((wg >> 6) << 1));
  const int tid = threadIdx.x, lane = tid & 63, wave = __builtin_amdgcn_readfirstlane(tid >> 6);
  const int c = lane & 15, g = lane >> 4;
  DB0();
  if (PRIO) __builtin_amdgcn_s_setprio(3);
  const auto rk = __builtin_amdgcn_make_buffer_rsrc((void*)K, 0, H * C * D * 2, 0x00020000);
  const auto rv = __builtin_amdgcn_make_buffer_rsrc((void*)V, 0, H * C * D * 2, 0x00020000);
  constexpr int SW = PAT == 0 ? NT : PAT == 1 ? 1 : SPLITS, ST = PAT == 0 ? 1 : PAT == 1 ? WAVES : SPLITS * WAVES;
  const unsigned so = (unsigned)(head * 1024 + (PAT == 2 ? split : split * 128) + wave * SW) * 1024u;
  const unsigned vo = lane * 16u;
  u32x4 qv;
#define QLOAD() qv = q[head * 16 + c]
  const int d0 = 32 * g + 16 * (c >> 3) + (c & 7);
  const unsigned nw = wave == 0 ? WS_BYTES : 0u, nt_ = wave == 0 ? H * D * 2 + 16 : 0u;
  const auto rw = __builtin_amdgcn_make_buffer_rsrc((void*)ws, 0, nw, 0x00020000);
  const auto rkt = __builtin_amdgcn_make_buffer_rsrc((void*)kt, 0, nt_, 0x00020000);
  const auto rvt = __builtin_amdgcn_make_buffer_rsrc((void*)vt, 0, nt_, 0x00020000);
  const auto rmk = __builtin_amdgcn_make_buffer_rsrc((void*)mask, 0, nt_ ? 16u : 0u, 0x00020000);
  unsigned epoch; u32x2 w0; u32x4 ktv; unsigned short vt0, vt1, mk;
#define SMALL_LOADS() do { epoch = __builtin_amdgcn_raw_buffer_load_b32(rw, 0, head * HSTR, 16); \
    w0 = __builtin_amdgcn_raw_buffer_load_b64(rw, 128, head * HSTR, 16); \
    ktv = __builtin_amdgcn_raw_buffer_load_b128(rkt, c * 16, head * 256, 0); \
    vt0 = __builtin_amdgcn_raw_buffer_load_b16(rvt, d0 * 2, head * 256, 0); \
    vt1 = __builtin_amdgcn_raw_buffer_load_b16(rvt, d0 * 2 + 16, head * 256, 0); \
    mk = __builtin_amdgcn_raw_buffer_load_b16(rmk, 0, 0, 0); } while (0)
  u32x4 k[NT], v[NT];
  asm volatile("" :: "s"(out));
  if (AMP == 4 || AMP == 6) { asm volatile("" :: "s"(K), "s"(V), "s"(q)); RT(tb); }
#pragma unroll
  for (int t = 0; t < NT; ++t) {
    k[t] = __builtin_amdgcn_raw_buffer_load_b128(rk, vo, so + t * ST * 1024, AUX);
    if (t == 0 && AMP == 6) { asm volatile("" : "+v"(k[0])); RT(ti); }
    if (t == 0 && EARLYV) { v[0] = __builtin_amdgcn_raw_buffer_load_b128(rv, vo, so, AUX); if (EARLYV == 2) { QLOAD(); SMALL_LOADS(); } __builtin_amdgcn_sched_barrier(0); }
  }
  if (EARLYV != 2) { QLOAD(); SMALL_LOADS(); }
  if (KBAR) { __builtin_amdgcn_sched_barrier(0); __builtin_amdgcn_s_barrier(); __builtin_amdgcn_sched_barrier(0); }
#pragma unroll
  for (int t = EARLYV ? 1 : 0; t < PRE; ++t) v[t] = __builtin_amdgcn_raw_buffer_load_b128(rv, vo, so + t * ST * 1024, AUX);
  __builtin_amdgcn_sched_barrier(0);
  float x[NT];
#pragma unroll
  for (int t = 0; t < NT; ++t) {
    float d = dot2(k[t][0], qv[0], 0.f);
    d = dot2(k[t][1], qv[1], d); d = dot2(k[t][2], qv[2], d); d = dot2(k[t][3], qv[3], d);
    x[t] = row_sum16(d) * C2;
  }
  float m = x[0];
#pragma unroll
  for (int t = 1; t < NT; ++t) m = fmaxf(m, x[t]);
  m = xrow_max(m);
  const float M = m;
  float* lm = lds + WAVES * 128;
  const int n = c & 7;
  const unsigned lomask = (c >= 8) ? 0xffff0000u : 0u;
  unsigned sel[4];
#pragma unroll
  for (int r = 0; r < 4; ++r) sel[r] = ((n >> 1) == r) ? ((n & 1) ? 0x03020c0cu : 0x0c0c0302u) : 0x0c0c0c0cu;
  float l = 0.f;
  u32x4 b[NT];
#pragma unroll
  for (int t = 0; t < NT; ++t) {
    const float p = __builtin_amdgcn_exp2f(x[t] - M);
    l += p;
    const unsigned pu = __float_as_uint(p);
    const unsigned s = __float_as_uint(p - __uint_as_float(pu & lomask));
#pragma unroll
    for (int r = 0; r < 4; ++r) b[t][r] = __builtin_amdgcn_perm(s, s, sel[r]);
  }
  l = xrow_sum(l);
  if (lane == 0) *reinterpret_cast<f32x2*>(lm + 2 * wave) = f32x2{M, l};
  float mx, vtf0 = bf2f(vt0), vtf1 = bf2f(vt1);
  {
    float d = dot2(ktv[0], qv[0], 0.f);
    d = dot2(ktv[1], qv[1], d); d = dot2(ktv[2], qv[2], d); d = dot2(ktv[3], qv[3], d);
    const float st = (row_sum16(d) * SCALE + (1.0f - bf2f(mk)) * -1000000000.0f) * LOG2E;
    mx = c == 8 ? st : -INFINITY;
  }
  unsigned tag = epoch + 1u;
  asm volatile("" : "+v"(mx), "+v"(vtf0), "+v"(vtf1), "+v"(tag));
  gu64* cnt = (gu64*)(ws + head * HSTR + 128);
  if (!(KILL & 4) && tid == 0 && w0[1] != tag) {
    unsigned long long e = ((unsigned long long)w0[1] << 32) | w0[0];
    __hip_atomic_compare_exchange_strong(cnt, &e, (unsigned long long)tag << 32, __ATOMIC_RELAXED, __ATOMIC_RELAXED, __HIP_MEMORY_SCOPE_AGENT);
  }
  __builtin_amdgcn_sched_barrier(0);
  if (PRIO) __builtin_amdgcn_s_setprio(0);
#pragma unroll
  for (int t = PRE; t < NT; ++t) v[t] = __builtin_amdgcn_raw_buffer_load_b128(rv, vo, so + t * ST * 1024, AUX);
  __builtin_amdgcn_sched_barrier(0);
  __syncthreads();
  const f32x2 ml = *reinterpret_cast<const f32x2*>(lm + 2 * (c & (WAVES - 1)));
  const float MW = __uint_as_float(__float_as_uint(row_max16(ml[0])) & 0xffffff00u);
  float L = row_sum16(__builtin_amdgcn_exp2f(ml[0] - MW) * ml[1]) * (WAVES / 16.0f);
  float fw = __builtin_amdgcn_exp2f(M - MW);
  L = __uint_as_float(__float_as_uint(L) & 0xffffff00u);
  float lx = c == split ? L : 1.0f;
  mx = c == split ? MW : mx;
  unsigned tg = tag & 0xffu;
  u32x2 gml = {__float_as_uint(MW) | tg, __float_as_uint(L) | tg};
  const bool oth = c < 8 && c != split;
  unsigned keepml = oth ? 0xffffff00u : 0u;
  const unsigned hb = head * RSTR + CPY;
  const unsigned long long wa = (unsigned long long)ws + hb + 2048;
  const unsigned near = NUMA ? ((((unsigned)(wa >> 13) ^ (unsigned)(wa >> 21) ^ ((unsigned)__builtin_amdgcn_s_getreg(20 | (3 << 11)) >> 2)) & 1u) << 13) : 0u;
  unsigned ca = hb + near, cb = hb + (near ^ 8192u), mine = hb + split * REC;
  unsigned vo8 = lane * 8u, vo8h = vo8 + 4 * REC, mlo = (c & 7) * REC + 512u + g * 8u, lo8 = lane * 8u;
  unsigned ato = lane == 0 ? head * HSTR + 128u : WS_BYTES, mo = lane < 16 ? vo8 + 512u : WS_BYTES;
  unsigned kbml = keepml >> 24;
  mlo = oth ? mlo : WS_BYTES;
  int old = 1;
  unsigned ks[SPLITS];
#pragma unroll
  for (int s = 0; s < SPLITS; ++s) { ks[s] = s == split ? 0u : 0xffffff00u; asm volatile("" : "+s"(ks[s])); }
  __hip_bfloat16* op = out + head * D + d0;
  asm volatile("" : "+v"(fw), "+v"(lx), "+v"(mx), "+v"(gml), "+v"(keepml), "+v"(vo8), "+v"(vo8h), "+v"(mlo), "+v"(ato), "+v"(mo), "+v"(tg), "+v"(kbml), "+v"(old), "+v"(op), "+v"(lo8));
  asm volatile("" : "+s"(ca), "+s"(cb), "+s"(mine));
  __builtin_amdgcn_sched_barrier(0);
  f32x4 acc = {0.f, 0.f, 0.f, 0.f};
#pragma unroll
  for (int t = 0; t < NT; ++t)
    acc = __builtin_amdgcn_mfma_f32_16x16x32_bf16(__builtin_bit_cast(bf16x8, v[t]), __builtin_bit_cast(bf16x8, b[t]), acc, 0, 0, 0);
#pragma unroll
  for (int r = 0; r < 4; ++r) acc[r] += dpp<0x128>(acc[r]);
  f32x2 o2 = {c < 8 ? acc[0] : acc[2], c < 8 ? acc[1] : acc[3]};
  o2 *= fw;
  *reinterpret_cast<f32x2*>(lds + wave * 128 + lane * 2) = o2;
  __syncthreads();
  if (wave != 0) return;
  DB1();
  u32x2 ga[SPLITS], gb[SPLITS], ma, mb;
#define SWEEP(G, MLV, BASE) do { MLV = __builtin_amdgcn_raw_buffer_load_b64(rw, mlo, BASE, 16); \
    _Pragma("unroll") for (int s = 0; s < SPLITS; ++s) G[s] = __builtin_amdgcn_raw_buffer_load_b64(rw, (s < 4 ? vo8 : vo8h) + (s & 3) * REC, (BASE) + (~ks[s] & 0x40000000u), 16); } while (0)
  old = (KILL & 4) ? 7 : __builtin_amdgcn_raw_ptr_buffer_atomic_add_i32(old, rw, ato, 0, 0);
  __builtin_amdgcn_sched_barrier(0);
  f32x2 ow[WAVES];
#pragma unroll
  for (int w = 0; w < WAVES; ++w) ow[w] = *reinterpret_cast<const f32x2*>(reinterpret_cast<const char*>(lds) + lo8 + w * 512);
#pragma unroll
  for (int st = 1; st < WAVES; st *= 2)
#pragma unroll
    for (int w = 0; w < WAVES; w += 2 * st) ow[w] += ow[w + st];
  unsigned oc0 = __float_as_uint(ow[0][0]) & 0xffffff00u, oc1 = __float_as_uint(ow[0][1]) & 0xffffff00u;
  {
    const u32x2 gv = {oc0 | tg, oc1 | tg};
    if (!(KILL & 2)) __builtin_amdgcn_raw_buffer_store_b64(gv, rw, vo8, mine, SAUX);
    if (!(KILL & 1)) __builtin_amdgcn_raw_buffer_store_b64(gv, rw, vo8, mine + 8192u, SAUX);
    if (!(KILL & 2)) __builtin_amdgcn_raw_buffer_store_b64(gml, rw, mo, mine, SAUX);
    if (!(KILL & 1)) __builtin_amdgcn_raw_buffer_store_b64(gml, rw, mo, mine + 8192u, SAUX);
  }
  DBS(dbp);
  __builtin_amdgcn_sched_barrier(0);
  if (__builtin_amdgcn_readfirstlane(old) != SPLITS - 1) { DBEND(0u, 0u); AMPEND(); return; }
  SWEEP(ga, ma, ca);
  __builtin_amdgcn_sched_barrier(0);
  f32x2 o;
  unsigned bad;
#define BFI(M, X, Y) (((X) & (M)) | ((Y) & ~(M)))
#define ACC(G, S) { asm volatile("" : "+v"(G[S])); const float w_ = dpp<0x150 + S>(wc); \
    bad |= ((G[S][0] ^ tg) | (G[S][1] ^ tg)) & (ks[S] >> 24); \
    const f32x2 x_ = {__uint_as_float(BFI(ks[S], G[S][0], oc0)), __uint_as_float(BFI(ks[S], G[S][1], oc1))}; \
    o = __builtin_elementwise_fma(f32x2{w_, w_}, x_, o); asm volatile("" : "+v"(o), "+v"(bad)); }
#define FINISH(G, MLV) do { asm volatile("" : "+v"(MLV)); \
    bad = ((MLV[0] ^ tg) | (MLV[1] ^ tg)) & kbml; \
    const float mc = __uint_as_float(BFI(keepml, MLV[0], __float_as_uint(mx))); \
    const float lc = __uint_as_float(BFI(keepml, MLV[1], __float_as_uint(lx))); \
    float wc = __builtin_amdgcn_exp2f(mc - row_max16(mc)); \
    wc *= __builtin_amdgcn_rcpf(row_sum16(wc * lc)); \
    const float wt = dpp<0x158>(wc); \
    o = f32x2{wt * vtf0, wt * vtf1}; \
    asm volatile("" : "+v"(o), "+v"(bad), "+v"(wc)); \
    ACC(G, 0) ACC(G, 1) ACC(G, 2) ACC(G, 3) ACC(G, 4) ACC(G, 5) ACC(G, 6) ACC(G, 7) } while (0)
  unsigned nsw = 1;
  FINISH(ga, ma);
  DBS(dba);
  if (!(KILL & 2) && !__all(bad == 0)) {
    for (unsigned spins = 0; !__all(bad == 0) && spins < (1u << 20); ++spins) {
      asm volatile("" ::: "memory");
      SWEEP(gb, mb, (KILL & 1) ? ca : cb); ++nsw;
      FINISH(gb, mb);
    }
  }
  DBS(dbb);
  op[0] = __float2bfloat16(o[0]);
  op[8] = __float2bfloat16(o[1]);
  __builtin_amdgcn_sched_barrier(0);
  if (lane == 0) __builtin_amdgcn_raw_buffer_store_b32(tag, rw, 0, head * HSTR, 16);
  DBEND(nsw, 1u);
  AMPEND();
}

extern "C" void kernel_launch(void* const* d_in, const int* in_sizes, int n_in, void* d_out, int out_size, void* d_ws,
                              size_t ws_size, hipStream_t stream) {
  attn_fused<<<H * SPLITS, WAVES * 64, 0, stream>>>(d_in[1], d_in[2], (const u32x4*)d_in[0], (unsigned char*)d_ws,
                                                    (const u32x4*)d_in[3], (const unsigned short*)d_in[4],
                                                    (const unsigned short*)d_in[5], (__hip_bfloat16*)d_out);
}
```

```cpp
#define AUX 2 PAT=0 WAVES=8 PRE=4 NUMA=0 KILL=1 HSTRV=16384 EARLYV=2 AMP=4 AMPK=10
#define KM 0
#include <hip/hip_runtime.h>
#include <hip/hip_bf16.h>
#include <stdint.h>

#define WAVES 8
#define PAT 1
#define HMAP 0
#define PRE 4
#define NUMA 1
#define HSTRV 20480
#define EARLYV 0
#define KBAR 0
#define PRIO 0
#define KILL 0
#define SAUX 16
#define AMP 0
#define AMPK 1

constexpr int H = 32, C = 4096, D = 128, SPLITS = 8;
constexpr int NT = C / SPLITS / WAVES / 4;
constexpr float SCALE = 0.088388347648318440f, LOG2E = 1.4426950408889634f;
constexpr float C2 = SCALE * LOG2E;

using bf16x2 = __attribute__((ext_vector_type(2))) __bf16;
using bf16x8 = __attribute__((ext_vector_type(8))) __bf16;
using u32x4 = __attribute__((ext_vector_type(4))) unsigned;
using f32x4 = __attribute__((ext_vector_type(4))) float;
using u32x2 = __attribute__((ext_vector_type(2))) unsigned;
typedef __attribute__((address_space(1))) unsigned long long gu64;
using f32x2 = __attribute__((ext_vector_type(2))) float;
#define GAS __attribute__((address_space(1)))
typedef GAS unsigned gu32;
#define RLX_AGENT __ATOMIC_RELAXED, __HIP_MEMORY_SCOPE_AGENT

__device__ __forceinline__ float dot2(unsigned k, unsigned q, float acc) {
  return __builtin_amdgcn_fdot2_f32_bf16(__builtin_bit_cast(bf16x2, k), __builtin_bit_cast(bf16x2, q), acc, false);
}
template <int CTRL> __device__ __forceinline__ float dpp(float x) {
  return __builtin_bit_cast(float, __builtin_amdgcn_mov_dpp(__builtin_bit_cast(int, x), CTRL, 0xf, 0xf, true));
}
__device__ __forceinline__ float row_sum16(float x) {
  x += dpp<0x128>(x); x += dpp<0x124>(x); x += dpp<0x122>(x); x += dpp<0x121>(x);
  return x;
}
__device__ __forceinline__ float row_max16(float x) {
  asm volatile("s_nop 1\n\tv_max_f32_dpp %0, %0, %0 row_ror:8 row_mask:0xf bank_mask:0xf\n\ts_nop 1\n\t"
               "v_max_f32_dpp %0, %0, %0 row_ror:4 row_mask:0xf bank_mask:0xf\n\ts_nop 1\n\t"
               "v_max_f32_dpp %0, %0, %0 row_ror:2 row_mask:0xf bank_mask:0xf\n\ts_nop 1\n\t"
               "v_max_f32_dpp %0, %0, %0 row_ror:1 row_mask:0xf bank_mask:0xf" : "+v"(x));
  return x;
}
__device__ __forceinline__ float xrow_max(float x) {
  auto s = __builtin_amdgcn_permlane16_swap(__float_as_uint(x), __float_as_uint(x), false, false);
  x = fmaxf(__uint_as_float(s[0]), __uint_as_float(s[1]));
  auto t = __builtin_amdgcn_permlane32_swap(__float_as_uint(x), __float_as_uint(x), false, false);
  return fmaxf(__uint_as_float(t[0]), __uint_as_float(t[1]));
}
__device__ __forceinline__ float xrow_sum(float x) {
  auto s = __builtin_amdgcn_permlane16_swap(__float_as_uint(x), __float_as_uint(x), false, false);
  x = __uint_as_float(s[0]) + __uint_as_float(s[1]);
  auto t = __builtin_amdgcn_permlane32_swap(__float_as_uint(x), __float_as_uint(x), false, false);
  return __uint_as_float(t[0]) + __uint_as_float(t[1]);
}
__device__ __forceinline__ float bf2f(unsigned short h) { return __uint_as_float((unsigned)h << 16); }

constexpr unsigned HSTR = 256, RSTR = HSTRV, CPY = 16384, REC = 640, WS_BYTES = CPY + H * RSTR;

#define AMPEND() do { if (AMP) { RT(te); const unsigned long long dl = AMP == 1 ? te - ta : AMP == 2 ? te - t5 : AMP == 3 ? t5 - t4 : AMP == 4 ? tb - ta : AMP == 5 ? t4 - ta : AMP == 6 ? t4 - t3 : 0; \
  unsigned long long tgt = te + dl * AMPK, now; do { RT(now); } while (now < tgt); } } while (0)
#define RT(x) do { __builtin_amdgcn_sched_barrier(0); asm volatile("s_memrealtime %0\n\ts_waitcnt lgkmcnt(0)" : "=s"(x) :: "memory"); __builtin_amdgcn_sched_barrier(0); } while (0)
#define DB0()
#define DB1()
#define DBS(x)
#define DBEND(sw, last)

__global__ void __launch_bounds__(WAVES * 64) attn_fused(const void* K_, const void* V_, const u32x4* q_, unsigned char* ws_, const u32x4* kt_, const unsigned short* vt_,
                                                         const unsigned short* mask_, __hip_bfloat16* out_) {
  __shared__ __attribute__((aligned(16))) float lds[WAVES * 128 + 2 * WAVES];
  unsigned long long ta = 0, tb = 0, ti = 0, t3 = 0, t4 = 0, t5 = 0, te = 0;
  if (AMP) RT(ta);
  typedef unsigned long long u64x4 __attribute__((ext_vector_type(4)));
  typedef unsigned long long u64x8 __attribute__((ext_vector_type(8)));
  const void* K; const void* V; const u32x4* q; unsigned char* ws; const u32x4* kt; const unsigned short* vt; const unsigned short* mask; __hip_bfloat16* out;
  {
    const unsigned long long ka = (unsigned long long)__builtin_amdgcn_kernarg_segment_ptr();
    u64x8 a;
    if (KM == 0) {
      u64x4 lo, hi;
      asm volatile("s_load_dwordx8 %0, %2, 0x0\n\ts_load_dwordx8 %1, %2, 0x20\n\ts_waitcnt lgkmcnt(0)" : "=&s"(lo), "=&s"(hi) : "s"(ka) : "memory");
      a = u64x8{lo[0], lo[1], lo[2], lo[3], hi[0], hi[1], hi[2], hi[3]};
    } else if (KM == 1) {
      asm volatile("s_load_dwordx16 %0, %1, 0x0\n\ts_waitcnt lgkmcnt(0)" : "=&s"(a) : "s"(ka) : "memory");
    } else {
      __shared__ unsigned sh[16];
      if (__builtin_amdgcn_readfirstlane(threadIdx.x) < 64) {
        asm volatile("s_load_dwordx16 %0, %1, 0x0\n\ts_waitcnt lgkmcnt(0)" : "=&s"(a) : "s"(ka) : "memory");
        if (threadIdx.x == 0) for (int i = 0; i < 8; ++i) { sh[2 * i] = (unsigned)a[i]; sh[2 * i + 1] = (unsigned)(a[i] >> 32); }
      }
      __syncthreads();
      const unsigned z = sh[threadIdx.x & 15];
      for (int i = 0; i < 8; ++i) a[i] = ((unsigned long long)__builtin_amdgcn_readlane(z, 2 * i + 1) << 32) | __builtin_amdgcn_readlane(z, 2 * i);
    }
    K = (const void*)a[0]; V = (const void*)a[1]; q = (const u32x4*)a[2]; ws = (unsigned char*)a[3]; kt = (const u32x4*)a[4]; vt = (const unsigned short*)a[5]; mask = (const unsigned short*)a[6]; out = (__hip_bfloat16*)a[7];
  }
  const int wg = blockIdx.x;
  const int head = HMAP == 0 ? (wg >> 3) : HMAP == 1 ? (wg & 31) : HMAP == 2 ? ((wg & 7) * 4 + ((wg >> 3) & 3)) : (((wg & 3) << 3) | ((wg >> 3) & 7)), split = HMAP == 0 ? (wg & 7) : HMAP <= 2 ? (wg >> 5) : (((wg >> 2) & 1) | ((wg >> 6) << 1));
  const int tid = threadIdx.x, lane = tid & 63, wave = __builtin_amdgcn_readfirstlane(tid >> 6);
  const int c = lane & 15, g = lane >> 4;
  DB0();
  if (PRIO) __builtin_amdgcn_s_setprio(3);
  const auto rk = __builtin_amdgcn_make_buffer_rsrc((void*)K, 0, H * C * D * 2, 0x00020000);
  const auto rv = __builtin_amdgcn_make_buffer_rsrc((void*)V, 0, H * C * D * 2, 0x00020000);
  constexpr int SW = PAT == 0 ? NT : PAT == 1 ? 1 : SPLITS, ST = PAT == 0 ? 1 : PAT == 1 ? WAVES : SPLITS * WAVES;
  const unsigned so = (unsigned)(head * 1024 + (PAT == 2 ? split : split * 128) + wave * SW) * 1024u;
  const unsigned vo = lane * 16u;
  u32x4 qv;
#define QLOAD() qv = *(const GAS u32x4*)(q + head * 16 + c)
  const int d0 = 32 * g + 16 * (c >> 3) + (c & 7);
  const unsigned nw = wave == 0 ? WS_BYTES : 0u, nt_ = wave == 0 ? H * D * 2 + 16 : 0u;
  const auto rw = __builtin_amdgcn_make_buffer_rsrc((void*)ws, 0, nw, 0x00020000);
  const auto rkt = __builtin_amdgcn_make_buffer_rsrc((void*)kt, 0, nt_, 0x00020000);
  const auto rvt = __builtin_amdgcn_make_buffer_rsrc((void*)vt, 0, nt_, 0x00020000);
  const auto rmk = __builtin_amdgcn_make_buffer_rsrc((void*)mask, 0, nt_ ? 16u : 0u, 0x00020000);
  unsigned epoch; u32x2 w0; u32x4 ktv; unsigned short vt0, vt1, mk;
#define SMALL_LOADS() do { epoch = __builtin_amdgcn_raw_buffer_load_b32(rw, 0, head * HSTR, 16); \
    w0 = __builtin_amdgcn_raw_buffer_load_b64(rw, 128, head * HSTR, 16); \
    ktv = __builtin_amdgcn_raw_buffer_load_b128(rkt, c * 16, head * 256, 0); \
    vt0 = __builtin_amdgcn_raw_buffer_load_b16(rvt, d0 * 2, head * 256, 0); \
    vt1 = __builtin_amdgcn_raw_buffer_load_b16(rvt, d0 * 2 + 16, head * 256, 0); \
    mk = __builtin_amdgcn_raw_buffer_load_b16(rmk, 0, 0, 0); } while (0)
  u32x4 k[NT], v[NT];
  asm volatile("" :: "s"(out));
  if (AMP == 4) { asm volatile("" :: "s"(K), "s"(V), "s"(q)); RT(tb); }
#pragma unroll
  for (int t = 0; t < NT; ++t) {
    k[t] = __builtin_amdgcn_raw_buffer_load_b128(rk, vo, so + t * ST * 1024, AUX);
    if (t == 0 && EARLYV) { v[0] = __builtin_amdgcn_raw_buffer_load_b128(rv, vo, so, AUX); if (EARLYV == 2) { QLOAD(); SMALL_LOADS(); } __builtin_amdgcn_sched_barrier(0); }
  }
  if (EARLYV != 2) { QLOAD(); SMALL_LOADS(); }
  if (KBAR) { __builtin_amdgcn_sched_barrier(0); __builtin_amdgcn_s_barrier(); __builtin_amdgcn_sched_barrier(0); }
#pragma unroll
  for (int t = EARLYV ? 1 : 0; t < PRE; ++t) v[t] = __builtin_amdgcn_raw_buffer_load_b128(rv, vo, so + t * ST * 1024, AUX);
  __builtin_amdgcn_sched_barrier(0);
  float x[NT];
#pragma unroll
  for (int t = 0; t < NT; ++t) {
    float d = dot2(k[t][0], qv[0], 0.f);
    d = dot2(k[t][1], qv[1], d); d = dot2(k[t][2], qv[2], d); d = dot2(k[t][3], qv[3], d);
    x[t] = row_sum16(d) * C2;
  }
  float m = x[0];
#pragma unroll
  for (int t = 1; t < NT; ++t) m = fmaxf(m, x[t]);
  m = xrow_max(m);
  const float M = m;
  float* lm = lds + WAVES * 128;
  const int n = c & 7;
  const unsigned lomask = (c >= 8) ? 0xffff0000u : 0u;
  unsigned sel[4];
#pragma unroll
  for (int r = 0; r < 4; ++r) sel[r] = ((n >> 1) == r) ? ((n & 1) ? 0x03020c0cu : 0x0c0c0302u) : 0x0c0c0c0cu;
  float l = 0.f;
  u32x4 b[NT];
#pragma unroll
  for (int t = 0; t < NT; ++t) {
    const float p = __builtin_amdgcn_exp2f(x[t] - M);
    l += p;
    const unsigned pu = __float_as_uint(p);
    const unsigned s = __float_as_uint(p - __uint_as_float(pu & lomask));
#pragma unroll
    for (int r = 0; r < 4; ++r) b[t][r] = __builtin_amdgcn_perm(s, s, sel[r]);
  }
  l = xrow_sum(l);
  if (lane == 0) *reinterpret_cast<f32x2*>(lm + 2 * wave) = f32x2{M, l};
  float mx, vtf0 = bf2f(vt0), vtf1 = bf2f(vt1);
  {
    float d = dot2(ktv[0], qv[0], 0.f);
    d = dot2(ktv[1], qv[1], d); d = dot2(ktv[2], qv[2], d); d = dot2(ktv[3], qv[3], d);
    const float st = (row_sum16(d) * SCALE + (1.0f - bf2f(mk)) * -1000000000.0f) * LOG2E;
    mx = c == 8 ? st : -INFINITY;
  }
  unsigned tag = epoch + 1u;
  asm volatile("" : "+v"(mx), "+v"(vtf0), "+v"(vtf1), "+v"(tag));
  gu64* cnt = (gu64*)(ws + head * HSTR + 128);
  if (!(KILL & 4) && tid == 0 && w0[1] != tag) {
    unsigned long long e = ((unsigned long long)w0[1] << 32) | w0[0];
    __hip_atomic_compare_exchange_strong(cnt, &e, (unsigned long long)tag << 32, __ATOMIC_RELAXED, __ATOMIC_RELAXED, __HIP_MEMORY_SCOPE_AGENT);
  }
  __builtin_amdgcn_sched_barrier(0);
  if (PRIO) __builtin_amdgcn_s_setprio(0);
#pragma unroll
  for (int t = PRE; t < NT; ++t) v[t] = __builtin_amdgcn_raw_buffer_load_b128(rv, vo, so + t * ST * 1024, AUX);
  __builtin_amdgcn_sched_barrier(0);
  __syncthreads();
  const f32x2 ml = *reinterpret_cast<const f32x2*>(lm + 2 * (c & (WAVES - 1)));
  const float MW = __uint_as_float(__float_as_uint(row_max16(ml[0])) & 0xffffff00u);
  float L = row_sum16(__builtin_amdgcn_exp2f(ml[0] - MW) * ml[1]) * (WAVES / 16.0f);
  float fw = __builtin_amdgcn_exp2f(M - MW);
  L = __uint_as_float(__float_as_uint(L) & 0xffffff00u);
  float lx = c == split ? L : 1.0f;
  mx = c == split ? MW : mx;
  unsigned tg = tag & 0xffu;
  u32x2 gml = {__float_as_uint(MW) | tg, __float_as_uint(L) | tg};
  const bool oth = c < 8 && c != split;
  unsigned keepml = oth ? 0xffffff00u : 0u;
  const unsigned hb = head * RSTR + CPY;
  const unsigned long long wa = (unsigned long long)ws + hb + 2048;
  const unsigned near = NUMA ? ((((unsigned)(wa >> 13) ^ (unsigned)(wa >> 21) ^ ((unsigned)__builtin_amdgcn_s_getreg(20 | (3 << 11)) >> 2)) & 1u) << 13) : 0u;
  unsigned ca = hb + near, cb = hb + (near ^ 8192u), mine = hb + split * REC;
  unsigned vo8 = lane * 8u, vo8h = vo8 + 4 * REC, mlo = (c & 7) * REC + 512u + g * 8u, lo8 = lane * 8u;
  unsigned ato = lane == 0 ? head * HSTR + 128u : WS_BYTES, mo = lane < 16 ? vo8 + 512u : WS_BYTES;
  unsigned kbml = keepml >> 24;
  mlo = oth ? mlo : WS_BYTES;
  int old = 1;
  unsigned ks[SPLITS];
#pragma unroll
  for (int s = 0; s < SPLITS; ++s) { ks[s] = s == split ? 0u : 0xffffff00u; asm volatile("" : "+s"(ks[s])); }
  GAS unsigned short* op = (GAS unsigned short*)(out + head * D + d0);
  asm volatile("" : "+v"(fw), "+v"(lx), "+v"(mx), "+v"(gml), "+v"(keepml), "+v"(vo8), "+v"(vo8h), "+v"(mlo), "+v"(ato), "+v"(mo), "+v"(tg), "+v"(kbml), "+v"(old), "+v"(op), "+v"(lo8));
  asm volatile("" : "+s"(ca), "+s"(cb), "+s"(mine));
  __builtin_amdgcn_sched_barrier(0);
  f32x4 acc = {0.f, 0.f, 0.f, 0.f};
#pragma unroll
  for (int t = 0; t < NT; ++t)
    acc = __builtin_amdgcn_mfma_f32_16x16x32_bf16(__builtin_bit_cast(bf16x8, v[t]), __builtin_bit_cast(bf16x8, b[t]), acc, 0, 0, 0);
#pragma unroll
  for (int r = 0; r < 4; ++r) acc[r] += dpp<0x128>(acc[r]);
  f32x2 o2 = {c < 8 ? acc[0] : acc[2], c < 8 ? acc[1] : acc[3]};
  o2 *= fw;
  *reinterpret_cast<f32x2*>(lds + wave * 128 + lane * 2) = o2;
  if (AMP == 6) RT(t3);
  __syncthreads();
  if (wave != 0) return;
  DB1();
  u32x2 ga[SPLITS], gb[SPLITS], ma, mb;
#define SWEEP(G, MLV, BASE) do { MLV = __builtin_amdgcn_raw_buffer_load_b64(rw, mlo, BASE, 16); \
    _Pragma("unroll") for (int s = 0; s < SPLITS; ++s) G[s] = __builtin_amdgcn_raw_buffer_load_b64(rw, (s < 4 ? vo8 : vo8h) + (s & 3) * REC, (BASE) + (~ks[s] & 0x40000000u), 16); } while (0)
  old = (KILL & 4) ? 7 : __builtin_amdgcn_raw_ptr_buffer_atomic_add_i32(old, rw, ato, 0, 0);
  __builtin_amdgcn_sched_barrier(0);
  f32x2 ow[WAVES];
#pragma unroll
  for (int w = 0; w < WAVES; ++w) ow[w] = *reinterpret_cast<const f32x2*>(reinterpret_cast<const char*>(lds) + lo8 + w * 512);
#pragma unroll
  for (int st = 1; st < WAVES; st *= 2)
#pragma unroll
    for (int w = 0; w < WAVES; w += 2 * st) ow[w] += ow[w + st];
  unsigned oc0 = __float_as_uint(ow[0][0]) & 0xffffff00u, oc1 = __float_as_uint(ow[0][1]) & 0xffffff00u;
  {
    const u32x2 gv = {oc0 | tg, oc1 | tg};
    if (!(KILL & 2)) __builtin_amdgcn_raw_buffer_store_b64(gv, rw, vo8, mine, SAUX);
    if (!(KILL & 1)) __builtin_amdgcn_raw_buffer_store_b64(gv, rw, vo8, mine + 8192u, SAUX);
    if (!(KILL & 2)) __builtin_amdgcn_raw_buffer_store_b64(gml, rw, mo, mine, SAUX);
    if (!(KILL & 1)) __builtin_amdgcn_raw_buffer_store_b64(gml, rw, mo, mine + 8192u, SAUX);
  }
  DBS(dbp);
  __builtin_amdgcn_sched_barrier(0);
  if (__builtin_amdgcn_readfirstlane(old) != SPLITS - 1) { DBEND(0u, 0u); AMPEND(); return; }
  SWEEP(ga, ma, ca);
  __builtin_amdgcn_sched_barrier(0);
  f32x2 o;
  unsigned bad;
#define BFI(M, X, Y) (((X) & (M)) | ((Y) & ~(M)))
#define ACC(G, S) { asm volatile("" : "+v"(G[S])); const float w_ = dpp<0x150 + S>(wc); \
    bad |= ((G[S][0] ^ tg) | (G[S][1] ^ tg)) & (ks[S] >> 24); \
    const f32x2 x_ = {__uint_as_float(BFI(ks[S], G[S][0], oc0)), __uint_as_float(BFI(ks[S], G[S][1], oc1))}; \
    o = __builtin_elementwise_fma(f32x2{w_, w_}, x_, o); asm volatile("" : "+v"(o), "+v"(bad)); }
#define FINISH(G, MLV) do { asm volatile("" : "+v"(MLV)); \
    bad = ((MLV[0] ^ tg) | (MLV[1] ^ tg)) & kbml; \
    const float mc = __uint_as_float(BFI(keepml, MLV[0], __float_as_uint(mx))); \
    const float lc = __uint_as_float(BFI(keepml, MLV[1], __float_as_uint(lx))); \
    float wc = __builtin_amdgcn_exp2f(mc - row_max16(mc)); \
    wc *= __builtin_amdgcn_rcpf(row_sum16(wc * lc)); \
    const float wt = dpp<0x158>(wc); \
    o = f32x2{wt * vtf0, wt * vtf1}; \
    asm volatile("" : "+v"(o), "+v"(bad), "+v"(wc)); \
    ACC(G, 0) ACC(G, 1) ACC(G, 2) ACC(G, 3) ACC(G, 4) ACC(G, 5) ACC(G, 6) ACC(G, 7) } while (0)
  unsigned nsw = 1;
  FINISH(ga, ma);
  DBS(dba);
  if (!(KILL & 2) && !__all(bad == 0)) {
    for (unsigned spins = 0; !__all(bad == 0) && spins < (1u << 20); ++spins) {
      asm volatile("" ::: "memory");
      SWEEP(gb, mb, (KILL & 1) ? ca : cb); ++nsw;
      FINISH(gb, mb);
    }
  }
  DBS(dbb);
  op[0] = __builtin_bit_cast(unsigned short, (__bf16)o[0]);
  op[8] = __builtin_bit_cast(unsigned short, (__bf16)o[1]);
  __builtin_amdgcn_sched_barrier(0);
  if (lane == 0) __builtin_amdgcn_raw_buffer_store_b32(tag, rw, 0, head * HSTR, 16);
  DBEND(nsw, 1u);
  AMPEND();
}

extern "C" void kernel_launch(void* const* d_in, const int* in_sizes, int n_in, void* d_out, int out_size, void* d_ws,
                              size_t ws_size, hipStream_t stream) {
  attn_fused<<<H * SPLITS, WAVES * 64, 0, stream>>>(d_in[1], d_in[2], (const u32x4*)d_in[0], (unsigned char*)d_ws,
                                                    (const u32x4*)d_in[3], (const unsigned short*)d_in[4],
                                                    (const unsigned short*)d_in[5], (__hip_bfloat16*)d_out);
}
```

```cpp
#define AUX 2 PAT=0 WAVES=8 PRE=4 NUMA=0 KILL=1 HSTRV=16384 EARLYV=2
#include <hip/hip_runtime.h>
#include <hip/hip_bf16.h>
#include <stdint.h>

#define WAVES 8
#define PAT 1
#define HMAP 0
#define PRE 4
#define NUMA 1
#define HSTRV 20480
#define EARLYV 0
#define KBAR 0
#define PRIO 0
#define KILL 0
#define SAUX 16
#define AMP 0
#define AMPK 1

constexpr int H = 32, C = 4096, D = 128, SPLITS = 8;
constexpr int NT = C / SPLITS / WAVES / 4;
constexpr float SCALE = 0.088388347648318440f, LOG2E = 1.4426950408889634f;
constexpr float C2 = SCALE * LOG2E;

using bf16x2 = __attribute__((ext_vector_type(2))) __bf16;
using bf16x8 = __attribute__((ext_vector_type(8))) __bf16;
using u32x4 = __attribute__((ext_vector_type(4))) unsigned;
using f32x4 = __attribute__((ext_vector_type(4))) float;
using u32x2 = __attribute__((ext_vector_type(2))) unsigned;
typedef __attribute__((address_space(1))) unsigned long long gu64;
using f32x2 = __attribute__((ext_vector_type(2))) float;
#define GAS __attribute__((address_space(1)))
typedef GAS unsigned gu32;
#define RLX_AGENT __ATOMIC_RELAXED, __HIP_MEMORY_SCOPE_AGENT

__device__ __forceinline__ float dot2(unsigned k, unsigned q, float acc) {
  return __builtin_amdgcn_fdot2_f32_bf16(__builtin_bit_cast(bf16x2, k), __builtin_bit_cast(bf16x2, q), acc, false);
}
template <int CTRL> __device__ __forceinline__ float dpp(float x) {
  return __builtin_bit_cast(float, __builtin_amdgcn_mov_dpp(__builtin_bit_cast(int, x), CTRL, 0xf, 0xf, true));
}
__device__ __forceinline__ float row_sum16(float x) {
  x += dpp<0x128>(x); x += dpp<0x124>(x); x += dpp<0x122>(x); x += dpp<0x121>(x);
  return x;
}
__device__ __forceinline__ float row_max16(float x) {
  asm volatile("s_nop 1\n\tv_max_f32_dpp %0, %0, %0 row_ror:8 row_mask:0xf bank_mask:0xf\n\ts_nop 1\n\t"
               "v_max_f32_dpp %0, %0, %0 row_ror:4 row_mask:0xf bank_mask:0xf\n\ts_nop 1\n\t"
               "v_max_f32_dpp %0, %0, %0 row_ror:2 row_mask:0xf bank_mask:0xf\n\ts_nop 1\n\t"
               "v_max_f32_dpp %0, %0, %0 row_ror:1 row_mask:0xf bank_mask:0xf" : "+v"(x));
  return x;
}
__device__ __forceinline__ float xrow_max(float x) {
  auto s = __builtin_amdgcn_permlane16_swap(__float_as_uint(x), __float_as_uint(x), false, false);
  x = fmaxf(__uint_as_float(s[0]), __uint_as_float(s[1]));
  auto t = __builtin_amdgcn_permlane32_swap(__float_as_uint(x), __float_as_uint(x), false, false);
  return fmaxf(__uint_as_float(t[0]), __uint_as_float(t[1]));
}
__device__ __forceinline__ float xrow_sum(float x) {
  auto s = __builtin_amdgcn_permlane16_swap(__float_as_uint(x), __float_as_uint(x), false, false);
  x = __uint_as_float(s[0]) + __uint_as_float(s[1]);
  auto t = __builtin_amdgcn_permlane32_swap(__float_as_uint(x), __float_as_uint(x), false, false);
  return __uint_as_float(t[0]) + __uint_as_float(t[1]);
}
__device__ __forceinline__ float bf2f(unsigned short h) { return __uint_as_float((unsigned)h << 16); }

constexpr unsigned HSTR = 256, RSTR = HSTRV, CPY = 16384, REC = 640, WS_BYTES = CPY + H * RSTR;

#define AMPEND() do { if (AMP) { RT(te); const unsigned long long dl = AMP == 1 ? te - ta : AMP == 2 ? te - t5 : AMP == 3 ? t5 - t4 : AMP == 4 ? ti - tb : AMP == 5 ? t4 - ta : AMP == 6 ? t4 - t3 : 0; \
  unsigned long long tgt = te + dl * AMPK, now; do { RT(now); } while (now < tgt); } } while (0)
#define RT(x) do { __builtin_amdgcn_sched_barrier(0); asm volatile("s_memrealtime %0\n\ts_waitcnt lgkmcnt(0)" : "=s"(x) :: "memory"); __builtin_amdgcn_sched_barrier(0); } while (0)
#define DB0()
#define DB1()
#define DBS(x)
#define DBEND(sw, last)

__global__ void __launch_bounds__(WAVES * 64) attn_fused(const void* __restrict__ K, const void* __restrict__ V,
                                                         const u32x4* __restrict__ q, unsigned char* ws,
                                                         const u32x4* __restrict__ kt, const unsigned short* __restrict__ vt,
                                                         const unsigned short* __restrict__ mask, __hip_bfloat16* __restrict__ out) {
  __shared__ __attribute__((aligned(16))) float lds[WAVES * 128 + 2 * WAVES];
  unsigned long long ta = 0, tb = 0, ti = 0, t3 = 0, t4 = 0, t5 = 0, te = 0;
  if (AMP) RT(ta);
  const int wg = blockIdx.x;
  const int head = HMAP == 0 ? (wg >> 3) : HMAP == 1 ? (wg & 31) : HMAP == 2 ? ((wg & 7) * 4 + ((wg >> 3) & 3)) : (((wg & 3) << 3) | ((wg >> 3) & 7)), split = HMAP == 0 ? (wg & 7) : HMAP <= 2 ? (wg >> 5) : (((wg >> 2) & 1) | ((wg >> 6) << 1));
  const int tid = threadIdx.x, lane = tid & 63, wave = __builtin_amdgcn_readfirstlane(tid >> 6);
  const int c = lane & 15, g = lane >> 4;
  DB0();
  if (PRIO) __builtin_amdgcn_s_setprio(3);
  const auto rk = __builtin_amdgcn_make_buffer_rsrc((void*)K, 0, H * C * D * 2, 0x00020000);
  const auto rv = __builtin_amdgcn_make_buffer_rsrc((void*)V, 0, H * C * D * 2, 0x00020000);
  constexpr int SW = PAT == 0 ? NT : PAT == 1 ? 1 : SPLITS, ST = PAT == 0 ? 1 : PAT == 1 ? WAVES : SPLITS * WAVES;
  const unsigned so = (unsigned)(head * 1024 + (PAT == 2 ? split : split * 128) + wave * SW) * 1024u;
  const unsigned vo = lane * 16u;
  u32x4 qv;
#define QLOAD() qv = q[head * 16 + c]
  const int d0 = 32 * g + 16 * (c >> 3) + (c & 7);
  const unsigned nw = wave == 0 ? WS_BYTES : 0u, nt_ = wave == 0 ? H * D * 2 + 16 : 0u;
  const auto rw = __builtin_amdgcn_make_buffer_rsrc((void*)ws, 0, nw, 0x00020000);
  const auto rkt = __builtin_amdgcn_make_buffer_rsrc((void*)kt, 0, nt_, 0x00020000);
  const auto rvt = __builtin_amdgcn_make_buffer_rsrc((void*)vt, 0, nt_, 0x00020000);
  const auto rmk = __builtin_amdgcn_make_buffer_rsrc((void*)mask, 0, nt_ ? 16u : 0u, 0x00020000);
  unsigned epoch; u32x2 w0; u32x4 ktv; unsigned short vt0, vt1, mk;
#define SMALL_LOADS() do { epoch = __builtin_amdgcn_raw_buffer_load_b32(rw, 0, head * HSTR, 16); \
    w0 = __builtin_amdgcn_raw_buffer_load_b64(rw, 128, head * HSTR, 16); \
    ktv = __builtin_amdgcn_raw_buffer_load_b128(rkt, c * 16, head * 256, 0); \
    vt0 = __builtin_amdgcn_raw_buffer_load_b16(rvt, d0 * 2, head * 256, 0); \
    vt1 = __builtin_amdgcn_raw_buffer_load_b16(rvt, d0 * 2 + 16, head * 256, 0); \
    mk = __builtin_amdgcn_raw_buffer_load_b16(rmk, 0, 0, 0); } while (0)
  u32x4 k[NT], v[NT];
  asm volatile("" :: "s"(out));
  if (AMP == 4) { asm volatile("" :: "s"(K), "s"(V), "s"(q)); RT(tb); }
#pragma unroll
  for (int t = 0; t < NT; ++t) {
    k[t] = __builtin_amdgcn_raw_buffer_load_b128(rk, vo, so + t * ST * 1024, AUX);
    if (t == 0 && EARLYV) { v[0] = __builtin_amdgcn_raw_buffer_load_b128(rv, vo, so, AUX); if (EARLYV == 2) { QLOAD(); SMALL_LOADS(); } __builtin_amdgcn_sched_barrier(0); }
  }
  if (AMP == 4) RT(ti);
  if (EARLYV != 2) { QLOAD(); SMALL_LOADS(); }
  if (KBAR) { __builtin_amdgcn_sched_barrier(0); __builtin_amdgcn_s_barrier(); __builtin_amdgcn_sched_barrier(0); }
#pragma unroll
  for (int t = EARLYV ? 1 : 0; t < PRE; ++t) v[t] = __builtin_amdgcn_raw_buffer_load_b128(rv, vo, so + t * ST * 1024, AUX);
  __builtin_amdgcn_sched_barrier(0);
  float x[NT];
#pragma unroll
  for (int t = 0; t < NT; ++t) {
    float d = dot2(k[t][0], qv[0], 0.f);
    d = dot2(k[t][1], qv[1], d); d = dot2(k[t][2], qv[2], d); d = dot2(k[t][3], qv[3], d);
    x[t] = row_sum16(d) * C2;
  }
  float m = x[0];
#pragma unroll
  for (int t = 1; t < NT; ++t) m = fmaxf(m, x[t]);
  m = xrow_max(m);
  const float M = m;
  float* lm = lds + WAVES * 128;
  const int n = c & 7;
  const unsigned lomask = (c >= 8) ? 0xffff0000u : 0u;
  unsigned sel[4];
#pragma unroll
  for (int r = 0; r < 4; ++r) sel[r] = ((n >> 1) == r) ? ((n & 1) ? 0x03020c0cu : 0x0c0c0302u) : 0x0c0c0c0cu;
  float l = 0.f;
  u32x4 b[NT];
#pragma unroll
  for (int t = 0; t < NT; ++t) {
    const float p = __builtin_amdgcn_exp2f(x[t] - M);
    l += p;
    const unsigned pu = __float_as_uint(p);
    const unsigned s = __float_as_uint(p - __uint_as_float(pu & lomask));
#pragma unroll
    for (int r = 0; r < 4; ++r) b[t][r] = __builtin_amdgcn_perm(s, s, sel[r]);
  }
  l = xrow_sum(l);
  if (lane == 0) *reinterpret_cast<f32x2*>(lm + 2 * wave) = f32x2{M, l};
  float mx, vtf0 = bf2f(vt0), vtf1 = bf2f(vt1);
  {
    float d = dot2(ktv[0], qv[0], 0.f);
    d = dot2(ktv[1], qv[1], d); d = dot2(ktv[2], qv[2], d); d = dot2(ktv[3], qv[3], d);
    const float st = (row_sum16(d) * SCALE + (1.0f - bf2f(mk)) * -1000000000.0f) * LOG2E;
    mx = c == 8 ? st : -INFINITY;
  }
  unsigned tag = epoch + 1u;
  asm volatile("" : "+v"(mx), "+v"(vtf0), "+v"(vtf1), "+v"(tag));
  gu64* cnt = (gu64*)(ws + head * HSTR + 128);
  if (!(KILL & 4) && tid == 0 && w0[1] != tag) {
    unsigned long long e = ((unsigned long long)w0[1] << 32) | w0[0];
    __hip_atomic_compare_exchange_strong(cnt, &e, (unsigned long long)tag << 32, __ATOMIC_RELAXED, __ATOMIC_RELAXED, __HIP_MEMORY_SCOPE_AGENT);
  }
  __builtin_amdgcn_sched_barrier(0);
  if (PRIO) __builtin_amdgcn_s_setprio(0);
#pragma unroll
  for (int t = PRE; t < NT; ++t) v[t] = __builtin_amdgcn_raw_buffer_load_b128(rv, vo, so + t * ST * 1024, AUX);
  __builtin_amdgcn_sched_barrier(0);
  __syncthreads();
  const f32x2 ml = *reinterpret_cast<const f32x2*>(lm + 2 * (c & (WAVES - 1)));
  const float MW = __uint_as_float(__float_as_uint(row_max16(ml[0])) & 0xffffff00u);
  float L = row_sum16(__builtin_amdgcn_exp2f(ml[0] - MW) * ml[1]) * (WAVES / 16.0f);
  float fw = __builtin_amdgcn_exp2f(M - MW);
  L = __uint_as_float(__float_as_uint(L) & 0xffffff00u);
  float lx = c == split ? L : 1.0f;
  mx = c == split ? MW : mx;
  unsigned tg = tag & 0xffu;
  u32x2 gml = {__float_as_uint(MW) | tg, __float_as_uint(L) | tg};
  const bool oth = c < 8 && c != split;
  unsigned keepml = oth ? 0xffffff00u : 0u;
  const unsigned hb = head * RSTR + CPY;
  const unsigned long long wa = (unsigned long long)ws + hb + 2048;
  const unsigned near = NUMA ? ((((unsigned)(wa >> 13) ^ (unsigned)(wa >> 21) ^ ((unsigned)__builtin_amdgcn_s_getreg(20 | (3 << 11)) >> 2)) & 1u) << 13) : 0u;
  unsigned ca = hb + near, cb = hb + (near ^ 8192u), mine = hb + split * REC;
  unsigned vo8 = lane * 8u, vo8h = vo8 + 4 * REC, mlo = (c & 7) * REC + 512u + g * 8u, lo8 = lane * 8u;
  unsigned ato = lane == 0 ? head * HSTR + 128u : WS_BYTES, mo = lane < 16 ? vo8 + 512u : WS_BYTES;
  unsigned kbml = keepml >> 24;
  mlo = oth ? mlo : WS_BYTES;
  int old = 1;
  unsigned ks[SPLITS];
#pragma unroll
  for (int s = 0; s < SPLITS; ++s) { ks[s] = s == split ? 0u : 0xffffff00u; asm volatile("" : "+s"(ks[s])); }
  __hip_bfloat16* op = out + head * D + d0;
  asm volatile("" : "+v"(fw), "+v"(lx), "+v"(mx), "+v"(gml), "+v"(keepml), "+v"(vo8), "+v"(vo8h), "+v"(mlo), "+v"(ato), "+v"(mo), "+v"(tg), "+v"(kbml), "+v"(old), "+v"(op), "+v"(lo8));
  asm volatile("" : "+s"(ca), "+s"(cb), "+s"(mine));
  __builtin_amdgcn_sched_barrier(0);
  f32x4 acc = {0.f, 0.f, 0.f, 0.f};
#pragma unroll
  for (int t = 0; t < NT; ++t)
    acc = __builtin_amdgcn_mfma_f32_16x16x32_bf16(__builtin_bit_cast(bf16x8, v[t]), __builtin_bit_cast(bf16x8, b[t]), acc, 0, 0, 0);
#pragma unroll
  for (int r = 0; r < 4; ++r) acc[r] += dpp<0x128>(acc[r]);
  f32x2 o2 = {c < 8 ? acc[0] : acc[2], c < 8 ? acc[1] : acc[3]};
  o2 *= fw;
  *reinterpret_cast<f32x2*>(lds + wave * 128 + lane * 2) = o2;
  if (AMP == 6) RT(t3);
  __syncthreads();
  if (wave != 0) return;
  DB1();
  u32x2 ga[SPLITS], gb[SPLITS], ma, mb;
#define SWEEP(G, MLV, BASE) do { MLV = __builtin_amdgcn_raw_buffer_load_b64(rw, mlo, BASE, 16); \
    _Pragma("unroll") for (int s = 0; s < SPLITS; ++s) G[s] = __builtin_amdgcn_raw_buffer_load_b64(rw, (s < 4 ? vo8 : vo8h) + (s & 3) * REC, (BASE) + (~ks[s] & 0x40000000u), 16); } while (0)
  old = (KILL & 4) ? 7 : __builtin_amdgcn_raw_ptr_buffer_atomic_add_i32(old, rw, ato, 0, 0);
  __builtin_amdgcn_sched_barrier(0);
  f32x2 ow[WAVES];
#pragma unroll
  for (int w = 0; w < WAVES; ++w) ow[w] = *reinterpret_cast<const f32x2*>(reinterpret_cast<const char*>(lds) + lo8 + w * 512);
#pragma unroll
  for (int st = 1; st < WAVES; st *= 2)
#pragma unroll
    for (int w = 0; w < WAVES; w += 2 * st) ow[w] += ow[w + st];
  unsigned oc0 = __float_as_uint(ow[0][0]) & 0xffffff00u, oc1 = __float_as_uint(ow[0][1]) & 0xffffff00u;
  {
    const u32x2 gv = {oc0 | tg, oc1 | tg};
    if (!(KILL & 2)) __builtin_amdgcn_raw_buffer_store_b64(gv, rw, vo8, mine, SAUX);
    if (!(KILL & 1)) __builtin_amdgcn_raw_buffer_store_b64(gv, rw, vo8, mine + 8192u, SAUX);
    if (!(KILL & 2)) __builtin_amdgcn_raw_buffer_store_b64(gml, rw, mo, mine, SAUX);
    if (!(KILL & 1)) __builtin_amdgcn_raw_buffer_store_b64(gml, rw, mo, mine + 8192u, SAUX);
  }
  DBS(dbp);
  __builtin_amdgcn_sched_barrier(0);
  if (__builtin_amdgcn_readfirstlane(old) != SPLITS - 1) { DBEND(0u, 0u); return; }
  SWEEP(ga, ma, ca);
  __builtin_amdgcn_sched_barrier(0);
  f32x2 o;
  unsigned bad;
#define BFI(M, X, Y) (((X) & (M)) | ((Y) & ~(M)))
#define ACC(G, S) { asm volatile("" : "+v"(G[S])); const float w_ = dpp<0x150 + S>(wc); \
    bad |= ((G[S][0] ^ tg) | (G[S][1] ^ tg)) & (ks[S] >> 24); \
    const f32x2 x_ = {__uint_as_float(BFI(ks[S], G[S][0], oc0)), __uint_as_float(BFI(ks[S], G[S][1], oc1))}; \
    o = __builtin_elementwise_fma(f32x2{w_, w_}, x_, o); asm volatile("" : "+v"(o), "+v"(bad)); }
#define FINISH(G, MLV) do { asm volatile("" : "+v"(MLV)); \
    bad = ((MLV[0] ^ tg) | (MLV[1] ^ tg)) & kbml; \
    const float mc = __uint_as_float(BFI(keepml, MLV[0], __float_as_uint(mx))); \
    const float lc = __uint_as_float(BFI(keepml, MLV[1], __float_as_uint(lx))); \
    float wc = __builtin_amdgcn_exp2f(mc - row_max16(mc)); \
    wc *= __builtin_amdgcn_rcpf(row_sum16(wc * lc)); \
    const float wt = dpp<0x158>(wc); \
    o = f32x2{wt * vtf0, wt * vtf1}; \
    asm volatile("" : "+v"(o), "+v"(bad), "+v"(wc)); \
    ACC(G, 0) ACC(G, 1) ACC(G, 2) ACC(G, 3) ACC(G, 4) ACC(G, 5) ACC(G, 6) ACC(G, 7) } while (0)
  unsigned nsw = 1;
  FINISH(ga, ma);
  DBS(dba);
  if (!(KILL & 2) && !__all(bad == 0)) {
    for (unsigned spins = 0; !__all(bad == 0) && spins < (1u << 20); ++spins) {
      asm volatile("" ::: "memory");
      SWEEP(gb, mb, (KILL & 1) ? ca : cb); ++nsw;
      FINISH(gb, mb);
    }
  }
  DBS(dbb);
  op[0] = __float2bfloat16(o[0]);
  op[8] = __float2bfloat16(o[1]);
  __builtin_amdgcn_sched_barrier(0);
  if (lane == 0) __builtin_amdgcn_raw_buffer_store_b32(tag, rw, 0, head * HSTR, 16);
  DBEND(nsw, 1u);
  AMPEND();
}

extern "C" void kernel_launch(void* const* d_in, const int* in_sizes, int n_in, void* d_out, int out_size, void* d_ws,
                              size_t ws_size, hipStream_t stream) {
  attn_fused<<<H * SPLITS, WAVES * 64, 0, stream>>>(d_in[1], d_in[2], (const u32x4*)d_in[0], (unsigned char*)d_ws,
                                                    (const u32x4*)d_in[3], (const unsigned short*)d_in[4],
                                                    (const unsigned short*)d_in[5], (__hip_bfloat16*)d_out);
}
```

```cpp
#define EARLYW 0
#define AMP 3
#define AMPK 10
#include <hip/hip_runtime.h>
#include <hip/hip_bf16.h>
#include <stdint.h>

#define AUX 2
#define HMAP 2
#define PRE 4
#define NEAR 1
#define OAUX 0
#define MLLATE 0
#define PINB 1

constexpr int H = 32, C = 4096, D = 128, SPLITS = 8, WAVES = 8;
constexpr int NT = C / SPLITS / WAVES / 4;
constexpr float SCALE = 0.088388347648318440f, LOG2E = 1.4426950408889634f;
constexpr float C2 = SCALE * LOG2E;

using bf16x2 = __attribute__((ext_vector_type(2))) __bf16;
using bf16x8 = __attribute__((ext_vector_type(8))) __bf16;
using u32x4 = __attribute__((ext_vector_type(4))) unsigned;
using u32x2 = __attribute__((ext_vector_type(2))) unsigned;
using f32x4 = __attribute__((ext_vector_type(4))) float;
using f32x2 = __attribute__((ext_vector_type(2))) float;
typedef __attribute__((address_space(1))) unsigned long long gu64;

__device__ __forceinline__ float dot2(unsigned k, unsigned q, float acc) {
  return __builtin_amdgcn_fdot2_f32_bf16(__builtin_bit_cast(bf16x2, k), __builtin_bit_cast(bf16x2, q), acc, false);
}
template <int CTRL> __device__ __forceinline__ float dpp(float x) {
  return __builtin_bit_cast(float, __builtin_amdgcn_mov_dpp(__builtin_bit_cast(int, x), CTRL, 0xf, 0xf, true));
}
__device__ __forceinline__ float row_sum16(float x) {
  x += dpp<0x128>(x); x += dpp<0x124>(x); x += dpp<0x122>(x); x += dpp<0x121>(x);
  return x;
}
__device__ __forceinline__ float row_max16(float x) {
  asm volatile("s_nop 1\n\tv_max_f32_dpp %0, %0, %0 row_ror:8 row_mask:0xf bank_mask:0xf\n\ts_nop 1\n\t"
               "v_max_f32_dpp %0, %0, %0 row_ror:4 row_mask:0xf bank_mask:0xf\n\ts_nop 1\n\t"
               "v_max_f32_dpp %0, %0, %0 row_ror:2 row_mask:0xf bank_mask:0xf\n\ts_nop 1\n\t"
               "v_max_f32_dpp %0, %0, %0 row_ror:1 row_mask:0xf bank_mask:0xf" : "+v"(x));
  return x;
}
__device__ __forceinline__ float xrow_max(float x) {
  auto s = __builtin_amdgcn_permlane16_swap(__float_as_uint(x), __float_as_uint(x), false, false);
  x = fmaxf(__uint_as_float(s[0]), __uint_as_float(s[1]));
  auto t = __builtin_amdgcn_permlane32_swap(__float_as_uint(x), __float_as_uint(x), false, false);
  return fmaxf(__uint_as_float(t[0]), __uint_as_float(t[1]));
}
__device__ __forceinline__ float xrow_sum(float x) {
  auto s = __builtin_amdgcn_permlane16_swap(__float_as_uint(x), __float_as_uint(x), false, false);
  x = __uint_as_float(s[0]) + __uint_as_float(s[1]);
  auto t = __builtin_amdgcn_permlane32_swap(__float_as_uint(x), __float_as_uint(x), false, false);
  return __uint_as_float(t[0]) + __uint_as_float(t[1]);
}
__device__ __forceinline__ float bf2f(unsigned short h) { return __uint_as_float((unsigned)h << 16); }
__device__ __forceinline__ unsigned bfi(unsigned m, unsigned x, unsigned y) { return (x & m) | (y & ~m); }

constexpr unsigned HSTR = 16384, REC = 512, O_ML = 4096, O_EP = 5120, O_CNT = 5248, WS_BYTES = H * HSTR;
constexpr unsigned KEEP = 0xffffff00u;

#define RT(x) do { __builtin_amdgcn_sched_barrier(0); asm volatile("s_memrealtime %0\n\ts_waitcnt lgkmcnt(0)" : "=s"(x) :: "memory"); __builtin_amdgcn_sched_barrier(0); } while (0)
#define DB0()
#define DB1()
#define DBEND(sw, last)
#define AMPEND(LAST) do { if (AMP && (LAST || AMP == 2)) { asm volatile("s_waitcnt vmcnt(0)" ::: "memory"); unsigned long long te, now; RT(te); \
   const unsigned long long dl = AMP == 1 ? te - ta : AMP == 2 ? t4 - ta : te - t4; const unsigned long long tgt = te + dl * AMPK; do { RT(now); } while (now < tgt); } } while (0)
#define SB() __builtin_amdgcn_sched_barrier(0)

__global__ void __launch_bounds__(WAVES * 64) attn_fused(const void* __restrict__ K, const void* __restrict__ V,
                                                         const u32x4* __restrict__ q, unsigned char* ws,
                                                         const u32x4* __restrict__ kt, const unsigned short* __restrict__ vt,
                                                         const unsigned short* __restrict__ mask, __hip_bfloat16* __restrict__ out) {
  __shared__ __attribute__((aligned(16))) float lds[WAVES * 128 + 2 * WAVES];
  unsigned long long ta = 0, t4 = 0;
  if (AMP) ta = __builtin_amdgcn_s_memrealtime();
  const int wg = blockIdx.x;
  const int head = HMAP == 0 ? (wg >> 3) : ((wg & 7) * 4 + ((wg >> 3) & 3)), split = HMAP == 0 ? (wg & 7) : (wg >> 5);
  const int tid = threadIdx.x, lane = tid & 63, wave = __builtin_amdgcn_readfirstlane(tid >> 6);
  const int c = lane & 15, g = lane >> 4;
  DB0();
  const auto rk = __builtin_amdgcn_make_buffer_rsrc((void*)K, 0, H * C * D * 2, 0x00020000);
  const auto rv = __builtin_amdgcn_make_buffer_rsrc((void*)V, 0, H * C * D * 2, 0x00020000);
  const unsigned so = (unsigned)(head * 1024 + split * 128 + wave * NT) * 1024u, vo = lane * 16u;
  u32x4 k[NT], v[NT];
#define LDK(t) k[t] = __builtin_amdgcn_raw_buffer_load_b128(rk, vo, so + (t) * 1024, AUX)
#define LDV(t) v[t] = __builtin_amdgcn_raw_buffer_load_b128(rv, vo, so + (t) * 1024, AUX)
  LDK(0); LDV(0);
  const u32x4 qv = q[head * 16 + c];
  SB();
#pragma unroll
  for (int t = 1; t < NT; ++t) LDK(t);
#pragma unroll
  for (int t = 1; t < PRE; ++t) LDV(t);
  SB();
  float x[NT];
#pragma unroll
  for (int t = 0; t < NT; ++t) {
    float d = dot2(k[t][0], qv[0], 0.f);
    d = dot2(k[t][1], qv[1], d); d = dot2(k[t][2], qv[2], d); d = dot2(k[t][3], qv[3], d);
    x[t] = row_sum16(d) * C2;
  }
  float m = x[0];
#pragma unroll
  for (int t = 1; t < NT; ++t) m = fmaxf(m, x[t]);
  m = xrow_max(m);
  const int n = c & 7;
  const unsigned lomask = (c >= 8) ? 0xffff0000u : 0u;
  unsigned sel[4];
#pragma unroll
  for (int r = 0; r < 4; ++r) sel[r] = ((n >> 1) == r) ? ((n & 1) ? 0x03020c0cu : 0x0c0c0302u) : 0x0c0c0c0cu;
  float l = 0.f;
  u32x4 b[NT];
#pragma unroll
  for (int t = 0; t < NT; ++t) {
    const float p = __builtin_amdgcn_exp2f(x[t] - m);
    l += p;
    const unsigned s = __float_as_uint(p - __uint_as_float(__float_as_uint(p) & lomask));
#pragma unroll
    for (int r = 0; r < 4; ++r) b[t][r] = __builtin_amdgcn_perm(s, s, sel[r]);
    if (PINB) asm volatile("" : "+v"(b[t]));
  }
  l = xrow_sum(l);
  float* lm = lds + WAVES * 128;
  if (lane == 0) *reinterpret_cast<f32x2*>(lm + 2 * wave) = f32x2{m, l};
  SB();
#pragma unroll
  for (int t = PRE; t < NT; ++t) LDV(t);
  const int d0 = 32 * g + 16 * (c >> 3) + n;
  const auto rw = __builtin_amdgcn_make_buffer_rsrc((void*)ws, 0, wave == 0 ? WS_BYTES : 0u, 0x00020000);
  const unsigned long long wa = (unsigned long long)ws + head * HSTR;
  unsigned hb = head * HSTR + (NEAR ? ((((unsigned)(wa >> 13) ^ (unsigned)(wa >> 21) ^ ((unsigned)head >> 4)) & 1u) << 13) : 0u);
  float mx = 0.f, vtf0 = 0.f, vtf1 = 0.f;
  unsigned tag = 0;
  if (wave == 0) {
    const unsigned epoch = __builtin_amdgcn_raw_buffer_load_b32(rw, 0, hb + O_EP, 16);
    const u32x2 w0 = __builtin_amdgcn_raw_buffer_load_b64(rw, 0, hb + O_CNT, 16);
    const u32x4 ktv = kt[head * 16 + c];
    vtf0 = bf2f(vt[head * D + d0]); vtf1 = bf2f(vt[head * D + d0 + 8]);
    float d = dot2(ktv[0], qv[0], 0.f);
    d = dot2(ktv[1], qv[1], d); d = dot2(ktv[2], qv[2], d); d = dot2(ktv[3], qv[3], d);
    const float st = (row_sum16(d) * SCALE + (1.0f - bf2f(mask[0])) * -1000000000.0f) * LOG2E;
    mx = c == 8 ? st : -INFINITY;
    tag = epoch + 1u;
    if (lane == 0 && w0[1] != tag) {
      unsigned long long e = ((unsigned long long)w0[1] << 32) | w0[0];
      __hip_atomic_compare_exchange_strong((gu64*)(ws + hb + O_CNT), &e, (unsigned long long)tag << 32, __ATOMIC_RELAXED, __ATOMIC_RELAXED,
                                           __HIP_MEMORY_SCOPE_AGENT);
    }
    asm volatile("s_waitcnt vmcnt(0)" : "+v"(mx), "+v"(vtf0), "+v"(vtf1), "+v"(tag) :: "memory");
  }
  SB();
  __syncthreads();
  const f32x2 ml = *reinterpret_cast<const f32x2*>(lm + 2 * n);
  const float MW = __uint_as_float(__float_as_uint(row_max16(ml[0])) & KEEP);
  const float L = __uint_as_float(__float_as_uint(row_sum16(__builtin_amdgcn_exp2f(ml[0] - MW) * ml[1]) * 0.5f) & KEEP);
  float fw = __builtin_amdgcn_exp2f(m - MW);
  unsigned tg = tag & 0xffu;
  unsigned vo8 = lane * 8u;
  u32x2 gml = {__float_as_uint(MW) | tg, __float_as_uint(L) | tg};
  unsigned mo = lane < 16 ? vo8 : WS_BYTES, mlm = hb + O_ML + split * 128;
  if (!MLLATE) __builtin_amdgcn_raw_buffer_store_b64(gml, rw, mo, mlm, 16);
  else asm volatile("" : "+v"(gml), "+v"(mo), "+s"(mlm));
  const bool oth = c < 8 && c != split;
  float lx = c == split ? L : 1.0f;
  mx = c == split ? MW : mx;
  unsigned keepml = oth ? KEEP : 0u, mlo = oth ? n * 128u + g * 8u : WS_BYTES;
  unsigned ato = lane == 0 ? hb + O_CNT : WS_BYTES, epo = lane == 0 ? hb + O_EP : WS_BYTES;
  unsigned op = (head * D + d0 + ((lane & 1) ? 7 : 0)) * 2u;
  int old = 1;
  unsigned ks[SPLITS], sb[SPLITS];
#pragma unroll
  for (int s = 0; s < SPLITS; ++s) {
    ks[s] = s == split ? 0u : KEEP; sb[s] = s == split ? WS_BYTES : hb + s * REC;
    asm volatile("" : "+s"(ks[s]), "+s"(sb[s]));
  }
  unsigned mine = hb + split * REC, mlb = hb + O_ML;
  const auto ro = __builtin_amdgcn_make_buffer_rsrc((void*)out, 0, H * D * 2, 0x00020000);
  asm volatile("" : "+v"(fw), "+v"(lx), "+v"(mx), "+v"(keepml), "+v"(vo8), "+v"(mlo), "+v"(ato), "+v"(epo), "+v"(tg), "+v"(old), "+v"(op));
  asm volatile("" : "+s"(mine), "+s"(mlb) : "s"(ro));
  SB();
  f32x4 acc = {0.f, 0.f, 0.f, 0.f};
#pragma unroll
  for (int t = 0; t < NT; ++t)
    acc = __builtin_amdgcn_mfma_f32_16x16x32_bf16(__builtin_bit_cast(bf16x8, v[t]), __builtin_bit_cast(bf16x8, b[t]), acc, 0, 0, 0);
#pragma unroll
  for (int r = 0; r < 4; ++r) acc[r] += dpp<0x128>(acc[r]);
  f32x2 o2 = {c < 8 ? acc[0] : acc[2], c < 8 ? acc[1] : acc[3]};
  o2 *= fw;
  *reinterpret_cast<f32x2*>(lds + wave * 128 + lane * 2) = o2;
  u32x2 gm;
  if (EARLYW) gm = __builtin_amdgcn_raw_buffer_load_b64(rw, mlo, mlb, 16);
  __syncthreads();
  if (wave != 0) return;
  DB1();
  if (AMP > 1) t4 = __builtin_amdgcn_s_memrealtime();
  old = __builtin_amdgcn_raw_ptr_buffer_atomic_add_i32(old, rw, ato, 0, 0);
  if (MLLATE == 2) __builtin_amdgcn_raw_buffer_store_b64(gml, rw, mo, mlm, 16);
  SB();
  f32x2 ow[WAVES];
#pragma unroll
  for (int w = 0; w < WAVES; ++w) ow[w] = *reinterpret_cast<const f32x2*>(reinterpret_cast<const char*>(lds) + vo8 + w * 512);
#pragma unroll
  for (int s2 = 1; s2 < WAVES; s2 *= 2)
#pragma unroll
    for (int w = 0; w < WAVES; w += 2 * s2) ow[w] += ow[w + s2];
  const unsigned oc0 = __float_as_uint(ow[0][0]) & KEEP, oc1 = __float_as_uint(ow[0][1]) & KEEP;
  __builtin_amdgcn_raw_buffer_store_b64(u32x2{oc0 | tg, oc1 | tg}, rw, vo8, mine, 16);
  if (MLLATE == 1) __builtin_amdgcn_raw_buffer_store_b64(gml, rw, mo, mlm, 16);
  SB();
  float wc;
  unsigned badw;
#define WEIGHTS() do { badw = ((gm[0] ^ tg) | (gm[1] ^ tg)) & (keepml >> 24); \
    const float mc = __uint_as_float(bfi(keepml, gm[0], __float_as_uint(mx))), lc = __uint_as_float(bfi(keepml, gm[1], __float_as_uint(lx))); \
    wc = __builtin_amdgcn_exp2f(mc - row_max16(mc)); wc *= __builtin_amdgcn_rcpf(row_sum16(wc * lc)); \
    asm volatile("" : "+v"(wc), "+v"(badw)); } while (0)
  if (EARLYW) WEIGHTS();
  SB();
  if (__builtin_amdgcn_readfirstlane(old) != SPLITS - 1) { DBEND(0u, 0u); AMPEND(0); return; }
  u32x2 gs[SPLITS];
  f32x2 o;
  unsigned bad, nsw = 1;
#define SWEEP() do { _Pragma("unroll") for (int s = 0; s < SPLITS; ++s) gs[s] = __builtin_amdgcn_raw_buffer_load_b64(rw, vo8, sb[s], 16); } while (0)
#define ACC(S) { asm volatile("" : "+v"(gs[S])); const float w_ = dpp<0x150 + S>(wc); \
    bad |= ((gs[S][0] ^ tg) | (gs[S][1] ^ tg)) & (ks[S] >> 24); \
    o = __builtin_elementwise_fma(f32x2{w_, w_}, f32x2{__uint_as_float(bfi(ks[S], gs[S][0], oc0)), __uint_as_float(bfi(ks[S], gs[S][1], oc1))}, o); \
    asm volatile("" : "+v"(o), "+v"(bad)); }
#define MERGE() do { const float wt = dpp<0x158>(wc); o = f32x2{wt * vtf0, wt * vtf1}; bad = badw; \
    ACC(0) ACC(1) ACC(2) ACC(3) ACC(4) ACC(5) ACC(6) ACC(7) } while (0)
  if (!EARLYW) gm = __builtin_amdgcn_raw_buffer_load_b64(rw, mlo, mlb, 16);
  SWEEP();
  __builtin_amdgcn_raw_buffer_store_b32(tag, rw, epo, 0, 16);
  SB();
  if (!EARLYW) WEIGHTS();
  MERGE();
  if (!__all(bad == 0)) {
#pragma nounroll
    for (unsigned spins = 0; spins < (1u << 20); ++spins) {
      asm volatile("" ::: "memory");
      gm = __builtin_amdgcn_raw_buffer_load_b64(rw, mlo, mlb, 16);
      SWEEP(); ++nsw;
      WEIGHTS();
      MERGE();
      if (__all(bad == 0)) break;
    }
  }
  {
    const float rcv = dpp<0xB1>((lane & 1) ? o[0] : o[1]);
    const bf16x2 pk = {(__bf16)((lane & 1) ? rcv : o[0]), (__bf16)((lane & 1) ? o[1] : rcv)};
    __builtin_amdgcn_raw_buffer_store_b32(__builtin_bit_cast(unsigned, pk), ro, op, 0, OAUX);
  }
  DBEND(nsw, 1u);
  AMPEND(1);
}

extern "C" void kernel_launch(void* const* d_in, const int* in_sizes, int n_in, void* d_out, int out_size, void* d_ws,
                              size_t ws_size, hipStream_t stream) {
  attn_fused<<<H * SPLITS, WAVES * 64, 0, stream>>>(d_in[1], d_in[2], (const u32x4*)d_in[0], (unsigned char*)d_ws,
                                                    (const u32x4*)d_in[3], (const unsigned short*)d_in[4],
                                                    (const unsigned short*)d_in[5], (__hip_bfloat16*)d_out);
}
```

```cpp
#define EARLYW 0
#define AUX 18
#define AMP 1
#define AMPK 4
#include <hip/hip_runtime.h>
#include <hip/hip_bf16.h>
#include <stdint.h>

#define HMAP 2
#define PRE 4
#define NEAR 1
#define OAUX 0
#define MLLATE 0
#define PINB 1

constexpr int H = 32, C = 4096, D = 128, SPLITS = 8, WAVES = 8;
constexpr int NT = C / SPLITS / WAVES / 4;
constexpr float SCALE = 0.088388347648318440f, LOG2E = 1.4426950408889634f;
constexpr float C2 = SCALE * LOG2E;

using bf16x2 = __attribute__((ext_vector_type(2))) __bf16;
using bf16x8 = __attribute__((ext_vector_type(8))) __bf16;
using u32x4 = __attribute__((ext_vector_type(4))) unsigned;
using u32x2 = __attribute__((ext_vector_type(2))) unsigned;
using f32x4 = __attribute__((ext_vector_type(4))) float;
using f32x2 = __attribute__((ext_vector_type(2))) float;
typedef __attribute__((address_space(1))) unsigned long long gu64;

__device__ __forceinline__ float dot2(unsigned k, unsigned q, float acc) {
  return __builtin_amdgcn_fdot2_f32_bf16(__builtin_bit_cast(bf16x2, k), __builtin_bit_cast(bf16x2, q), acc, false);
}
template <int CTRL> __device__ __forceinline__ float dpp(float x) {
  return __builtin_bit_cast(float, __builtin_amdgcn_mov_dpp(__builtin_bit_cast(int, x), CTRL, 0xf, 0xf, true));
}
__device__ __forceinline__ float row_sum16(float x) {
  x += dpp<0x128>(x); x += dpp<0x124>(x); x += dpp<0x122>(x); x += dpp<0x121>(x);
  return x;
}
__device__ __forceinline__ float row_max16(float x) {
  asm volatile("s_nop 1\n\tv_max_f32_dpp %0, %0, %0 row_ror:8 row_mask:0xf bank_mask:0xf\n\ts_nop 1\n\t"
               "v_max_f32_dpp %0, %0, %0 row_ror:4 row_mask:0xf bank_mask:0xf\n\ts_nop 1\n\t"
               "v_max_f32_dpp %0, %0, %0 row_ror:2 row_mask:0xf bank_mask:0xf\n\ts_nop 1\n\t"
               "v_max_f32_dpp %0, %0, %0 row_ror:1 row_mask:0xf bank_mask:0xf" : "+v"(x));
  return x;
}
__device__ __forceinline__ float xrow_max(float x) {
  auto s = __builtin_amdgcn_permlane16_swap(__float_as_uint(x), __float_as_uint(x), false, false);
  x = fmaxf(__uint_as_float(s[0]), __uint_as_float(s[1]));
  auto t = __builtin_amdgcn_permlane32_swap(__float_as_uint(x), __float_as_uint(x), false, false);
  return fmaxf(__uint_as_float(t[0]), __uint_as_float(t[1]));
}
__device__ __forceinline__ float xrow_sum(float x) {
  auto s = __builtin_amdgcn_permlane16_swap(__float_as_uint(x), __float_as_uint(x), false, false);
  x = __uint_as_float(s[0]) + __uint_as_float(s[1]);
  auto t = __builtin_amdgcn_permlane32_swap(__float_as_uint(x), __float_as_uint(x), false, false);
  return __uint_as_float(t[0]) + __uint_as_float(t[1]);
}
__device__ __forceinline__ float bf2f(unsigned short h) { return __uint_as_float((unsigned)h << 16); }
__device__ __forceinline__ unsigned bfi(unsigned m, unsigned x, unsigned y) { return (x & m) | (y & ~m); }

constexpr unsigned HSTR = 16384, REC = 512, O_ML = 4096, O_EP = 5120, O_CNT = 5248, WS_BYTES = H * HSTR;
constexpr unsigned KEEP = 0xffffff00u;

#define RT(x) do { __builtin_amdgcn_sched_barrier(0); asm volatile("s_memrealtime %0\n\ts_waitcnt lgkmcnt(0)" : "=s"(x) :: "memory"); __builtin_amdgcn_sched_barrier(0); } while (0)
#define DB0()
#define DB1()
#define DBEND(sw, last)
#define AMPEND(LAST) do { if (AMP && (LAST || AMP == 2)) { unsigned long long te, now; RT(te); \
   const unsigned long long dl = AMP == 1 ? te - ta : AMP == 2 ? t4 - ta : te - t4; const unsigned long long tgt = te + dl * AMPK; do { RT(now); } while (now < tgt); } } while (0)
#define SB() __builtin_amdgcn_sched_barrier(0)

__global__ void __launch_bounds__(WAVES * 64) attn_fused(const void* __restrict__ K, const void* __restrict__ V,
                                                         const u32x4* __restrict__ q, unsigned char* ws,
                                                         const u32x4* __restrict__ kt, const unsigned short* __restrict__ vt,
                                                         const unsigned short* __restrict__ mask, __hip_bfloat16* __restrict__ out) {
  __shared__ __attribute__((aligned(16))) float lds[WAVES * 128 + 2 * WAVES];
  unsigned long long ta = 0, t4 = 0;
  if (AMP) ta = __builtin_amdgcn_s_memrealtime();
  const int wg = blockIdx.x;
  const int head = HMAP == 0 ? (wg >> 3) : ((wg & 7) * 4 + ((wg >> 3) & 3)), split = HMAP == 0 ? (wg & 7) : (wg >> 5);
  const int tid = threadIdx.x, lane = tid & 63, wave = __builtin_amdgcn_readfirstlane(tid >> 6);
  const int c = lane & 15, g = lane >> 4;
  DB0();
  const auto rk = __builtin_amdgcn_make_buffer_rsrc((void*)K, 0, H * C * D * 2, 0x00020000);
  const auto rv = __builtin_amdgcn_make_buffer_rsrc((void*)V, 0, H * C * D * 2, 0x00020000);
  const unsigned so = (unsigned)(head * 1024 + split * 128 + wave * NT) * 1024u, vo = lane * 16u;
  u32x4 k[NT], v[NT];
#define LDK(t) k[t] = __builtin_amdgcn_raw_buffer_load_b128(rk, vo, so + (t) * 1024, AUX)
#define LDV(t) v[t] = __builtin_amdgcn_raw_buffer_load_b128(rv, vo, so + (t) * 1024, AUX)
  LDK(0); LDV(0);
  const u32x4 qv = q[head * 16 + c];
  SB();
#pragma unroll
  for (int t = 1; t < NT; ++t) LDK(t);
#pragma unroll
  for (int t = 1; t < PRE; ++t) LDV(t);
  SB();
  float x[NT];
#pragma unroll
  for (int t = 0; t < NT; ++t) {
    float d = dot2(k[t][0], qv[0], 0.f);
    d = dot2(k[t][1], qv[1], d); d = dot2(k[t][2], qv[2], d); d = dot2(k[t][3], qv[3], d);
    x[t] = row_sum16(d) * C2;
  }
  float m = x[0];
#pragma unroll
  for (int t = 1; t < NT; ++t) m = fmaxf(m, x[t]);
  m = xrow_max(m);
  const int n = c & 7;
  const unsigned lomask = (c >= 8) ? 0xffff0000u : 0u;
  unsigned sel[4];
#pragma unroll
  for (int r = 0; r < 4; ++r) sel[r] = ((n >> 1) == r) ? ((n & 1) ? 0x03020c0cu : 0x0c0c0302u) : 0x0c0c0c0cu;
  float l = 0.f;
  u32x4 b[NT];
#pragma unroll
  for (int t = 0; t < NT; ++t) {
    const float p = __builtin_amdgcn_exp2f(x[t] - m);
    l += p;
    const unsigned s = __float_as_uint(p - __uint_as_float(__float_as_uint(p) & lomask));
#pragma unroll
    for (int r = 0; r < 4; ++r) b[t][r] = __builtin_amdgcn_perm(s, s, sel[r]);
    if (PINB) asm volatile("" : "+v"(b[t]));
  }
  l = xrow_sum(l);
  float* lm = lds + WAVES * 128;
  if (lane == 0) *reinterpret_cast<f32x2*>(lm + 2 * wave) = f32x2{m, l};
  SB();
#pragma unroll
  for (int t = PRE; t < NT; ++t) LDV(t);
  const int d0 = 32 * g + 16 * (c >> 3) + n;
  const auto rw = __builtin_amdgcn_make_buffer_rsrc((void*)ws, 0, wave == 0 ? WS_BYTES : 0u, 0x00020000);
  const unsigned long long wa = (unsigned long long)ws + head * HSTR;
  unsigned hb = head * HSTR + (NEAR ? ((((unsigned)(wa >> 13) ^ (unsigned)(wa >> 21) ^ ((unsigned)head >> 4)) & 1u) << 13) : 0u);
  float mx = 0.f, vtf0 = 0.f, vtf1 = 0.f;
  unsigned tag = 0;
  if (wave == 0) {
    const unsigned epoch = __builtin_amdgcn_raw_buffer_load_b32(rw, 0, hb + O_EP, 16);
    const u32x2 w0 = __builtin_amdgcn_raw_buffer_load_b64(rw, 0, hb + O_CNT, 16);
    const u32x4 ktv = kt[head * 16 + c];
    vtf0 = bf2f(vt[head * D + d0]); vtf1 = bf2f(vt[head * D + d0 + 8]);
    float d = dot2(ktv[0], qv[0], 0.f);
    d = dot2(ktv[1], qv[1], d); d = dot2(ktv[2], qv[2], d); d = dot2(ktv[3], qv[3], d);
    const float st = (row_sum16(d) * SCALE + (1.0f - bf2f(mask[0])) * -1000000000.0f) * LOG2E;
    mx = c == 8 ? st : -INFINITY;
    tag = epoch + 1u;
    if (lane == 0 && w0[1] != tag) {
      unsigned long long e = ((unsigned long long)w0[1] << 32) | w0[0];
      __hip_atomic_compare_exchange_strong((gu64*)(ws + hb + O_CNT), &e, (unsigned long long)tag << 32, __ATOMIC_RELAXED, __ATOMIC_RELAXED,
                                           __HIP_MEMORY_SCOPE_AGENT);
    }
    asm volatile("s_waitcnt vmcnt(0)" : "+v"(mx), "+v"(vtf0), "+v"(vtf1), "+v"(tag) :: "memory");
  }
  SB();
  __syncthreads();
  const f32x2 ml = *reinterpret_cast<const f32x2*>(lm + 2 * n);
  const float MW = __uint_as_float(__float_as_uint(row_max16(ml[0])) & KEEP);
  const float L = __uint_as_float(__float_as_uint(row_sum16(__builtin_amdgcn_exp2f(ml[0] - MW) * ml[1]) * 0.5f) & KEEP);
  float fw = __builtin_amdgcn_exp2f(m - MW);
  unsigned tg = tag & 0xffu;
  unsigned vo8 = lane * 8u;
  u32x2 gml = {__float_as_uint(MW) | tg, __float_as_uint(L) | tg};
  unsigned mo = lane < 16 ? vo8 : WS_BYTES, mlm = hb + O_ML + split * 128;
  if (!MLLATE) __builtin_amdgcn_raw_buffer_store_b64(gml, rw, mo, mlm, 16);
  else asm volatile("" : "+v"(gml), "+v"(mo), "+s"(mlm));
  const bool oth = c < 8 && c != split;
  float lx = c == split ? L : 1.0f;
  mx = c == split ? MW : mx;
  unsigned keepml = oth ? KEEP : 0u, mlo = oth ? n * 128u + g * 8u : WS_BYTES;
  unsigned ato = lane == 0 ? hb + O_CNT : WS_BYTES, epo = lane == 0 ? hb + O_EP : WS_BYTES;
  unsigned op = (head * D + d0 + ((lane & 1) ? 7 : 0)) * 2u;
  int old = 1;
  unsigned ks[SPLITS], sb[SPLITS];
#pragma unroll
  for (int s = 0; s < SPLITS; ++s) {
    ks[s] = s == split ? 0u : KEEP; sb[s] = s == split ? WS_BYTES : hb + s * REC;
    asm volatile("" : "+s"(ks[s]), "+s"(sb[s]));
  }
  unsigned mine = hb + split * REC, mlb = hb + O_ML;
  const auto ro = __builtin_amdgcn_make_buffer_rsrc((void*)out, 0, H * D * 2, 0x00020000);
  asm volatile("" : "+v"(fw), "+v"(lx), "+v"(mx), "+v"(keepml), "+v"(vo8), "+v"(mlo), "+v"(ato), "+v"(epo), "+v"(tg), "+v"(old), "+v"(op));
  asm volatile("" : "+s"(mine), "+s"(mlb) : "s"(ro));
  SB();
  f32x4 acc = {0.f, 0.f, 0.f, 0.f};
#pragma unroll
  for (int t = 0; t < NT; ++t)
    acc = __builtin_amdgcn_mfma_f32_16x16x32_bf16(__builtin_bit_cast(bf16x8, v[t]), __builtin_bit_cast(bf16x8, b[t]), acc, 0, 0, 0);
#pragma unroll
  for (int r = 0; r < 4; ++r) acc[r] += dpp<0x128>(acc[r]);
  f32x2 o2 = {c < 8 ? acc[0] : acc[2], c < 8 ? acc[1] : acc[3]};
  o2 *= fw;
  *reinterpret_cast<f32x2*>(lds + wave * 128 + lane * 2) = o2;
  u32x2 gm;
  if (EARLYW) gm = __builtin_amdgcn_raw_buffer_load_b64(rw, mlo, mlb, 16);
  __syncthreads();
  if (wave != 0) return;
  DB1();
  if (AMP > 1) t4 = __builtin_amdgcn_s_memrealtime();
  old = __builtin_amdgcn_raw_ptr_buffer_atomic_add_i32(old, rw, ato, 0, 0);
  if (MLLATE == 2) __builtin_amdgcn_raw_buffer_store_b64(gml, rw, mo, mlm, 16);
  SB();
  f32x2 ow[WAVES];
#pragma unroll
  for (int w = 0; w < WAVES; ++w) ow[w] = *reinterpret_cast<const f32x2*>(reinterpret_cast<const char*>(lds) + vo8 + w * 512);
#pragma unroll
  for (int s2 = 1; s2 < WAVES; s2 *= 2)
#pragma unroll
    for (int w = 0; w < WAVES; w += 2 * s2) ow[w] += ow[w + s2];
  const unsigned oc0 = __float_as_uint(ow[0][0]) & KEEP, oc1 = __float_as_uint(ow[0][1]) & KEEP;
  __builtin_amdgcn_raw_buffer_store_b64(u32x2{oc0 | tg, oc1 | tg}, rw, vo8, mine, 16);
  if (MLLATE == 1) __builtin_amdgcn_raw_buffer_store_b64(gml, rw, mo, mlm, 16);
  SB();
  float wc;
  unsigned badw;
#define WEIGHTS() do { badw = ((gm[0] ^ tg) | (gm[1] ^ tg)) & (keepml >> 24); \
    const float mc = __uint_as_float(bfi(keepml, gm[0], __float_as_uint(mx))), lc = __uint_as_float(bfi(keepml, gm[1], __float_as_uint(lx))); \
    wc = __builtin_amdgcn_exp2f(mc - row_max16(mc)); wc *= __builtin_amdgcn_rcpf(row_sum16(wc * lc)); \
    asm volatile("" : "+v"(wc), "+v"(badw)); } while (0)
  if (EARLYW) WEIGHTS();
  SB();
  if (__builtin_amdgcn_readfirstlane(old) != SPLITS - 1) { DBEND(0u, 0u); AMPEND(0); return; }
  u32x2 gs[SPLITS];
  f32x2 o;
  unsigned bad, nsw = 1;
#define SWEEP() do { _Pragma("unroll") for (int s = 0; s < SPLITS; ++s) gs[s] = __builtin_amdgcn_raw_buffer_load_b64(rw, vo8, sb[s], 16); } while (0)
#define ACC(S) { asm volatile("" : "+v"(gs[S])); const float w_ = dpp<0x150 + S>(wc); \
    bad |= ((gs[S][0] ^ tg) | (gs[S][1] ^ tg)) & (ks[S] >> 24); \
    o = __builtin_elementwise_fma(f32x2{w_, w_}, f32x2{__uint_as_float(bfi(ks[S], gs[S][0], oc0)), __uint_as_float(bfi(ks[S], gs[S][1], oc1))}, o); \
    asm volatile("" : "+v"(o), "+v"(bad)); }
#define MERGE() do { const float wt = dpp<0x158>(wc); o = f32x2{wt * vtf0, wt * vtf1}; bad = badw; \
    ACC(0) ACC(1) ACC(2) ACC(3) ACC(4) ACC(5) ACC(6) ACC(7) } while (0)
  if (!EARLYW) gm = __builtin_amdgcn_raw_buffer_load_b64(rw, mlo, mlb, 16);
  SWEEP();
  __builtin_amdgcn_raw_buffer_store_b32(tag, rw, epo, 0, 16);
  SB();
  if (!EARLYW) WEIGHTS();
  MERGE();
  if (!__all(bad == 0)) {
#pragma nounroll
    for (unsigned spins = 0; spins < (1u << 20); ++spins) {
      asm volatile("" ::: "memory");
      gm = __builtin_amdgcn_raw_buffer_load_b64(rw, mlo, mlb, 16);
      SWEEP(); ++nsw;
      WEIGHTS();
      MERGE();
      if (__all(bad == 0)) break;
    }
  }
  {
    const float rcv = dpp<0xB1>((lane & 1) ? o[0] : o[1]);
    const bf16x2 pk = {(__bf16)((lane & 1) ? rcv : o[0]), (__bf16)((lane & 1) ? o[1] : rcv)};
    __builtin_amdgcn_raw_buffer_store_b32(__builtin_bit_cast(unsigned, pk), ro, op, 0, OAUX);
  }
  DBEND(nsw, 1u);
  AMPEND(1);
}

extern "C" void kernel_launch(void* const* d_in, const int* in_sizes, int n_in, void* d_out, int out_size, void* d_ws,
                              size_t ws_size, hipStream_t stream) {
  attn_fused<<<H * SPLITS, WAVES * 64, 0, stream>>>(d_in[1], d_in[2], (const u32x4*)d_in[0], (unsigned char*)d_ws,
                                                    (const u32x4*)d_in[3], (const unsigned short*)d_in[4],
                                                    (const unsigned short*)d_in[5], (__hip_bfloat16*)d_out);
}
```

```cpp
#define EARLYW 0 AUX=3 AMP=2 AMPK=4
#define HMAP 2
#define PMASK 1
#define PVAL 0
#include <hip/hip_runtime.h>
#include <hip/hip_bf16.h>
#include <stdint.h>

#define AUX 2
#define PRE 4
#define NEAR 1
#define OAUX 0
#define MLLATE 0
#define PINB 1
#define LOC 0

constexpr int H = 32, C = 4096, D = 128, SPLITS = 8, WAVES = 8;
constexpr int NT = C / SPLITS / WAVES / 4;
constexpr float SCALE = 0.088388347648318440f, LOG2E = 1.4426950408889634f;
constexpr float C2 = SCALE * LOG2E;

using bf16x2 = __attribute__((ext_vector_type(2))) __bf16;
using bf16x8 = __attribute__((ext_vector_type(8))) __bf16;
using u32x4 = __attribute__((ext_vector_type(4))) unsigned;
using u32x2 = __attribute__((ext_vector_type(2))) unsigned;
using f32x4 = __attribute__((ext_vector_type(4))) float;
using f32x2 = __attribute__((ext_vector_type(2))) float;
typedef __attribute__((address_space(1))) unsigned long long gu64;

__device__ __forceinline__ float dot2(unsigned k, unsigned q, float acc) {
  return __builtin_amdgcn_fdot2_f32_bf16(__builtin_bit_cast(bf16x2, k), __builtin_bit_cast(bf16x2, q), acc, false);
}
template <int CTRL> __device__ __forceinline__ float dpp(float x) {
  return __builtin_bit_cast(float, __builtin_amdgcn_mov_dpp(__builtin_bit_cast(int, x), CTRL, 0xf, 0xf, true));
}
__device__ __forceinline__ float row_sum16(float x) {
  x += dpp<0x128>(x); x += dpp<0x124>(x); x += dpp<0x122>(x); x += dpp<0x121>(x);
  return x;
}
__device__ __forceinline__ float row_max16(float x) {
  asm volatile("s_nop 1\n\tv_max_f32_dpp %0, %0, %0 row_ror:8 row_mask:0xf bank_mask:0xf\n\ts_nop 1\n\t"
               "v_max_f32_dpp %0, %0, %0 row_ror:4 row_mask:0xf bank_mask:0xf\n\ts_nop 1\n\t"
               "v_max_f32_dpp %0, %0, %0 row_ror:2 row_mask:0xf bank_mask:0xf\n\ts_nop 1\n\t"
               "v_max_f32_dpp %0, %0, %0 row_ror:1 row_mask:0xf bank_mask:0xf" : "+v"(x));
  return x;
}
__device__ __forceinline__ float xrow_max(float x) {
  auto s = __builtin_amdgcn_permlane16_swap(__float_as_uint(x), __float_as_uint(x), false, false);
  x = fmaxf(__uint_as_float(s[0]), __uint_as_float(s[1]));
  auto t = __builtin_amdgcn_permlane32_swap(__float_as_uint(x), __float_as_uint(x), false, false);
  return fmaxf(__uint_as_float(t[0]), __uint_as_float(t[1]));
}
__device__ __forceinline__ float xrow_sum(float x) {
  auto s = __builtin_amdgcn_permlane16_swap(__float_as_uint(x), __float_as_uint(x), false, false);
  x = __uint_as_float(s[0]) + __uint_as_float(s[1]);
  auto t = __builtin_amdgcn_permlane32_swap(__float_as_uint(x), __float_as_uint(x), false, false);
  return __uint_as_float(t[0]) + __uint_as_float(t[1]);
}
__device__ __forceinline__ float bf2f(unsigned short h) { return __uint_as_float((unsigned)h << 16); }
__device__ __forceinline__ unsigned bfi(unsigned m, unsigned x, unsigned y) { return (x & m) | (y & ~m); }

constexpr unsigned HSTR = 16384, REC = 512, O_ML = 4096, O_EP = 5120, O_CNT = 5248, WS_BYTES = H * HSTR;
constexpr unsigned KEEP = 0xffffff00u;

#define RT(x) do { __builtin_amdgcn_sched_barrier(0); asm volatile("s_memrealtime %0\n\ts_waitcnt lgkmcnt(0)" : "=s"(x) :: "memory"); __builtin_amdgcn_sched_barrier(0); } while (0)
#define DB0()
#define DB1()
#define DBEND(sw, last)
#define AMP 0
#define AMPK 1
#define AMPEND(LAST) do { if (AMP && (AMP == 2 ? ((blockIdx.x & PMASK) == PVAL) : LAST)) { unsigned long long te, now; RT(te); \
   const unsigned long long dl = AMP == 1 ? te - ta : AMP == 2 ? t4 - ta : te - t4; const unsigned long long tgt = te + dl * AMPK; do { RT(now); } while (now < tgt); } } while (0)
#define SB() __builtin_amdgcn_sched_barrier(0)

__global__ void __launch_bounds__(WAVES * 64) attn_fused(const void* __restrict__ K, const void* __restrict__ V,
                                                         const u32x4* __restrict__ q, unsigned char* ws,
                                                         const u32x4* __restrict__ kt, const unsigned short* __restrict__ vt,
                                                         const unsigned short* __restrict__ mask, __hip_bfloat16* __restrict__ out) {
  __shared__ __attribute__((aligned(16))) float lds[WAVES * 128 + 2 * WAVES];
  unsigned long long ta = 0, t4 = 0;
  if (AMP) ta = __builtin_amdgcn_s_memrealtime();
  const int wg = blockIdx.x;
  const int head = HMAP == 0 ? (wg >> 3) : ((wg & 7) * 4 + ((wg >> 3) & 3)), split = HMAP == 0 ? (wg & 7) : (wg >> 5);
  const int tid = threadIdx.x, lane = tid & 63, wave = __builtin_amdgcn_readfirstlane(tid >> 6);
  const int c = lane & 15, g = lane >> 4;
  DB0();
  const auto rk = __builtin_amdgcn_make_buffer_rsrc((void*)K, 0, H * C * D * 2, 0x00020000);
  const auto rv = __builtin_amdgcn_make_buffer_rsrc((void*)V, 0, H * C * D * 2, 0x00020000);
  unsigned so = (unsigned)(head * 1024 + split * 128 + wave * NT) * 1024u; const unsigned vo = lane * 16u;
  if (LOC) {
    const unsigned x = LOC == 3 ? (unsigned)split : ((unsigned)__builtin_amdgcn_s_getreg(20 | (3 << 11)) & 7u);
    const unsigned long long ka = (unsigned long long)K + head * 1048576u;
    const unsigned par = ((unsigned)(ka >> 13) ^ (unsigned)(ka >> 21) ^ (x >> 2) ^ (LOC == 2)) & 1u;
    so = head * 1048576u + ((x & 3) * 32u + wave * 4u + par) * 8192u;
  }
  u32x4 k[NT], v[NT];
#define OFF(t) (LOC ? ((t) >> 3) * 16384 + ((t) & 7) * 1024 : (t) * 1024)
#define LDK(t) k[t] = __builtin_amdgcn_raw_buffer_load_b128(rk, vo, so + OFF(t), AUX)
#define LDV(t) v[t] = __builtin_amdgcn_raw_buffer_load_b128(rv, vo, so + OFF(t), AUX)
  LDK(0); LDV(0);
  const u32x4 qv = q[head * 16 + c];
  SB();
#pragma unroll
  for (int t = 1; t < NT; ++t) LDK(t);
#pragma unroll
  for (int t = 1; t < PRE; ++t) LDV(t);
  SB();
  float x[NT];
#pragma unroll
  for (int t = 0; t < NT; ++t) {
    float d = dot2(k[t][0], qv[0], 0.f);
    d = dot2(k[t][1], qv[1], d); d = dot2(k[t][2], qv[2], d); d = dot2(k[t][3], qv[3], d);
    x[t] = row_sum16(d) * C2;
  }
  float m = x[0];
#pragma unroll
  for (int t = 1; t < NT; ++t) m = fmaxf(m, x[t]);
  m = xrow_max(m);
  const int n = c & 7;
  const unsigned lomask = (c >= 8) ? 0xffff0000u : 0u;
  unsigned sel[4];
#pragma unroll
  for (int r = 0; r < 4; ++r) sel[r] = ((n >> 1) == r) ? ((n & 1) ? 0x03020c0cu : 0x0c0c0302u) : 0x0c0c0c0cu;
  float l = 0.f;
  u32x4 b[NT];
#pragma unroll
  for (int t = 0; t < NT; ++t) {
    const float p = __builtin_amdgcn_exp2f(x[t] - m);
    l += p;
    const unsigned s = __float_as_uint(p - __uint_as_float(__float_as_uint(p) & lomask));
#pragma unroll
    for (int r = 0; r < 4; ++r) b[t][r] = __builtin_amdgcn_perm(s, s, sel[r]);
    if (PINB) asm volatile("" : "+v"(b[t]));
  }
  l = xrow_sum(l);
  float* lm = lds + WAVES * 128;
  if (lane == 0) *reinterpret_cast<f32x2*>(lm + 2 * wave) = f32x2{m, l};
  SB();
#pragma unroll
  for (int t = PRE; t < NT; ++t) LDV(t);
  const int d0 = 32 * g + 16 * (c >> 3) + n;
  const auto rw = __builtin_amdgcn_make_buffer_rsrc((void*)ws, 0, wave == 0 ? WS_BYTES : 0u, 0x00020000);
  const unsigned long long wa = (unsigned long long)ws + head * HSTR;
  unsigned hb = head * HSTR + (NEAR ? ((((unsigned)(wa >> 13) ^ (unsigned)(wa >> 21) ^ ((unsigned)head >> 4)) & 1u) << 13) : 0u);
  float mx = 0.f, vtf0 = 0.f, vtf1 = 0.f;
  unsigned tag = 0;
  if (wave == 0) {
    const unsigned epoch = __builtin_amdgcn_raw_buffer_load_b32(rw, 0, hb + O_EP, 16);
    const u32x2 w0 = __builtin_amdgcn_raw_buffer_load_b64(rw, 0, hb + O_CNT, 16);
    const u32x4 ktv = kt[head * 16 + c];
    vtf0 = bf2f(vt[head * D + d0]); vtf1 = bf2f(vt[head * D + d0 + 8]);
    float d = dot2(ktv[0], qv[0], 0.f);
    d = dot2(ktv[1], qv[1], d); d = dot2(ktv[2], qv[2], d); d = dot2(ktv[3], qv[3], d);
    const float st = (row_sum16(d) * SCALE + (1.0f - bf2f(mask[0])) * -1000000000.0f) * LOG2E;
    mx = c == 8 ? st : -INFINITY;
    tag = epoch + 1u;
    if (lane == 0 && w0[1] != tag) {
      unsigned long long e = ((unsigned long long)w0[1] << 32) | w0[0];
      __hip_atomic_compare_exchange_strong((gu64*)(ws + hb + O_CNT), &e, (unsigned long long)tag << 32, __ATOMIC_RELAXED, __ATOMIC_RELAXED,
                                           __HIP_MEMORY_SCOPE_AGENT);
    }
    asm volatile("s_waitcnt vmcnt(0)" : "+v"(mx), "+v"(vtf0), "+v"(vtf1), "+v"(tag) :: "memory");
  }
  SB();
  __syncthreads();
  const f32x2 ml = *reinterpret_cast<const f32x2*>(lm + 2 * n);
  const float MW = __uint_as_float(__float_as_uint(row_max16(ml[0])) & KEEP);
  const float L = __uint_as_float(__float_as_uint(row_sum16(__builtin_amdgcn_exp2f(ml[0] - MW) * ml[1]) * 0.5f) & KEEP);
  float fw = __builtin_amdgcn_exp2f(m - MW);
  unsigned tg = tag & 0xffu;
  unsigned vo8 = lane * 8u;
  u32x2 gml = {__float_as_uint(MW) | tg, __float_as_uint(L) | tg};
  unsigned mo = lane < 16 ? vo8 : WS_BYTES, mlm = hb + O_ML + split * 128;
  if (!MLLATE) __builtin_amdgcn_raw_buffer_store_b64(gml, rw, mo, mlm, 16);
  else asm volatile("" : "+v"(gml), "+v"(mo), "+s"(mlm));
  const bool oth = c < 8 && c != split;
  float lx = c == split ? L : 1.0f;
  mx = c == split ? MW : mx;
  unsigned keepml = oth ? KEEP : 0u, mlo = oth ? n * 128u + g * 8u : WS_BYTES;
  unsigned ato = lane == 0 ? hb + O_CNT : WS_BYTES, epo = lane == 0 ? hb + O_EP : WS_BYTES;
  unsigned op = (head * D + d0 + ((lane & 1) ? 7 : 0)) * 2u;
  int old = 1;
  unsigned ks[SPLITS], sb[SPLITS];
#pragma unroll
  for (int s = 0; s < SPLITS; ++s) {
    ks[s] = s == split ? 0u : KEEP; sb[s] = s == split ? WS_BYTES : hb + s * REC;
    asm volatile("" : "+s"(ks[s]), "+s"(sb[s]));
  }
  unsigned mine = hb + split * REC, mlb = hb + O_ML;
  const auto ro = __builtin_amdgcn_make_buffer_rsrc((void*)out, 0, H * D * 2, 0x00020000);
  asm volatile("" : "+v"(fw), "+v"(lx), "+v"(mx), "+v"(keepml), "+v"(vo8), "+v"(mlo), "+v"(ato), "+v"(epo), "+v"(tg), "+v"(old), "+v"(op));
  asm volatile("" : "+s"(mine), "+s"(mlb) : "s"(ro));
  SB();
  f32x4 acc = {0.f, 0.f, 0.f, 0.f};
#pragma unroll
  for (int t = 0; t < NT; ++t)
    acc = __builtin_amdgcn_mfma_f32_16x16x32_bf16(__builtin_bit_cast(bf16x8, v[t]), __builtin_bit_cast(bf16x8, b[t]), acc, 0, 0, 0);
#pragma unroll
  for (int r = 0; r < 4; ++r) acc[r] += dpp<0x128>(acc[r]);
  f32x2 o2 = {c < 8 ? acc[0] : acc[2], c < 8 ? acc[1] : acc[3]};
  o2 *= fw;
  *reinterpret_cast<f32x2*>(lds + wave * 128 + lane * 2) = o2;
  u32x2 gm;
  if (EARLYW) gm = __builtin_amdgcn_raw_buffer_load_b64(rw, mlo, mlb, 16);
  __syncthreads();
  if (wave != 0) return;
  DB1();
  if (AMP > 1) t4 = __builtin_amdgcn_s_memrealtime();
  old = __builtin_amdgcn_raw_ptr_buffer_atomic_add_i32(old, rw, ato, 0, 0);
  if (MLLATE == 2) __builtin_amdgcn_raw_buffer_store_b64(gml, rw, mo, mlm, 16);
  SB();
  f32x2 ow[WAVES];
#pragma unroll
  for (int w = 0; w < WAVES; ++w) ow[w] = *reinterpret_cast<const f32x2*>(reinterpret_cast<const char*>(lds) + vo8 + w * 512);
#pragma unroll
  for (int s2 = 1; s2 < WAVES; s2 *= 2)
#pragma unroll
    for (int w = 0; w < WAVES; w += 2 * s2) ow[w] += ow[w + s2];
  const unsigned oc0 = __float_as_uint(ow[0][0]) & KEEP, oc1 = __float_as_uint(ow[0][1]) & KEEP;
  __builtin_amdgcn_raw_buffer_store_b64(u32x2{oc0 | tg, oc1 | tg}, rw, vo8, mine, 16);
  if (MLLATE == 1) __builtin_amdgcn_raw_buffer_store_b64(gml, rw, mo, mlm, 16);
  SB();
  float wc;
  unsigned badw;
#define WEIGHTS() do { badw = ((gm[0] ^ tg) | (gm[1] ^ tg)) & (keepml >> 24); \
    const float mc = __uint_as_float(bfi(keepml, gm[0], __float_as_uint(mx))), lc = __uint_as_float(bfi(keepml, gm[1], __float_as_uint(lx))); \
    wc = __builtin_amdgcn_exp2f(mc - row_max16(mc)); wc *= __builtin_amdgcn_rcpf(row_sum16(wc * lc)); \
    asm volatile("" : "+v"(wc), "+v"(badw)); } while (0)
  if (EARLYW) WEIGHTS();
  SB();
  if (__builtin_amdgcn_readfirstlane(old) != SPLITS - 1) { DBEND(0u, 0u); AMPEND(0); return; }
  u32x2 gs[SPLITS];
  f32x2 o;
  unsigned bad, nsw = 1;
#define SWEEP() do { _Pragma("unroll") for (int s = 0; s < SPLITS; ++s) gs[s] = __builtin_amdgcn_raw_buffer_load_b64(rw, vo8, sb[s], 16); } while (0)
#define ACC(S) { asm volatile("" : "+v"(gs[S])); const float w_ = dpp<0x150 + S>(wc); \
    bad |= ((gs[S][0] ^ tg) | (gs[S][1] ^ tg)) & (ks[S] >> 24); \
    o = __builtin_elementwise_fma(f32x2{w_, w_}, f32x2{__uint_as_float(bfi(ks[S], gs[S][0], oc0)), __uint_as_float(bfi(ks[S], gs[S][1], oc1))}, o); \
    asm volatile("" : "+v"(o), "+v"(bad)); }
#define MERGE() do { const float wt = dpp<0x158>(wc); o = f32x2{wt * vtf0, wt * vtf1}; bad = badw; \
    ACC(0) ACC(1) ACC(2) ACC(3) ACC(4) ACC(5) ACC(6) ACC(7) } while (0)
  if (!EARLYW) gm = __builtin_amdgcn_raw_buffer_load_b64(rw, mlo, mlb, 16);
  SWEEP();
  __builtin_amdgcn_raw_buffer_store_b32(tag, rw, epo, 0, 16);
  SB();
  if (!EARLYW) WEIGHTS();
  MERGE();
  if (!__all(bad == 0)) {
#pragma nounroll
    for (unsigned spins = 0; spins < (1u << 20); ++spins) {
      asm volatile("" ::: "memory");
      gm = __builtin_amdgcn_raw_buffer_load_b64(rw, mlo, mlb, 16);
      SWEEP(); ++nsw;
      WEIGHTS();
      MERGE();
      if (__all(bad == 0)) break;
    }
  }
  {
    const float rcv = dpp<0xB1>((lane & 1) ? o[0] : o[1]);
    const bf16x2 pk = {(__bf16)((lane & 1) ? rcv : o[0]), (__bf16)((lane & 1) ? o[1] : rcv)};
    __builtin_amdgcn_raw_buffer_store_b32(__builtin_bit_cast(unsigned, pk), ro, op, 0, OAUX);
  }
  DBEND(nsw, 1u);
  AMPEND(1);
}

extern "C" void kernel_launch(void* const* d_in, const int* in_sizes, int n_in, void* d_out, int out_size, void* d_ws,
                              size_t ws_size, hipStream_t stream) {
  attn_fused<<<H * SPLITS, WAVES * 64, 0, stream>>>(d_in[1], d_in[2], (const u32x4*)d_in[0], (unsigned char*)d_ws,
                                                    (const u32x4*)d_in[3], (const unsigned short*)d_in[4],
                                                    (const unsigned short*)d_in[5], (__hip_bfloat16*)d_out);
}
```

```cpp
#define EARLYW 0 AUX=3 NEAR=0 AMP=1 AMPK=4
#define HMAP 4
#define SKEW 4
#define CPF 6
#include <hip/hip_runtime.h>
#include <hip/hip_bf16.h>
#include <stdint.h>

#define AUX 2
#define PRE 4
#define NEAR 1
#define OAUX 0
#define MLLATE 0
#define PINB 1
#define LOC 0
#define XLAB 0
#define TIGHT 1

constexpr int H = 32, C = 4096, D = 128, SPLITS = 8, WAVES = 8;
constexpr int NT = SKEW ? 17 : 16;
constexpr float SCALE = 0.088388347648318440f, LOG2E = 1.4426950408889634f;
constexpr float C2 = SCALE * LOG2E;

using bf16x2 = __attribute__((ext_vector_type(2))) __bf16;
using bf16x8 = __attribute__((ext_vector_type(8))) __bf16;
using u32x4 = __attribute__((ext_vector_type(4))) unsigned;
using u32x2 = __attribute__((ext_vector_type(2))) unsigned;
using f32x4 = __attribute__((ext_vector_type(4))) float;
using f32x2 = __attribute__((ext_vector_type(2))) float;
typedef __attribute__((address_space(1))) unsigned long long gu64;
using i32x8 = __attribute__((ext_vector_type(8))) int;
using i32x4 = __attribute__((ext_vector_type(4))) int;
__device__ i32x4 buf_load16(i32x4 rsrc, int voffset, int soffset, int aux) __asm("llvm.amdgcn.raw.buffer.load.v4i32");

__device__ __forceinline__ float dot2(unsigned k, unsigned q, float acc) {
  return __builtin_amdgcn_fdot2_f32_bf16(__builtin_bit_cast(bf16x2, k), __builtin_bit_cast(bf16x2, q), acc, false);
}
template <int CTRL> __device__ __forceinline__ float dpp(float x) {
  return __builtin_bit_cast(float, __builtin_amdgcn_mov_dpp(__builtin_bit_cast(int, x), CTRL, 0xf, 0xf, true));
}
__device__ __forceinline__ float row_sum16(float x) {
  x += dpp<0x128>(x); x += dpp<0x124>(x); x += dpp<0x122>(x); x += dpp<0x121>(x);
  return x;
}
__device__ __forceinline__ float row_max16(float x) {
  asm volatile("s_nop 1\n\tv_max_f32_dpp %0, %0, %0 row_ror:8 row_mask:0xf bank_mask:0xf\n\ts_nop 1\n\t"
               "v_max_f32_dpp %0, %0, %0 row_ror:4 row_mask:0xf bank_mask:0xf\n\ts_nop 1\n\t"
               "v_max_f32_dpp %0, %0, %0 row_ror:2 row_mask:0xf bank_mask:0xf\n\ts_nop 1\n\t"
               "v_max_f32_dpp %0, %0, %0 row_ror:1 row_mask:0xf bank_mask:0xf" : "+v"(x));
  return x;
}
__device__ __forceinline__ float xrow_max(float x) {
  auto s = __builtin_amdgcn_permlane16_swap(__float_as_uint(x), __float_as_uint(x), false, false);
  x = fmaxf(__uint_as_float(s[0]), __uint_as_float(s[1]));
  auto t = __builtin_amdgcn_permlane32_swap(__float_as_uint(x), __float_as_uint(x), false, false);
  return fmaxf(__uint_as_float(t[0]), __uint_as_float(t[1]));
}
__device__ __forceinline__ float xrow_sum(float x) {
  auto s = __builtin_amdgcn_permlane16_swap(__float_as_uint(x), __float_as_uint(x), false, false);
  x = __uint_as_float(s[0]) + __uint_as_float(s[1]);
  auto t = __builtin_amdgcn_permlane32_swap(__float_as_uint(x), __float_as_uint(x), false, false);
  return __uint_as_float(t[0]) + __uint_as_float(t[1]);
}
__device__ __forceinline__ float bf2f(unsigned short h) { return __uint_as_float((unsigned)h << 16); }
__device__ __forceinline__ unsigned bfi(unsigned m, unsigned x, unsigned y) { return (x & m) | (y & ~m); }

constexpr unsigned HSTR = 16384, REC = 512, O_ML = 4096, O_EP = 5120, O_CNT = 5248, WS_BYTES = H * HSTR;
constexpr unsigned KEEP = 0xffffff00u;

#define RT(x) do { __builtin_amdgcn_sched_barrier(0); asm volatile("s_memrealtime %0\n\ts_waitcnt lgkmcnt(0)" : "=s"(x) :: "memory"); __builtin_amdgcn_sched_barrier(0); } while (0)
#define DB0()
#define DB1()
#define DBEND(sw, last)
#define AMP 0
#define AMPK 1
#define PMASK 0
#define PVAL 0
#define AMPEND(LAST) do { if (AMP && (AMP == 2 ? ((blockIdx.x & PMASK) == PVAL) : (LAST) != 0)) { unsigned long long te, now; RT(te); \
   const unsigned long long dl = AMP == 1 ? te - ta : AMP == 2 ? t4 - ta : te - t4; const unsigned long long tgt = te + dl * AMPK; do { RT(now); } while (now < tgt); } } while (0)
#define SB() __builtin_amdgcn_sched_barrier(0)

__global__ void __launch_bounds__(WAVES * 64) attn_fused(const void* K, unsigned kn, unsigned kf, const void* V, unsigned vn, unsigned vf,
                                                         const u32x4* __restrict__ q, unsigned char* ws,
                                                         const u32x4* __restrict__ kt, const unsigned short* __restrict__ vt,
                                                         const unsigned short* __restrict__ mask, __hip_bfloat16* __restrict__ out) {
  __shared__ __attribute__((aligned(16))) float lds[WAVES * 128 + 2 * WAVES];
  i32x8 ka; unsigned long long qa;
  asm volatile("s_load_dwordx8 %0, %2, 0x0\n\ts_load_dwordx2 %1, %2, 0x20" : "=&s"(ka), "=&s"(qa) : "s"(__builtin_amdgcn_kernarg_segment_ptr()));
  SB();
  unsigned long long ta = 0, t4 = 0;
  if (AMP) asm volatile("s_memrealtime %0" : "=s"(ta));
  SB();
  const int wg = blockIdx.x;
  const int wl = XLAB ? ((wg & ~7) | ((int)__builtin_amdgcn_s_getreg(20 | (3 << 11)) & 7)) : wg;
  const int head = HMAP == 0 ? (wl >> 3) : HMAP == 2 ? ((wl & 7) * 4 + ((wl >> 3) & 3)) : (((wl & 6) << 2) | ((wl >> 3) & 7));
  const int split = HMAP == 0 ? (wl & 7) : HMAP == 2 ? (wl >> 5) : (((wl >> 6) << 1) | (wl & 1));
  const int tid = threadIdx.x, lane = tid & 63, wave = __builtin_amdgcn_readfirstlane(tid >> 6);
  const int c = lane & 15, g = lane >> 4;
  DB0();
  const unsigned odd = split & 1;
  constexpr auto tab = [](int o) { unsigned t = 0; for (int w = 0; w < 8; ++w) t |= (unsigned)(o ? SKEW - (w > WAVES - SKEW ? w - (WAVES - SKEW) : 0) : (w < SKEW ? w : SKEW)) << (4 * w); return t; };
  const unsigned g0 = split * 128 + wave * 16 + (((odd ? tab(1) : tab(0)) >> (4 * wave)) & 15u);
  unsigned so = (head * 1024 + g0) * 1024u, vo = lane * 16u;
  unsigned qo = (head * 16 + c) * 16u;
  u32x4 cpf = {0u, 0u, 0u, 0u};
  if (CPF) {
    const unsigned long long pc = __builtin_amdgcn_s_getpc() + (wave < CPF ? wave : 0) * 1024u;
    asm volatile("global_load_dwordx4 %0, %1, %2" : "=v"(cpf) : "v"(vo), "s"(pc) : "memory");
  }
  asm volatile("" : "+s"(so), "+v"(vo), "+v"(qo));
  SB();
  asm volatile("s_waitcnt lgkmcnt(0)" : "+s"(ka), "+s"(qa), "+s"(ta));
  const i32x4 rk = __builtin_shufflevector(ka, ka, 0, 1, 2, 3), rv = __builtin_shufflevector(ka, ka, 4, 5, 6, 7);
  u32x4 k[NT], v[NT];
#define LDX(r, t) __builtin_bit_cast(u32x4, (t) < 15 || !SKEW ? buf_load16(r, vo + ((t) & 3) * 1024, so + ((t) >> 2) * 4096, AUX) : buf_load16(r, vo, (t) == 15 ? so15 : so16, AUX))
#define LDK(t) k[t] = LDX(rk, t)
#define LDV(t) v[t] = LDX(rv, t)
  unsigned so15 = 0, so16 = 0;
  LDK(0); LDV(0);
  const u32x4 qv = *(const __attribute__((address_space(1))) u32x4*)(qa + qo);
  LDK(1); LDK(2); LDK(3);
  SB();
  const bool has16 = !odd || wave < WAVES - SKEW, has17 = !odd && wave < SKEW;
  so15 = has16 ? so + 15 * 1024 : 0x7fff0000u; so16 = has17 ? so + 16 * 1024 : 0x7fff0000u;
  if (SKEW) { LDK(15); LDK(16); }
#pragma unroll
  for (int t = 4; t < (SKEW ? 15 : 16); ++t) LDK(t);
  if (SKEW) { LDV(15); LDV(16); }
#pragma unroll
  for (int t = 1; t < PRE; ++t) LDV(t);
  SB();
  float x[NT];
#pragma unroll
  for (int t = 0; t < NT; ++t) {
    float d = dot2(k[t][0], qv[0], 0.f);
    d = dot2(k[t][1], qv[1], d); d = dot2(k[t][2], qv[2], d); d = dot2(k[t][3], qv[3], d);
    x[t] = row_sum16(d) * C2;
  }
  if (SKEW) { x[15] = has16 ? x[15] : -INFINITY; x[NT - 1] = has17 ? x[NT - 1] : -INFINITY; }
  if (CPF) asm volatile("" : "+v"(cpf));
  float m = x[0];
#pragma unroll
  for (int t = 1; t < NT; ++t) m = fmaxf(m, x[t]);
  m = xrow_max(m);
  const int n = c & 7;
  const unsigned lomask = (c >= 8) ? 0xffff0000u : 0u;
  unsigned sel[4];
#pragma unroll
  for (int r = 0; r < 4; ++r) sel[r] = ((n >> 1) == r) ? ((n & 1) ? 0x03020c0cu : 0x0c0c0302u) : 0x0c0c0c0cu;
  float l = 0.f;
  u32x4 b[NT];
#pragma unroll
  for (int t = 0; t < NT; ++t) {
    const float p = __builtin_amdgcn_exp2f(x[t] - m);
    l += p;
    const unsigned s = __float_as_uint(p - __uint_as_float(__float_as_uint(p) & lomask));
#pragma unroll
    for (int r = 0; r < 4; ++r) b[t][r] = __builtin_amdgcn_perm(s, s, sel[r]);
    if (PINB) asm volatile("" : "+v"(b[t]));
  }
  l = xrow_sum(l);
  float* lm = lds + WAVES * 128;
  if (lane == 0) *reinterpret_cast<f32x2*>(lm + 2 * wave) = f32x2{m, l};
  SB();
#pragma unroll
  for (int t = PRE; t < (SKEW ? 15 : 16); ++t) { LDV(t); SB(); }
  const int d0 = 32 * g + 16 * (c >> 3) + n;
  const auto rw = __builtin_amdgcn_make_buffer_rsrc((void*)ws, 0, wave == 0 ? WS_BYTES : 0u, 0x00020000);
  const unsigned long long wa = (unsigned long long)ws + head * HSTR;
  unsigned hb = head * HSTR + (NEAR ? ((((unsigned)(wa >> 13) ^ (unsigned)(wa >> 21) ^ ((unsigned)head >> 4)) & 1u) << 13) : 0u);
  float mx = 0.f, vtf0 = 0.f, vtf1 = 0.f;
  unsigned tag = 0;
  if (wave == 0) {
    const unsigned epoch = __builtin_amdgcn_raw_buffer_load_b32(rw, 0, hb + O_EP, 16);
    const u32x2 w0 = __builtin_amdgcn_raw_buffer_load_b64(rw, 0, hb + O_CNT, 16);
    const u32x4 ktv = kt[head * 16 + c];
    vtf0 = bf2f(vt[head * D + d0]); vtf1 = bf2f(vt[head * D + d0 + 8]);
    float d = dot2(ktv[0], qv[0], 0.f);
    d = dot2(ktv[1], qv[1], d); d = dot2(ktv[2], qv[2], d); d = dot2(ktv[3], qv[3], d);
    const float st = (row_sum16(d) * SCALE + (1.0f - bf2f(mask[0])) * -1000000000.0f) * LOG2E;
    mx = c == 8 ? st : -INFINITY;
    tag = epoch + 1u;
    if (lane == 0 && w0[1] != tag) {
      unsigned long long e = ((unsigned long long)w0[1] << 32) | w0[0];
      __hip_atomic_compare_exchange_strong((gu64*)(ws + hb + O_CNT), &e, (unsigned long long)tag << 32, __ATOMIC_RELAXED, __ATOMIC_RELAXED,
                                           __HIP_MEMORY_SCOPE_AGENT);
    }
    asm volatile("s_waitcnt vmcnt(0)" : "+v"(mx), "+v"(vtf0), "+v"(vtf1), "+v"(tag) :: "memory");
  }
  SB();
  __syncthreads();
  const f32x2 ml = *reinterpret_cast<const f32x2*>(lm + 2 * n);
  const float MW = __uint_as_float(__float_as_uint(row_max16(ml[0])) & KEEP);
  const float L = __uint_as_float(__float_as_uint(row_sum16(__builtin_amdgcn_exp2f(ml[0] - MW) * ml[1]) * 0.5f) & KEEP);
  float fw = __builtin_amdgcn_exp2f(m - MW);
  unsigned tg = tag & 0xffu;
  unsigned vo8 = lane * 8u;
  u32x2 gml = {__float_as_uint(MW) | tg, __float_as_uint(L) | tg};
  unsigned mo = lane < 16 ? vo8 : WS_BYTES, mlm = hb + O_ML + split * 128;
  if (!MLLATE) __builtin_amdgcn_raw_buffer_store_b64(gml, rw, mo, mlm, 16);
  else asm volatile("" : "+v"(gml), "+v"(mo), "+s"(mlm));
  const bool oth = c < 8 && c != split;
  float lx = c == split ? L : 1.0f;
  mx = c == split ? MW : mx;
  unsigned keepml = oth ? KEEP : 0u, mlo = oth ? n * 128u + g * 8u : WS_BYTES;
  unsigned ato = lane == 0 ? hb + O_CNT : WS_BYTES, epo = lane == 0 ? hb + O_EP : WS_BYTES;
  unsigned op = (head * D + d0 + ((lane & 1) ? 7 : 0)) * 2u;
  int old = 1;
  unsigned ks[SPLITS], sb[SPLITS];
#pragma unroll
  for (int s = 0; s < SPLITS; ++s) {
    ks[s] = s == split ? 0u : KEEP; sb[s] = s == split ? WS_BYTES : hb + s * REC;
    asm volatile("" : "+s"(ks[s]), "+s"(sb[s]));
  }
  unsigned mine = hb + split * REC, mlb = hb + O_ML;
  const auto ro = __builtin_amdgcn_make_buffer_rsrc((void*)out, 0, H * D * 2, 0x00020000);
  asm volatile("" : "+v"(fw), "+v"(lx), "+v"(mx), "+v"(keepml), "+v"(vo8), "+v"(mlo), "+v"(ato), "+v"(epo), "+v"(tg), "+v"(old), "+v"(op));
  asm volatile("" : "+s"(mine), "+s"(mlb) : "s"(ro));
  SB();
  f32x4 acc = {0.f, 0.f, 0.f, 0.f};
#pragma unroll
  for (int i = 0; i < NT; ++i) {
    const int t = !SKEW || i == 0 ? i : i < 3 ? 14 + i : i - 2;
    acc = __builtin_amdgcn_mfma_f32_16x16x32_bf16(__builtin_bit_cast(bf16x8, v[t]), __builtin_bit_cast(bf16x8, b[t]), acc, 0, 0, 0);
  }
#pragma unroll
  for (int r = 0; r < 4; ++r) acc[r] += dpp<0x128>(acc[r]);
  f32x2 o2 = {c < 8 ? acc[0] : acc[2], c < 8 ? acc[1] : acc[3]};
  o2 *= fw;
  *reinterpret_cast<f32x2*>(lds + wave * 128 + lane * 2) = o2;
  u32x2 gm;
  if (EARLYW) gm = __builtin_amdgcn_raw_buffer_load_b64(rw, mlo, mlb, 16);
  __syncthreads();
  if (wave != 0) return;
  DB1();
  if (AMP > 1) t4 = __builtin_amdgcn_s_memrealtime();
  old = __builtin_amdgcn_raw_ptr_buffer_atomic_add_i32(old, rw, ato, 0, 0);
  if (MLLATE == 2) __builtin_amdgcn_raw_buffer_store_b64(gml, rw, mo, mlm, 16);
  SB();
  f32x2 ow[WAVES];
#pragma unroll
  for (int w = 0; w < WAVES; ++w) ow[w] = *reinterpret_cast<const f32x2*>(reinterpret_cast<const char*>(lds) + vo8 + w * 512);
#pragma unroll
  for (int s2 = 1; s2 < WAVES; s2 *= 2)
#pragma unroll
    for (int w = 0; w < WAVES; w += 2 * s2) ow[w] += ow[w + s2];
  const unsigned oc0 = __float_as_uint(ow[0][0]) & KEEP, oc1 = __float_as_uint(ow[0][1]) & KEEP;
  __builtin_amdgcn_raw_buffer_store_b64(u32x2{oc0 | tg, oc1 | tg}, rw, vo8, mine, 16);
  if (MLLATE == 1) __builtin_amdgcn_raw_buffer_store_b64(gml, rw, mo, mlm, 16);
  SB();
  float wc;
  unsigned badw;
#define WEIGHTS() do { badw = ((gm[0] ^ tg) | (gm[1] ^ tg)) & (keepml >> 24); \
    const float mc = __uint_as_float(bfi(keepml, gm[0], __float_as_uint(mx))), lc = __uint_as_float(bfi(keepml, gm[1], __float_as_uint(lx))); \
    wc = __builtin_amdgcn_exp2f(mc - row_max16(mc)); wc *= __builtin_amdgcn_rcpf(row_sum16(wc * lc)); \
    asm volatile("" : "+v"(wc), "+v"(badw)); } while (0)
  if (EARLYW) WEIGHTS();
  SB();
  if (__builtin_amdgcn_readfirstlane(old) != SPLITS - 1) { DBEND(0u, 0u); AMPEND(0); return; }
  u32x2 gs[SPLITS];
  f32x2 o;
  unsigned bad, nsw = 1;
#define SWEEP() do { _Pragma("unroll") for (int s = 0; s < SPLITS; ++s) gs[s] = __builtin_amdgcn_raw_buffer_load_b64(rw, vo8, sb[s], 16); } while (0)
#define ACC(S) { asm volatile("" : "+v"(gs[S])); const float w_ = dpp<0x150 + S>(wc); \
    bad |= ((gs[S][0] ^ tg) | (gs[S][1] ^ tg)) & (ks[S] >> 24); \
    o = __builtin_elementwise_fma(f32x2{w_, w_}, f32x2{__uint_as_float(bfi(ks[S], gs[S][0], oc0)), __uint_as_float(bfi(ks[S], gs[S][1], oc1))}, o); \
    asm volatile("" : "+v"(o), "+v"(bad)); }
#define MERGE() do { const float wt = dpp<0x158>(wc); o = f32x2{wt * vtf0, wt * vtf1}; bad = badw; \
    ACC(0) ACC(1) ACC(2) ACC(3) ACC(4) ACC(5) ACC(6) ACC(7) } while (0)
  if (!EARLYW) gm = __builtin_amdgcn_raw_buffer_load_b64(rw, mlo, mlb, 16);
  SWEEP();
  __builtin_amdgcn_raw_buffer_store_b32(tag, rw, epo, 0, 16);
  SB();
  if (!EARLYW) WEIGHTS();
  MERGE();
  if (!__all(bad == 0)) {
#pragma nounroll
    for (unsigned spins = 0; spins < (1u << 20); ++spins) {
      asm volatile("" ::: "memory");
      gm = __builtin_amdgcn_raw_buffer_load_b64(rw, mlo, mlb, 16);
      SWEEP(); ++nsw;
      WEIGHTS();
      MERGE();
      if (__all(bad == 0)) break;
    }
  }
  {
    const float rcv = dpp<0xB1>((lane & 1) ? o[0] : o[1]);
    const bf16x2 pk = {(__bf16)((lane & 1) ? rcv : o[0]), (__bf16)((lane & 1) ? o[1] : rcv)};
    __builtin_amdgcn_raw_buffer_store_b32(__builtin_bit_cast(unsigned, pk), ro, op, 0, OAUX);
  }
  DBEND(nsw, 1u);
  AMPEND(1);
}

extern "C" void kernel_launch(void* const* d_in, const int* in_sizes, int n_in, void* d_out, int out_size, void* d_ws,
                              size_t ws_size, hipStream_t stream) {
  attn_fused<<<H * SPLITS, WAVES * 64, 0, stream>>>(d_in[1], H * C * D * 2, 0x00020000u, d_in[2], H * C * D * 2, 0x00020000u, (const u32x4*)d_in[0], (unsigned char*)d_ws,
                                                    (const u32x4*)d_in[3], (const unsigned short*)d_in[4],
                                                    (const unsigned short*)d_in[5], (__hip_bfloat16*)d_out);
}
```

```cpp
#define EARLYW 0 AUX=3 NEAR=0 HMAP=0 CPF=0 SKEW=6
#define OAUX 17
#include <hip/hip_runtime.h>
#include <hip/hip_bf16.h>
#include <stdint.h>

#define AUX 2
#define HMAP 2
#define PRE 4
#define NEAR 1
#define MLLATE 0
#define PINB 1
#define LOC 0
#define SKEW 4
#define XLAB 0
#define CPF 0
#define TIGHT 1

constexpr int H = 32, C = 4096, D = 128, SPLITS = 8, WAVES = 8;
constexpr int NT = SKEW ? 17 : 16;
constexpr float SCALE = 0.088388347648318440f, LOG2E = 1.4426950408889634f;
constexpr float C2 = SCALE * LOG2E;

using bf16x2 = __attribute__((ext_vector_type(2))) __bf16;
using bf16x8 = __attribute__((ext_vector_type(8))) __bf16;
using u32x4 = __attribute__((ext_vector_type(4))) unsigned;
using u32x2 = __attribute__((ext_vector_type(2))) unsigned;
using f32x4 = __attribute__((ext_vector_type(4))) float;
using f32x2 = __attribute__((ext_vector_type(2))) float;
typedef __attribute__((address_space(1))) unsigned long long gu64;
using i32x8 = __attribute__((ext_vector_type(8))) int;
using i32x4 = __attribute__((ext_vector_type(4))) int;
__device__ i32x4 buf_load16(i32x4 rsrc, int voffset, int soffset, int aux) __asm("llvm.amdgcn.raw.buffer.load.v4i32");

__device__ __forceinline__ float dot2(unsigned k, unsigned q, float acc) {
  return __builtin_amdgcn_fdot2_f32_bf16(__builtin_bit_cast(bf16x2, k), __builtin_bit_cast(bf16x2, q), acc, false);
}
template <int CTRL> __device__ __forceinline__ float dpp(float x) {
  return __builtin_bit_cast(float, __builtin_amdgcn_mov_dpp(__builtin_bit_cast(int, x), CTRL, 0xf, 0xf, true));
}
__device__ __forceinline__ float row_sum16(float x) {
  x += dpp<0x128>(x); x += dpp<0x124>(x); x += dpp<0x122>(x); x += dpp<0x121>(x);
  return x;
}
__device__ __forceinline__ float row_max16(float x) {
  asm volatile("s_nop 1\n\tv_max_f32_dpp %0, %0, %0 row_ror:8 row_mask:0xf bank_mask:0xf\n\ts_nop 1\n\t"
               "v_max_f32_dpp %0, %0, %0 row_ror:4 row_mask:0xf bank_mask:0xf\n\ts_nop 1\n\t"
               "v_max_f32_dpp %0, %0, %0 row_ror:2 row_mask:0xf bank_mask:0xf\n\ts_nop 1\n\t"
               "v_max_f32_dpp %0, %0, %0 row_ror:1 row_mask:0xf bank_mask:0xf" : "+v"(x));
  return x;
}
__device__ __forceinline__ float xrow_max(float x) {
  auto s = __builtin_amdgcn_permlane16_swap(__float_as_uint(x), __float_as_uint(x), false, false);
  x = fmaxf(__uint_as_float(s[0]), __uint_as_float(s[1]));
  auto t = __builtin_amdgcn_permlane32_swap(__float_as_uint(x), __float_as_uint(x), false, false);
  return fmaxf(__uint_as_float(t[0]), __uint_as_float(t[1]));
}
__device__ __forceinline__ float xrow_sum(float x) {
  auto s = __builtin_amdgcn_permlane16_swap(__float_as_uint(x), __float_as_uint(x), false, false);
  x = __uint_as_float(s[0]) + __uint_as_float(s[1]);
  auto t = __builtin_amdgcn_permlane32_swap(__float_as_uint(x), __float_as_uint(x), false, false);
  return __uint_as_float(t[0]) + __uint_as_float(t[1]);
}
__device__ __forceinline__ float bf2f(unsigned short h) { return __uint_as_float((unsigned)h << 16); }
__device__ __forceinline__ unsigned bfi(unsigned m, unsigned x, unsigned y) { return (x & m) | (y & ~m); }

constexpr unsigned HSTR = 16384, REC = 512, O_ML = 4096, O_EP = 5120, O_CNT = 5248, WS_BYTES = H * HSTR;
constexpr unsigned KEEP = 0xffffff00u;

#define RT(x) do { __builtin_amdgcn_sched_barrier(0); asm volatile("s_memrealtime %0\n\ts_waitcnt lgkmcnt(0)" : "=s"(x) :: "memory"); __builtin_amdgcn_sched_barrier(0); } while (0)
#define DB0()
#define DB1()
#define DBEND(sw, last)
#define AMP 0
#define AMPK 1
#define PMASK 0
#define PVAL 0
#define AMPEND(LAST) do { if (AMP && (AMP == 2 ? ((blockIdx.x & PMASK) == PVAL) : (LAST) != 0)) { unsigned long long te, now; RT(te); \
   const unsigned long long dl = AMP == 1 ? te - ta : AMP == 2 ? t4 - ta : te - t4; const unsigned long long tgt = te + dl * AMPK; do { RT(now); } while (now < tgt); } } while (0)
#define SB() __builtin_amdgcn_sched_barrier(0)

__global__ void __launch_bounds__(WAVES * 64) attn_fused(const void* K, unsigned kn, unsigned kf, const void* V, unsigned vn, unsigned vf,
                                                         const u32x4* __restrict__ q, unsigned char* ws,
                                                         const u32x4* __restrict__ kt, const unsigned short* __restrict__ vt,
                                                         const unsigned short* __restrict__ mask, __hip_bfloat16* __restrict__ out) {
  __shared__ __attribute__((aligned(16))) float lds[WAVES * 128 + 2 * WAVES];
  i32x8 ka; unsigned long long qa;
  asm volatile("s_load_dwordx8 %0, %2, 0x0\n\ts_load_dwordx2 %1, %2, 0x20" : "=&s"(ka), "=&s"(qa) : "s"(__builtin_amdgcn_kernarg_segment_ptr()));
  SB();
  unsigned long long ta = 0, t4 = 0;
  if (AMP) asm volatile("s_memrealtime %0" : "=s"(ta));
  SB();
  const int wg = blockIdx.x;
  const int wl = XLAB ? ((wg & ~7) | ((int)__builtin_amdgcn_s_getreg(20 | (3 << 11)) & 7)) : wg;
  const int head = HMAP == 0 ? (wl >> 3) : HMAP == 2 ? ((wl & 7) * 4 + ((wl >> 3) & 3)) : (((wl & 6) << 2) | ((wl >> 3) & 7));
  const int split = HMAP == 0 ? (wl & 7) : HMAP == 2 ? (wl >> 5) : (((wl >> 6) << 1) | (wl & 1));
  const int tid = threadIdx.x, lane = tid & 63, wave = __builtin_amdgcn_readfirstlane(tid >> 6);
  const int c = lane & 15, g = lane >> 4;
  DB0();
  const unsigned odd = split & 1;
  constexpr auto tab = [](int o) { unsigned t = 0; for (int w = 0; w < 8; ++w) t |= (unsigned)(o ? SKEW - (w > WAVES - SKEW ? w - (WAVES - SKEW) : 0) : (w < SKEW ? w : SKEW)) << (4 * w); return t; };
  const unsigned g0 = split * 128 + wave * 16 + (((odd ? tab(1) : tab(0)) >> (4 * wave)) & 15u);
  unsigned so = (head * 1024 + g0) * 1024u, vo = lane * 16u;
  unsigned qo = (head * 16 + c) * 16u;
  u32x4 cpf = {0u, 0u, 0u, 0u};
  if (CPF) {
    const unsigned long long pc = __builtin_amdgcn_s_getpc() + (wave < CPF ? wave : 0) * 1024u;
    asm volatile("global_load_dwordx4 %0, %1, %2" : "=v"(cpf) : "v"(vo), "s"(pc) : "memory");
  }
  asm volatile("" : "+s"(so), "+v"(vo), "+v"(qo));
  SB();
  asm volatile("s_waitcnt lgkmcnt(0)" : "+s"(ka), "+s"(qa), "+s"(ta));
  const i32x4 rk = __builtin_shufflevector(ka, ka, 0, 1, 2, 3), rv = __builtin_shufflevector(ka, ka, 4, 5, 6, 7);
  u32x4 k[NT], v[NT];
#define LDX(r, t) __builtin_bit_cast(u32x4, (t) < 15 || !SKEW ? buf_load16(r, vo + ((t) & 3) * 1024, so + ((t) >> 2) * 4096, AUX) : buf_load16(r, vo, (t) == 15 ? so15 : so16, AUX))
#define LDK(t) k[t] = LDX(rk, t)
#define LDV(t) v[t] = LDX(rv, t)
  unsigned so15 = 0, so16 = 0;
  LDK(0); LDV(0);
  const u32x4 qv = *(const __attribute__((address_space(1))) u32x4*)(qa + qo);
  LDK(1); LDK(2); LDK(3);
  SB();
  const bool has16 = !odd || wave < WAVES - SKEW, has17 = !odd && wave < SKEW;
  so15 = has16 ? so + 15 * 1024 : 0x7fff0000u; so16 = has17 ? so + 16 * 1024 : 0x7fff0000u;
  if (SKEW) { LDK(15); LDK(16); }
#pragma unroll
  for (int t = 4; t < (SKEW ? 15 : 16); ++t) LDK(t);
  if (SKEW) { LDV(15); LDV(16); }
#pragma unroll
  for (int t = 1; t < PRE; ++t) LDV(t);
  SB();
  float x[NT];
#pragma unroll
  for (int t = 0; t < NT; ++t) {
    float d = dot2(k[t][0], qv[0], 0.f);
    d = dot2(k[t][1], qv[1], d); d = dot2(k[t][2], qv[2], d); d = dot2(k[t][3], qv[3], d);
    x[t] = row_sum16(d) * C2;
  }
  if (SKEW) { x[15] = has16 ? x[15] : -INFINITY; x[NT - 1] = has17 ? x[NT - 1] : -INFINITY; }
  if (CPF) asm volatile("" : "+v"(cpf));
  float m = x[0];
#pragma unroll
  for (int t = 1; t < NT; ++t) m = fmaxf(m, x[t]);
  m = xrow_max(m);
  const int n = c & 7;
  const unsigned lomask = (c >= 8) ? 0xffff0000u : 0u;
  unsigned sel[4];
#pragma unroll
  for (int r = 0; r < 4; ++r) sel[r] = ((n >> 1) == r) ? ((n & 1) ? 0x03020c0cu : 0x0c0c0302u) : 0x0c0c0c0cu;
  float l = 0.f;
  u32x4 b[NT];
#pragma unroll
  for (int t = 0; t < NT; ++t) {
    const float p = __builtin_amdgcn_exp2f(x[t] - m);
    l += p;
    const unsigned s = __float_as_uint(p - __uint_as_float(__float_as_uint(p) & lomask));
#pragma unroll
    for (int r = 0; r < 4; ++r) b[t][r] = __builtin_amdgcn_perm(s, s, sel[r]);
    if (PINB) asm volatile("" : "+v"(b[t]));
  }
  l = xrow_sum(l);
  float* lm = lds + WAVES * 128;
  if (lane == 0) *reinterpret_cast<f32x2*>(lm + 2 * wave) = f32x2{m, l};
  SB();
#pragma unroll
  for (int t = PRE; t < (SKEW ? 15 : 16); ++t) { LDV(t); SB(); }
  const int d0 = 32 * g + 16 * (c >> 3) + n;
  const auto rw = __builtin_amdgcn_make_buffer_rsrc((void*)ws, 0, wave == 0 ? WS_BYTES : 0u, 0x00020000);
  const unsigned long long wa = (unsigned long long)ws + head * HSTR;
  unsigned hb = head * HSTR + (NEAR ? ((((unsigned)(wa >> 13) ^ (unsigned)(wa >> 21) ^ ((unsigned)head >> 4)) & 1u) << 13) : 0u);
  float mx = 0.f, vtf0 = 0.f, vtf1 = 0.f;
  unsigned tag = 0;
  if (wave == 0) {
    const unsigned epoch = __builtin_amdgcn_raw_buffer_load_b32(rw, 0, hb + O_EP, 16);
    const u32x2 w0 = __builtin_amdgcn_raw_buffer_load_b64(rw, 0, hb + O_CNT, 16);
    const u32x4 ktv = kt[head * 16 + c];
    vtf0 = bf2f(vt[head * D + d0]); vtf1 = bf2f(vt[head * D + d0 + 8]);
    float d = dot2(ktv[0], qv[0], 0.f);
    d = dot2(ktv[1], qv[1], d); d = dot2(ktv[2], qv[2], d); d = dot2(ktv[3], qv[3], d);
    const float st = (row_sum16(d) * SCALE + (1.0f - bf2f(mask[0])) * -1000000000.0f) * LOG2E;
    mx = c == 8 ? st : -INFINITY;
    tag = epoch + 1u;
    if (lane == 0 && w0[1] != tag) {
      unsigned long long e = ((unsigned long long)w0[1] << 32) | w0[0];
      __hip_atomic_compare_exchange_strong((gu64*)(ws + hb + O_CNT), &e, (unsigned long long)tag << 32, __ATOMIC_RELAXED, __ATOMIC_RELAXED,
                                           __HIP_MEMORY_SCOPE_AGENT);
    }
    asm volatile("s_waitcnt vmcnt(0)" : "+v"(mx), "+v"(vtf0), "+v"(vtf1), "+v"(tag) :: "memory");
  }
  SB();
  __syncthreads();
  const f32x2 ml = *reinterpret_cast<const f32x2*>(lm + 2 * n);
  const float MW = __uint_as_float(__float_as_uint(row_max16(ml[0])) & KEEP);
  const float L = __uint_as_float(__float_as_uint(row_sum16(__builtin_amdgcn_exp2f(ml[0] - MW) * ml[1]) * 0.5f) & KEEP);
  float fw = __builtin_amdgcn_exp2f(m - MW);
  unsigned tg = tag & 0xffu;
  unsigned vo8 = lane * 8u;
  u32x2 gml = {__float_as_uint(MW) | tg, __float_as_uint(L) | tg};
  unsigned mo = lane < 16 ? vo8 : WS_BYTES, mlm = hb + O_ML + split * 128;
  if (!MLLATE) __builtin_amdgcn_raw_buffer_store_b64(gml, rw, mo, mlm, 16);
  else asm volatile("" : "+v"(gml), "+v"(mo), "+s"(mlm));
  const bool oth = c < 8 && c != split;
  float lx = c == split ? L : 1.0f;
  mx = c == split ? MW : mx;
  unsigned keepml = oth ? KEEP : 0u, mlo = oth ? n * 128u + g * 8u : WS_BYTES;
  unsigned ato = lane == 0 ? hb + O_CNT : WS_BYTES, epo = lane == 0 ? hb + O_EP : WS_BYTES;
  unsigned op = (head * D + d0 + ((lane & 1) ? 7 : 0)) * 2u;
  int old = 1;
  unsigned ks[SPLITS], sb[SPLITS];
#pragma unroll
  for (int s = 0; s < SPLITS; ++s) {
    ks[s] = s == split ? 0u : KEEP; sb[s] = s == split ? WS_BYTES : hb + s * REC;
    asm volatile("" : "+s"(ks[s]), "+s"(sb[s]));
  }
  unsigned mine = hb + split * REC, mlb = hb + O_ML;
  const auto ro = __builtin_amdgcn_make_buffer_rsrc((void*)out, 0, H * D * 2, 0x00020000);
  asm volatile("" : "+v"(fw), "+v"(lx), "+v"(mx), "+v"(keepml), "+v"(vo8), "+v"(mlo), "+v"(ato), "+v"(epo), "+v"(tg), "+v"(old), "+v"(op));
  asm volatile("" : "+s"(mine), "+s"(mlb) : "s"(ro));
  SB();
  f32x4 acc = {0.f, 0.f, 0.f, 0.f};
#pragma unroll
  for (int i = 0; i < NT; ++i) {
    const int t = !SKEW || i == 0 ? i : i < 3 ? 14 + i : i - 2;
    acc = __builtin_amdgcn_mfma_f32_16x16x32_bf16(__builtin_bit_cast(bf16x8, v[t]), __builtin_bit_cast(bf16x8, b[t]), acc, 0, 0, 0);
  }
#pragma unroll
  for (int r = 0; r < 4; ++r) acc[r] += dpp<0x128>(acc[r]);
  f32x2 o2 = {c < 8 ? acc[0] : acc[2], c < 8 ? acc[1] : acc[3]};
  o2 *= fw;
  *reinterpret_cast<f32x2*>(lds + wave * 128 + lane * 2) = o2;
  u32x2 gm;
  if (EARLYW) gm = __builtin_amdgcn_raw_buffer_load_b64(rw, mlo, mlb, 16);
  __syncthreads();
  if (wave != 0) return;
  DB1();
  if (AMP > 1) t4 = __builtin_amdgcn_s_memrealtime();
  old = __builtin_amdgcn_raw_ptr_buffer_atomic_add_i32(old, rw, ato, 0, 0);
  if (MLLATE == 2) __builtin_amdgcn_raw_buffer_store_b64(gml, rw, mo, mlm, 16);
  SB();
  f32x2 ow[WAVES];
#pragma unroll
  for (int w = 0; w < WAVES; ++w) ow[w] = *reinterpret_cast<const f32x2*>(reinterpret_cast<const char*>(lds) + vo8 + w * 512);
#pragma unroll
  for (int s2 = 1; s2 < WAVES; s2 *= 2)
#pragma unroll
    for (int w = 0; w < WAVES; w += 2 * s2) ow[w] += ow[w + s2];
  const unsigned oc0 = __float_as_uint(ow[0][0]) & KEEP, oc1 = __float_as_uint(ow[0][1]) & KEEP;
  __builtin_amdgcn_raw_buffer_store_b64(u32x2{oc0 | tg, oc1 | tg}, rw, vo8, mine, 16);
  if (MLLATE == 1) __builtin_amdgcn_raw_buffer_store_b64(gml, rw, mo, mlm, 16);
  SB();
  float wc;
  unsigned badw;
#define WEIGHTS() do { badw = ((gm[0] ^ tg) | (gm[1] ^ tg)) & (keepml >> 24); \
    const float mc = __uint_as_float(bfi(keepml, gm[0], __float_as_uint(mx))), lc = __uint_as_float(bfi(keepml, gm[1], __float_as_uint(lx))); \
    wc = __builtin_amdgcn_exp2f(mc - row_max16(mc)); wc *= __builtin_amdgcn_rcpf(row_sum16(wc * lc)); \
    asm volatile("" : "+v"(wc), "+v"(badw)); } while (0)
  if (EARLYW) WEIGHTS();
  SB();
  if (__builtin_amdgcn_readfirstlane(old) != SPLITS - 1) { DBEND(0u, 0u); AMPEND(0); return; }
  u32x2 gs[SPLITS];
  f32x2 o;
  unsigned bad, nsw = 1;
#define SWEEP() do { _Pragma("unroll") for (int s = 0; s < SPLITS; ++s) gs[s] = __builtin_amdgcn_raw_buffer_load_b64(rw, vo8, sb[s], 16); } while (0)
#define ACC(S) { asm volatile("" : "+v"(gs[S])); const float w_ = dpp<0x150 + S>(wc); \
    bad |= ((gs[S][0] ^ tg) | (gs[S][1] ^ tg)) & (ks[S] >> 24); \
    o = __builtin_elementwise_fma(f32x2{w_, w_}, f32x2{__uint_as_float(bfi(ks[S], gs[S][0], oc0)), __uint_as_float(bfi(ks[S], gs[S][1], oc1))}, o); \
    asm volatile("" : "+v"(o), "+v"(bad)); }
#define MERGE() do { const float wt = dpp<0x158>(wc); o = f32x2{wt * vtf0, wt * vtf1}; bad = badw; \
    ACC(0) ACC(1) ACC(2) ACC(3) ACC(4) ACC(5) ACC(6) ACC(7) } while (0)
  if (!EARLYW) gm = __builtin_amdgcn_raw_buffer_load_b64(rw, mlo, mlb, 16);
  SWEEP();
  __builtin_amdgcn_raw_buffer_store_b32(tag, rw, epo, 0, 16);
  SB();
  if (!EARLYW) WEIGHTS();
  MERGE();
  if (!__all(bad == 0)) {
#pragma nounroll
    for (unsigned spins = 0; spins < (1u << 20); ++spins) {
      asm volatile("" ::: "memory");
      gm = __builtin_amdgcn_raw_buffer_load_b64(rw, mlo, mlb, 16);
      SWEEP(); ++nsw;
      WEIGHTS();
      MERGE();
      if (__all(bad == 0)) break;
    }
  }
  {
    const float rcv = dpp<0xB1>((lane & 1) ? o[0] : o[1]);
    const bf16x2 pk = {(__bf16)((lane & 1) ? rcv : o[0]), (__bf16)((lane & 1) ? o[1] : rcv)};
    __builtin_amdgcn_raw_buffer_store_b32(__builtin_bit_cast(unsigned, pk), ro, op, 0, OAUX);
  }
  DBEND(nsw, 1u);
  AMPEND(1);
}

extern "C" void kernel_launch(void* const* d_in, const int* in_sizes, int n_in, void* d_out, int out_size, void* d_ws,
                              size_t ws_size, hipStream_t stream) {
  attn_fused<<<H * SPLITS, WAVES * 64, 0, stream>>>(d_in[1], H * C * D * 2, 0x00020000u, d_in[2], H * C * D * 2, 0x00020000u, (const u32x4*)d_in[0], (unsigned char*)d_ws,
                                                    (const u32x4*)d_in[3], (const unsigned short*)d_in[4],
                                                    (const unsigned short*)d_in[5], (__hip_bfloat16*)d_out);
}
```

```cpp
#define EARLYW 0 AUX=3 NEAR=0 HMAP=0 CPF=0 SKEW=6 AMPK=10
#define AMP 7
#include <hip/hip_runtime.h>
#include <hip/hip_bf16.h>
#include <stdint.h>

#define AUX 2
#define HMAP 2
#define PRE 4
#define NEAR 1
#define OAUX 0
#define MLLATE 0
#define PINB 1
#define LOC 0
#define SKEW 4
#define XLAB 0
#define CPF 0
#define TIGHT 1

constexpr int H = 32, C = 4096, D = 128, SPLITS = 8, WAVES = 8;
constexpr int NT = SKEW ? 17 : 16;
constexpr float SCALE = 0.088388347648318440f, LOG2E = 1.4426950408889634f;
constexpr float C2 = SCALE * LOG2E;

using bf16x2 = __attribute__((ext_vector_type(2))) __bf16;
using bf16x8 = __attribute__((ext_vector_type(8))) __bf16;
using u32x4 = __attribute__((ext_vector_type(4))) unsigned;
using u32x2 = __attribute__((ext_vector_type(2))) unsigned;
using f32x4 = __attribute__((ext_vector_type(4))) float;
using f32x2 = __attribute__((ext_vector_type(2))) float;
typedef __attribute__((address_space(1))) unsigned long long gu64;
using i32x8 = __attribute__((ext_vector_type(8))) int;
using i32x4 = __attribute__((ext_vector_type(4))) int;
__device__ i32x4 buf_load16(i32x4 rsrc, int voffset, int soffset, int aux) __asm("llvm.amdgcn.raw.buffer.load.v4i32");

__device__ __forceinline__ float dot2(unsigned k, unsigned q, float acc) {
  return __builtin_amdgcn_fdot2_f32_bf16(__builtin_bit_cast(bf16x2, k), __builtin_bit_cast(bf16x2, q), acc, false);
}
template <int CTRL> __device__ __forceinline__ float dpp(float x) {
  return __builtin_bit_cast(float, __builtin_amdgcn_mov_dpp(__builtin_bit_cast(int, x), CTRL, 0xf, 0xf, true));
}
__device__ __forceinline__ float row_sum16(float x) {
  x += dpp<0x128>(x); x += dpp<0x124>(x); x += dpp<0x122>(x); x += dpp<0x121>(x);
  return x;
}
__device__ __forceinline__ float row_max16(float x) {
  asm volatile("s_nop 1\n\tv_max_f32_dpp %0, %0, %0 row_ror:8 row_mask:0xf bank_mask:0xf\n\ts_nop 1\n\t"
               "v_max_f32_dpp %0, %0, %0 row_ror:4 row_mask:0xf bank_mask:0xf\n\ts_nop 1\n\t"
               "v_max_f32_dpp %0, %0, %0 row_ror:2 row_mask:0xf bank_mask:0xf\n\ts_nop 1\n\t"
               "v_max_f32_dpp %0, %0, %0 row_ror:1 row_mask:0xf bank_mask:0xf" : "+v"(x));
  return x;
}
__device__ __forceinline__ float xrow_max(float x) {
  auto s = __builtin_amdgcn_permlane16_swap(__float_as_uint(x), __float_as_uint(x), false, false);
  x = fmaxf(__uint_as_float(s[0]), __uint_as_float(s[1]));
  auto t = __builtin_amdgcn_permlane32_swap(__float_as_uint(x), __float_as_uint(x), false, false);
  return fmaxf(__uint_as_float(t[0]), __uint_as_float(t[1]));
}
__device__ __forceinline__ float xrow_sum(float x) {
  auto s = __builtin_amdgcn_permlane16_swap(__float_as_uint(x), __float_as_uint(x), false, false);
  x = __uint_as_float(s[0]) + __uint_as_float(s[1]);
  auto t = __builtin_amdgcn_permlane32_swap(__float_as_uint(x), __float_as_uint(x), false, false);
  return __uint_as_float(t[0]) + __uint_as_float(t[1]);
}
__device__ __forceinline__ float bf2f(unsigned short h) { return __uint_as_float((unsigned)h << 16); }
__device__ __forceinline__ unsigned bfi(unsigned m, unsigned x, unsigned y) { return (x & m) | (y & ~m); }

constexpr unsigned HSTR = 16384, REC = 512, O_ML = 4096, O_EP = 5120, O_CNT = 5248, WS_BYTES = H * HSTR;
constexpr unsigned KEEP = 0xffffff00u;

#define RT(x) do { __builtin_amdgcn_sched_barrier(0); asm volatile("s_memrealtime %0\n\ts_waitcnt lgkmcnt(0)" : "=s"(x) :: "memory"); __builtin_amdgcn_sched_barrier(0); } while (0)
#define DB0()
#define DB1()
#define DBEND(sw, last)
#define PMASK 0
#define PVAL 0
#define AMPEND(LAST) do { if (AMP && (AMP == 2 || AMP >= 5 ? ((blockIdx.x & PMASK) == PVAL) : (LAST) != 0)) { unsigned long long te, now; RT(te); \
   const unsigned long long dl = AMP == 1 ? te - ta : AMP == 2 ? t4 - ta : AMP >= 5 ? tb - ta : te - t4; const unsigned long long tgt = te + dl * AMPK; do { RT(now); } while (now < tgt); } } while (0)
#define SB() __builtin_amdgcn_sched_barrier(0)

__global__ void __launch_bounds__(WAVES * 64) attn_fused(const void* K, unsigned kn, unsigned kf, const void* V, unsigned vn, unsigned vf,
                                                         const u32x4* __restrict__ q, unsigned char* ws,
                                                         const u32x4* __restrict__ kt, const unsigned short* __restrict__ vt,
                                                         const unsigned short* __restrict__ mask, __hip_bfloat16* __restrict__ out) {
  __shared__ __attribute__((aligned(16))) float lds[WAVES * 128 + 2 * WAVES];
  i32x8 ka; unsigned long long qa;
  asm volatile("s_load_dwordx8 %0, %2, 0x0\n\ts_load_dwordx2 %1, %2, 0x20" : "=&s"(ka), "=&s"(qa) : "s"(__builtin_amdgcn_kernarg_segment_ptr()));
  SB();
  unsigned long long ta = 0, t4 = 0, tb = 0;
  if (AMP) asm volatile("s_memrealtime %0" : "=s"(ta));
  SB();
  const int wg = blockIdx.x;
  const int wl = XLAB ? ((wg & ~7) | ((int)__builtin_amdgcn_s_getreg(20 | (3 << 11)) & 7)) : wg;
  const int head = HMAP == 0 ? (wl >> 3) : HMAP == 2 ? ((wl & 7) * 4 + ((wl >> 3) & 3)) : (((wl & 6) << 2) | ((wl >> 3) & 7));
  const int split = HMAP == 0 ? (wl & 7) : HMAP == 2 ? (wl >> 5) : (((wl >> 6) << 1) | (wl & 1));
  const int tid = threadIdx.x, lane = tid & 63, wave = __builtin_amdgcn_readfirstlane(tid >> 6);
  const int c = lane & 15, g = lane >> 4;
  DB0();
  const unsigned odd = split & 1;
  constexpr auto tab = [](int o) { unsigned t = 0; for (int w = 0; w < 8; ++w) t |= (unsigned)(o ? SKEW - (w > WAVES - SKEW ? w - (WAVES - SKEW) : 0) : (w < SKEW ? w : SKEW)) << (4 * w); return t; };
  const unsigned g0 = split * 128 + wave * 16 + (((odd ? tab(1) : tab(0)) >> (4 * wave)) & 15u);
  unsigned so = (head * 1024 + g0) * 1024u, vo = lane * 16u;
  unsigned qo = (head * 16 + c) * 16u;
  u32x4 cpf = {0u, 0u, 0u, 0u};
  if (CPF) {
    const unsigned long long pc = __builtin_amdgcn_s_getpc() + (wave < CPF ? wave : 0) * 1024u;
    asm volatile("global_load_dwordx4 %0, %1, %2" : "=v"(cpf) : "v"(vo), "s"(pc) : "memory");
  }
  asm volatile("" : "+s"(so), "+v"(vo), "+v"(qo));
  SB();
  if (AMP == 7) asm volatile("s_memrealtime %0" : "=s"(tb));
  asm volatile("s_waitcnt lgkmcnt(0)" : "+s"(ka), "+s"(qa), "+s"(ta), "+s"(tb));
  if (AMP == 5) asm volatile("s_memrealtime %0" : "=s"(tb));
  const i32x4 rk = __builtin_shufflevector(ka, ka, 0, 1, 2, 3), rv = __builtin_shufflevector(ka, ka, 4, 5, 6, 7);
  u32x4 k[NT], v[NT];
#define LDX(r, t) __builtin_bit_cast(u32x4, (t) < 15 || !SKEW ? buf_load16(r, vo + ((t) & 3) * 1024, so + ((t) >> 2) * 4096, AUX) : buf_load16(r, vo, (t) == 15 ? so15 : so16, AUX))
#define LDK(t) k[t] = LDX(rk, t)
#define LDV(t) v[t] = LDX(rv, t)
  unsigned so15 = 0, so16 = 0;
  LDK(0); LDV(0);
  const u32x4 qv = *(const __attribute__((address_space(1))) u32x4*)(qa + qo);
  LDK(1); LDK(2); LDK(3);
  SB();
  if (AMP == 6) asm volatile("s_memrealtime %0" : "=s"(tb));
  if (AMP == 5 || AMP == 6) asm volatile("s_waitcnt lgkmcnt(0)" : "+s"(tb));
  SB();
  const bool has16 = !odd || wave < WAVES - SKEW, has17 = !odd && wave < SKEW;
  so15 = has16 ? so + 15 * 1024 : 0x7fff0000u; so16 = has17 ? so + 16 * 1024 : 0x7fff0000u;
  if (SKEW) { LDK(15); LDK(16); }
#pragma unroll
  for (int t = 4; t < (SKEW ? 15 : 16); ++t) LDK(t);
  if (SKEW) { LDV(15); LDV(16); }
#pragma unroll
  for (int t = 1; t < PRE; ++t) LDV(t);
  SB();
  float x[NT];
#pragma unroll
  for (int t = 0; t < NT; ++t) {
    float d = dot2(k[t][0], qv[0], 0.f);
    d = dot2(k[t][1], qv[1], d); d = dot2(k[t][2], qv[2], d); d = dot2(k[t][3], qv[3], d);
    x[t] = row_sum16(d) * C2;
  }
  if (SKEW) { x[15] = has16 ? x[15] : -INFINITY; x[NT - 1] = has17 ? x[NT - 1] : -INFINITY; }
  if (CPF) asm volatile("" : "+v"(cpf));
  float m = x[0];
#pragma unroll
  for (int t = 1; t < NT; ++t) m = fmaxf(m, x[t]);
  m = xrow_max(m);
  const int n = c & 7;
  const unsigned lomask = (c >= 8) ? 0xffff0000u : 0u;
  unsigned sel[4];
#pragma unroll
  for (int r = 0; r < 4; ++r) sel[r] = ((n >> 1) == r) ? ((n & 1) ? 0x03020c0cu : 0x0c0c0302u) : 0x0c0c0c0cu;
  float l = 0.f;
  u32x4 b[NT];
#pragma unroll
  for (int t = 0; t < NT; ++t) {
    const float p = __builtin_amdgcn_exp2f(x[t] - m);
    l += p;
    const unsigned s = __float_as_uint(p - __uint_as_float(__float_as_uint(p) & lomask));
#pragma unroll
    for (int r = 0; r < 4; ++r) b[t][r] = __builtin_amdgcn_perm(s, s, sel[r]);
    if (PINB) asm volatile("" : "+v"(b[t]));
  }
  l = xrow_sum(l);
  float* lm = lds + WAVES * 128;
  if (lane == 0) *reinterpret_cast<f32x2*>(lm + 2 * wave) = f32x2{m, l};
  SB();
#pragma unroll
  for (int t = PRE; t < (SKEW ? 15 : 16); ++t) { LDV(t); SB(); }
  const int d0 = 32 * g + 16 * (c >> 3) + n;
  const auto rw = __builtin_amdgcn_make_buffer_rsrc((void*)ws, 0, wave == 0 ? WS_BYTES : 0u, 0x00020000);
  const unsigned long long wa = (unsigned long long)ws + head * HSTR;
  unsigned hb = head * HSTR + (NEAR ? ((((unsigned)(wa >> 13) ^ (unsigned)(wa >> 21) ^ ((unsigned)head >> 4)) & 1u) << 13) : 0u);
  float mx = 0.f, vtf0 = 0.f, vtf1 = 0.f;
  unsigned tag = 0;
  if (wave == 0) {
    const unsigned epoch = __builtin_amdgcn_raw_buffer_load_b32(rw, 0, hb + O_EP, 16);
    const u32x2 w0 = __builtin_amdgcn_raw_buffer_load_b64(rw, 0, hb + O_CNT, 16);
    const u32x4 ktv = kt[head * 16 + c];
    vtf0 = bf2f(vt[head * D + d0]); vtf1 = bf2f(vt[head * D + d0 + 8]);
    float d = dot2(ktv[0], qv[0], 0.f);
    d = dot2(ktv[1], qv[1], d); d = dot2(ktv[2], qv[2], d); d = dot2(ktv[3], qv[3], d);
    const float st = (row_sum16(d) * SCALE + (1.0f - bf2f(mask[0])) * -1000000000.0f) * LOG2E;
    mx = c == 8 ? st : -INFINITY;
    tag = epoch + 1u;
    if (lane == 0 && w0[1] != tag) {
      unsigned long long e = ((unsigned long long)w0[1] << 32) | w0[0];
      __hip_atomic_compare_exchange_strong((gu64*)(ws + hb + O_CNT), &e, (unsigned long long)tag << 32, __ATOMIC_RELAXED, __ATOMIC_RELAXED,
                                           __HIP_MEMORY_SCOPE_AGENT);
    }
    asm volatile("s_waitcnt vmcnt(0)" : "+v"(mx), "+v"(vtf0), "+v"(vtf1), "+v"(tag) :: "memory");
  }
  SB();
  __syncthreads();
  const f32x2 ml = *reinterpret_cast<const f32x2*>(lm + 2 * n);
  const float MW = __uint_as_float(__float_as_uint(row_max16(ml[0])) & KEEP);
  const float L = __uint_as_float(__float_as_uint(row_sum16(__builtin_amdgcn_exp2f(ml[0] - MW) * ml[1]) * 0.5f) & KEEP);
  float fw = __builtin_amdgcn_exp2f(m - MW);
  unsigned tg = tag & 0xffu;
  unsigned vo8 = lane * 8u;
  u32x2 gml = {__float_as_uint(MW) | tg, __float_as_uint(L) | tg};
  unsigned mo = lane < 16 ? vo8 : WS_BYTES, mlm = hb + O_ML + split * 128;
  if (!MLLATE) __builtin_amdgcn_raw_buffer_store_b64(gml, rw, mo, mlm, 16);
  else asm volatile("" : "+v"(gml), "+v"(mo), "+s"(mlm));
  const bool oth = c < 8 && c != split;
  float lx = c == split ? L : 1.0f;
  mx = c == split ? MW : mx;
  unsigned keepml = oth ? KEEP : 0u, mlo = oth ? n * 128u + g * 8u : WS_BYTES;
  unsigned ato = lane == 0 ? hb + O_CNT : WS_BYTES, epo = lane == 0 ? hb + O_EP : WS_BYTES;
  unsigned op = (head * D + d0 + ((lane & 1) ? 7 : 0)) * 2u;
  int old = 1;
  unsigned ks[SPLITS], sb[SPLITS];
#pragma unroll
  for (int s = 0; s < SPLITS; ++s) {
    ks[s] = s == split ? 0u : KEEP; sb[s] = s == split ? WS_BYTES : hb + s * REC;
    asm volatile("" : "+s"(ks[s]), "+s"(sb[s]));
  }
  unsigned mine = hb + split * REC, mlb = hb + O_ML;
  const auto ro = __builtin_amdgcn_make_buffer_rsrc((void*)out, 0, H * D * 2, 0x00020000);
  asm volatile("" : "+v"(fw), "+v"(lx), "+v"(mx), "+v"(keepml), "+v"(vo8), "+v"(mlo), "+v"(ato), "+v"(epo), "+v"(tg), "+v"(old), "+v"(op));
  asm volatile("" : "+s"(mine), "+s"(mlb) : "s"(ro));
  SB();
  f32x4 acc = {0.f, 0.f, 0.f, 0.f};
#pragma unroll
  for (int i = 0; i < NT; ++i) {
    const int t = !SKEW || i == 0 ? i : i < 3 ? 14 + i : i - 2;
    acc = __builtin_amdgcn_mfma_f32_16x16x32_bf16(__builtin_bit_cast(bf16x8, v[t]), __builtin_bit_cast(bf16x8, b[t]), acc, 0, 0, 0);
  }
#pragma unroll
  for (int r = 0; r < 4; ++r) acc[r] += dpp<0x128>(acc[r]);
  f32x2 o2 = {c < 8 ? acc[0] : acc[2], c < 8 ? acc[1] : acc[3]};
  o2 *= fw;
  *reinterpret_cast<f32x2*>(lds + wave * 128 + lane * 2) = o2;
  u32x2 gm;
  if (EARLYW) gm = __builtin_amdgcn_raw_buffer_load_b64(rw, mlo, mlb, 16);
  __syncthreads();
  if (wave != 0) return;
  DB1();
  if (AMP > 1) t4 = __builtin_amdgcn_s_memrealtime();
  old = __builtin_amdgcn_raw_ptr_buffer_atomic_add_i32(old, rw, ato, 0, 0);
  if (MLLATE == 2) __builtin_amdgcn_raw_buffer_store_b64(gml, rw, mo, mlm, 16);
  SB();
  f32x2 ow[WAVES];
#pragma unroll
  for (int w = 0; w < WAVES; ++w) ow[w] = *reinterpret_cast<const f32x2*>(reinterpret_cast<const char*>(lds) + vo8 + w * 512);
#pragma unroll
  for (int s2 = 1; s2 < WAVES; s2 *= 2)
#pragma unroll
    for (int w = 0; w < WAVES; w += 2 * s2) ow[w] += ow[w + s2];
  const unsigned oc0 = __float_as_uint(ow[0][0]) & KEEP, oc1 = __float_as_uint(ow[0][1]) & KEEP;
  __builtin_amdgcn_raw_buffer_store_b64(u32x2{oc0 | tg, oc1 | tg}, rw, vo8, mine, 16);
  if (MLLATE == 1) __builtin_amdgcn_raw_buffer_store_b64(gml, rw, mo, mlm, 16);
  SB();
  float wc;
  unsigned badw;
#define WEIGHTS() do { badw = ((gm[0] ^ tg) | (gm[1] ^ tg)) & (keepml >> 24); \
    const float mc = __uint_as_float(bfi(keepml, gm[0], __float_as_uint(mx))), lc = __uint_as_float(bfi(keepml, gm[1], __float_as_uint(lx))); \
    wc = __builtin_amdgcn_exp2f(mc - row_max16(mc)); wc *= __builtin_amdgcn_rcpf(row_sum16(wc * lc)); \
    asm volatile("" : "+v"(wc), "+v"(badw)); } while (0)
  if (EARLYW) WEIGHTS();
  SB();
  if (__builtin_amdgcn_readfirstlane(old) != SPLITS - 1) { DBEND(0u, 0u); AMPEND(0); return; }
  u32x2 gs[SPLITS];
  f32x2 o;
  unsigned bad, nsw = 1;
#define SWEEP() do { _Pragma("unroll") for (int s = 0; s < SPLITS; ++s) gs[s] = __builtin_amdgcn_raw_buffer_load_b64(rw, vo8, sb[s], 16); } while (0)
#define ACC(S) { asm volatile("" : "+v"(gs[S])); const float w_ = dpp<0x150 + S>(wc); \
    bad |= ((gs[S][0] ^ tg) | (gs[S][1] ^ tg)) & (ks[S] >> 24); \
    o = __builtin_elementwise_fma(f32x2{w_, w_}, f32x2{__uint_as_float(bfi(ks[S], gs[S][0], oc0)), __uint_as_float(bfi(ks[S], gs[S][1], oc1))}, o); \
    asm volatile("" : "+v"(o), "+v"(bad)); }
#define MERGE() do { const float wt = dpp<0x158>(wc); o = f32x2{wt * vtf0, wt * vtf1}; bad = badw; \
    ACC(0) ACC(1) ACC(2) ACC(3) ACC(4) ACC(5) ACC(6) ACC(7) } while (0)
  if (!EARLYW) gm = __builtin_amdgcn_raw_buffer_load_b64(rw, mlo, mlb, 16);
  SWEEP();
  __builtin_amdgcn_raw_buffer_store_b32(tag, rw, epo, 0, 16);
  SB();
  if (!EARLYW) WEIGHTS();
  MERGE();
  if (!__all(bad == 0)) {
#pragma nounroll
    for (unsigned spins = 0; spins < (1u << 20); ++spins) {
      asm volatile("" ::: "memory");
      gm = __builtin_amdgcn_raw_buffer_load_b64(rw, mlo, mlb, 16);
      SWEEP(); ++nsw;
      WEIGHTS();
      MERGE();
      if (__all(bad == 0)) break;
    }
  }
  {
    const float rcv = dpp<0xB1>((lane & 1) ? o[0] : o[1]);
    const bf16x2 pk = {(__bf16)((lane & 1) ? rcv : o[0]), (__bf16)((lane & 1) ? o[1] : rcv)};
    __builtin_amdgcn_raw_buffer_store_b32(__builtin_bit_cast(unsigned, pk), ro, op, 0, OAUX);
  }
  DBEND(nsw, 1u);
  AMPEND(1);
}

extern "C" void kernel_launch(void* const* d_in, const int* in_sizes, int n_in, void* d_out, int out_size, void* d_ws,
                              size_t ws_size, hipStream_t stream) {
  attn_fused<<<H * SPLITS, WAVES * 64, 0, stream>>>(d_in[1], H * C * D * 2, 0x00020000u, d_in[2], H * C * D * 2, 0x00020000u, (const u32x4*)d_in[0], (unsigned char*)d_ws,
                                                    (const u32x4*)d_in[3], (const unsigned short*)d_in[4],
                                                    (const unsigned short*)d_in[5], (__hip_bfloat16*)d_out);
}
```

```cpp
#define EARLYW 0 AUX=3 NEAR=0
#define HMAP 2
#define SKEW 0
#define CPF 0
#include <hip/hip_runtime.h>
#include <hip/hip_bf16.h>
#include <stdint.h>

#define AUX 2
#define PRE 4
#define NEAR 1
#define OAUX 0
#define MLLATE 0
#define PINB 1
#define LOC 0
#define XLAB 0
#define TIGHT 1

constexpr int H = 32, C = 4096, D = 128, SPLITS = 8, WAVES = 8;
constexpr int NT = SKEW ? 17 : 16;
constexpr float SCALE = 0.088388347648318440f, LOG2E = 1.4426950408889634f;
constexpr float C2 = SCALE * LOG2E;

using bf16x2 = __attribute__((ext_vector_type(2))) __bf16;
using bf16x8 = __attribute__((ext_vector_type(8))) __bf16;
using u32x4 = __attribute__((ext_vector_type(4))) unsigned;
using u32x2 = __attribute__((ext_vector_type(2))) unsigned;
using f32x4 = __attribute__((ext_vector_type(4))) float;
using f32x2 = __attribute__((ext_vector_type(2))) float;
typedef __attribute__((address_space(1))) unsigned long long gu64;
using i32x8 = __attribute__((ext_vector_type(8))) int;
using i32x4 = __attribute__((ext_vector_type(4))) int;
__device__ i32x4 buf_load16(i32x4 rsrc, int voffset, int soffset, int aux) __asm("llvm.amdgcn.raw.buffer.load.v4i32");

__device__ __forceinline__ float dot2(unsigned k, unsigned q, float acc) {
  return __builtin_amdgcn_fdot2_f32_bf16(__builtin_bit_cast(bf16x2, k), __builtin_bit_cast(bf16x2, q), acc, false);
}
template <int CTRL> __device__ __forceinline__ float dpp(float x) {
  return __builtin_bit_cast(float, __builtin_amdgcn_mov_dpp(__builtin_bit_cast(int, x), CTRL, 0xf, 0xf, true));
}
__device__ __forceinline__ float row_sum16(float x) {
  x += dpp<0x128>(x); x += dpp<0x124>(x); x += dpp<0x122>(x); x += dpp<0x121>(x);
  return x;
}
__device__ __forceinline__ float row_max16(float x) {
  asm volatile("s_nop 1\n\tv_max_f32_dpp %0, %0, %0 row_ror:8 row_mask:0xf bank_mask:0xf\n\ts_nop 1\n\t"
               "v_max_f32_dpp %0, %0, %0 row_ror:4 row_mask:0xf bank_mask:0xf\n\ts_nop 1\n\t"
               "v_max_f32_dpp %0, %0, %0 row_ror:2 row_mask:0xf bank_mask:0xf\n\ts_nop 1\n\t"
               "v_max_f32_dpp %0, %0, %0 row_ror:1 row_mask:0xf bank_mask:0xf" : "+v"(x));
  return x;
}
__device__ __forceinline__ float xrow_max(float x) {
  auto s = __builtin_amdgcn_permlane16_swap(__float_as_uint(x), __float_as_uint(x), false, false);
  x = fmaxf(__uint_as_float(s[0]), __uint_as_float(s[1]));
  auto t = __builtin_amdgcn_permlane32_swap(__float_as_uint(x), __float_as_uint(x), false, false);
  return fmaxf(__uint_as_float(t[0]), __uint_as_float(t[1]));
}
__device__ __forceinline__ float xrow_sum(float x) {
  auto s = __builtin_amdgcn_permlane16_swap(__float_as_uint(x), __float_as_uint(x), false, false);
  x = __uint_as_float(s[0]) + __uint_as_float(s[1]);
  auto t = __builtin_amdgcn_permlane32_swap(__float_as_uint(x), __float_as_uint(x), false, false);
  return __uint_as_float(t[0]) + __uint_as_float(t[1]);
}
__device__ __forceinline__ float bf2f(unsigned short h) { return __uint_as_float((unsigned)h << 16); }
__device__ __forceinline__ unsigned bfi(unsigned m, unsigned x, unsigned y) { return (x & m) | (y & ~m); }

constexpr unsigned HSTR = 16384, REC = 512, O_ML = 4096, O_EP = 5120, O_CNT = 5248, WS_BYTES = H * HSTR;
constexpr unsigned KEEP = 0xffffff00u;

#define RT(x) do { __builtin_amdgcn_sched_barrier(0); asm volatile("s_memrealtime %0\n\ts_waitcnt lgkmcnt(0)" : "=s"(x) :: "memory"); __builtin_amdgcn_sched_barrier(0); } while (0)
#define DB0()
#define DB1()
#define DBEND(sw, last)
#define AMP 0
#define AMPK 1
#define PMASK 0
#define PVAL 0
#define AMPEND(LAST) do { if (AMP && (AMP == 2 || AMP >= 5 ? ((blockIdx.x & PMASK) == PVAL) : (LAST) != 0)) { unsigned long long te, now; RT(te); \
   const unsigned long long dl = AMP == 1 ? te - ta : AMP == 2 ? t4 - ta : AMP >= 5 ? tb - ta : te - t4; const unsigned long long tgt = te + dl * AMPK; do { RT(now); } while (now < tgt); } } while (0)
#define SB() __builtin_amdgcn_sched_barrier(0)

__global__ void __launch_bounds__(WAVES * 64) attn_fused(const void* K, unsigned kn, unsigned kf, const void* V, unsigned vn, unsigned vf,
                                                         const u32x4* __restrict__ q, unsigned char* ws,
                                                         const u32x4* __restrict__ kt, const unsigned short* __restrict__ vt,
                                                         const unsigned short* __restrict__ mask, __hip_bfloat16* __restrict__ out) {
  __shared__ __attribute__((aligned(16))) float lds[WAVES * 128 + 2 * WAVES];
  i32x8 ka; unsigned long long qa;
  asm volatile("s_load_dwordx8 %0, %2, 0x0\n\ts_load_dwordx2 %1, %2, 0x20" : "=&s"(ka), "=&s"(qa) : "s"(__builtin_amdgcn_kernarg_segment_ptr()));
  SB();
  unsigned long long ta = 0, t4 = 0, tb = 0;
  if (AMP) asm volatile("s_memrealtime %0" : "=s"(ta));
  SB();
  const int wg = blockIdx.x;
  const int wl = XLAB ? ((wg & ~7) | ((int)__builtin_amdgcn_s_getreg(20 | (3 << 11)) & 7)) : wg;
  const int head = HMAP == 0 ? (wl >> 3) : HMAP == 2 ? ((wl & 7) * 4 + ((wl >> 3) & 3)) : (((wl & 6) << 2) | ((wl >> 3) & 7));
  const int split = HMAP == 0 ? (wl & 7) : HMAP == 2 ? (wl >> 5) : (((wl >> 6) << 1) | (wl & 1));
  const int tid = threadIdx.x, lane = tid & 63, wave = __builtin_amdgcn_readfirstlane(tid >> 6);
  const int c = lane & 15, g = lane >> 4;
  DB0();
  const unsigned odd = split & 1;
  constexpr auto tab = [](int o) { unsigned t = 0; for (int w = 0; w < 8; ++w) t |= (unsigned)(o ? SKEW - (w > WAVES - SKEW ? w - (WAVES - SKEW) : 0) : (w < SKEW ? w : SKEW)) << (4 * w); return t; };
  const unsigned g0 = split * 128 + wave * 16 + (((odd ? tab(1) : tab(0)) >> (4 * wave)) & 15u);
  unsigned so = (head * 1024 + g0) * 1024u, vo = lane * 16u;
  unsigned qo = (head * 16 + c) * 16u;
  u32x4 cpf = {0u, 0u, 0u, 0u};
  if (CPF) {
    const unsigned long long pc = __builtin_amdgcn_s_getpc() + (wave < CPF ? wave : 0) * 1024u;
    asm volatile("global_load_dwordx4 %0, %1, %2" : "=v"(cpf) : "v"(vo), "s"(pc) : "memory");
  }
  asm volatile("" : "+s"(so), "+v"(vo), "+v"(qo));
  SB();
  if (AMP == 7) asm volatile("s_memrealtime %0" : "=s"(tb));
  asm volatile("s_waitcnt lgkmcnt(0)" : "+s"(ka), "+s"(qa), "+s"(ta), "+s"(tb));
  if (AMP == 5) asm volatile("s_memrealtime %0" : "=s"(tb));
  const i32x4 rk = __builtin_shufflevector(ka, ka, 0, 1, 2, 3), rv = __builtin_shufflevector(ka, ka, 4, 5, 6, 7);
  u32x4 k[NT], v[NT];
#define LDX(r, t) __builtin_bit_cast(u32x4, (t) < 15 || !SKEW ? buf_load16(r, vo + ((t) & 3) * 1024, so + ((t) >> 2) * 4096, AUX) : buf_load16(r, vo, (t) == 15 ? so15 : so16, AUX))
#define LDK(t) k[t] = LDX(rk, t)
#define LDV(t) v[t] = LDX(rv, t)
  unsigned so15 = 0, so16 = 0;
  LDK(0); LDV(0);
  const u32x4 qv = *(const __attribute__((address_space(1))) u32x4*)(qa + qo);
  LDK(1); LDK(2); LDK(3);
  SB();
  if (AMP == 6) asm volatile("s_memrealtime %0" : "=s"(tb));
  if (AMP == 5 || AMP == 6) asm volatile("s_waitcnt lgkmcnt(0)" : "+s"(tb));
  SB();
  const bool has16 = !odd || wave < WAVES - SKEW, has17 = !odd && wave < SKEW;
  so15 = has16 ? so + 15 * 1024 : 0x7fff0000u; so16 = has17 ? so + 16 * 1024 : 0x7fff0000u;
  if (SKEW) { LDK(15); LDK(16); }
#pragma unroll
  for (int t = 4; t < (SKEW ? 15 : 16); ++t) LDK(t);
  if (SKEW) { LDV(15); LDV(16); }
#pragma unroll
  for (int t = 1; t < PRE; ++t) LDV(t);
  SB();
  float x[NT];
#pragma unroll
  for (int t = 0; t < NT; ++t) {
    float d = dot2(k[t][0], qv[0], 0.f);
    d = dot2(k[t][1], qv[1], d); d = dot2(k[t][2], qv[2], d); d = dot2(k[t][3], qv[3], d);
    x[t] = row_sum16(d) * C2;
  }
  if (SKEW) { x[15] = has16 ? x[15] : -INFINITY; x[NT - 1] = has17 ? x[NT - 1] : -INFINITY; }
  if (CPF) asm volatile("" : "+v"(cpf));
  float m = x[0];
#pragma unroll
  for (int t = 1; t < NT; ++t) m = fmaxf(m, x[t]);
  m = xrow_max(m);
  const int n = c & 7;
  const unsigned lomask = (c >= 8) ? 0xffff0000u : 0u;
  unsigned sel[4];
#pragma unroll
  for (int r = 0; r < 4; ++r) sel[r] = ((n >> 1) == r) ? ((n & 1) ? 0x03020c0cu : 0x0c0c0302u) : 0x0c0c0c0cu;
  float l = 0.f;
  u32x4 b[NT];
#pragma unroll
  for (int t = 0; t < NT; ++t) {
    const float p = __builtin_amdgcn_exp2f(x[t] - m);
    l += p;
    const unsigned s = __float_as_uint(p - __uint_as_float(__float_as_uint(p) & lomask));
#pragma unroll
    for (int r = 0; r < 4; ++r) b[t][r] = __builtin_amdgcn_perm(s, s, sel[r]);
    if (PINB) asm volatile("" : "+v"(b[t]));
  }
  l = xrow_sum(l);
  float* lm = lds + WAVES * 128;
  if (lane == 0) *reinterpret_cast<f32x2*>(lm + 2 * wave) = f32x2{m, l};
  SB();
#pragma unroll
  for (int t = PRE; t < (SKEW ? 15 : 16); ++t) { LDV(t); SB(); }
  const int d0 = 32 * g + 16 * (c >> 3) + n;
  const auto rw = __builtin_amdgcn_make_buffer_rsrc((void*)ws, 0, wave == 0 ? WS_BYTES : 0u, 0x00020000);
  const unsigned long long wa = (unsigned long long)ws + head * HSTR;
  unsigned hb = head * HSTR + (NEAR ? ((((unsigned)(wa >> 13) ^ (unsigned)(wa >> 21) ^ ((unsigned)head >> 4)) & 1u) << 13) : 0u);
  float mx = 0.f, vtf0 = 0.f, vtf1 = 0.f;
  unsigned tag = 0;
  if (wave == 0) {
    const unsigned epoch = __builtin_amdgcn_raw_buffer_load_b32(rw, 0, hb + O_EP, 16);
    const u32x2 w0 = __builtin_amdgcn_raw_buffer_load_b64(rw, 0, hb + O_CNT, 16);
    const u32x4 ktv = kt[head * 16 + c];
    vtf0 = bf2f(vt[head * D + d0]); vtf1 = bf2f(vt[head * D + d0 + 8]);
    float d = dot2(ktv[0], qv[0], 0.f);
    d = dot2(ktv[1], qv[1], d); d = dot2(ktv[2], qv[2], d); d = dot2(ktv[3], qv[3], d);
    const float st = (row_sum16(d) * SCALE + (1.0f - bf2f(mask[0])) * -1000000000.0f) * LOG2E;
    mx = c == 8 ? st : -INFINITY;
    tag = epoch + 1u;
    if (lane == 0 && w0[1] != tag) {
      unsigned long long e = ((unsigned long long)w0[1] << 32) | w0[0];
      __hip_atomic_compare_exchange_strong((gu64*)(ws + hb + O_CNT), &e, (unsigned long long)tag << 32, __ATOMIC_RELAXED, __ATOMIC_RELAXED,
                                           __HIP_MEMORY_SCOPE_AGENT);
    }
    asm volatile("s_waitcnt vmcnt(0)" : "+v"(mx), "+v"(vtf0), "+v"(vtf1), "+v"(tag) :: "memory");
  }
  SB();
  __syncthreads();
  const f32x2 ml = *reinterpret_cast<const f32x2*>(lm + 2 * n);
  const float MW = __uint_as_float(__float_as_uint(row_max16(ml[0])) & KEEP);
  const float L = __uint_as_float(__float_as_uint(row_sum16(__builtin_amdgcn_exp2f(ml[0] - MW) * ml[1]) * 0.5f) & KEEP);
  float fw = __builtin_amdgcn_exp2f(m - MW);
  unsigned tg = tag & 0xffu;
  unsigned vo8 = lane * 8u;
  u32x2 gml = {__float_as_uint(MW) | tg, __float_as_uint(L) | tg};
  unsigned mo = lane < 16 ? vo8 : WS_BYTES, mlm = hb + O_ML + split * 128;
  if (!MLLATE) __builtin_amdgcn_raw_buffer_store_b64(gml, rw, mo, mlm, 16);
  else asm volatile("" : "+v"(gml), "+v"(mo), "+s"(mlm));
  const bool oth = c < 8 && c != split;
  float lx = c == split ? L : 1.0f;
  mx = c == split ? MW : mx;
  unsigned keepml = oth ? KEEP : 0u, mlo = oth ? n * 128u + g * 8u : WS_BYTES;
  unsigned ato = lane == 0 ? hb + O_CNT : WS_BYTES, epo = lane == 0 ? hb + O_EP : WS_BYTES;
  unsigned op = (head * D + d0 + ((lane & 1) ? 7 : 0)) * 2u;
  int old = 1;
  unsigned ks[SPLITS], sb[SPLITS];
#pragma unroll
  for (int s = 0; s < SPLITS; ++s) {
    ks[s] = s == split ? 0u : KEEP; sb[s] = s == split ? WS_BYTES : hb + s * REC;
    asm volatile("" : "+s"(ks[s]), "+s"(sb[s]));
  }
  unsigned mine = hb + split * REC, mlb = hb + O_ML;
  const auto ro = __builtin_amdgcn_make_buffer_rsrc((void*)out, 0, H * D * 2, 0x00020000);
  asm volatile("" : "+v"(fw), "+v"(lx), "+v"(mx), "+v"(keepml), "+v"(vo8), "+v"(mlo), "+v"(ato), "+v"(epo), "+v"(tg), "+v"(old), "+v"(op));
  asm volatile("" : "+s"(mine), "+s"(mlb) : "s"(ro));
  SB();
  f32x4 acc = {0.f, 0.f, 0.f, 0.f};
#pragma unroll
  for (int i = 0; i < NT; ++i) {
    const int t = !SKEW || i == 0 ? i : i < 3 ? 14 + i : i - 2;
    acc = __builtin_amdgcn_mfma_f32_16x16x32_bf16(__builtin_bit_cast(bf16x8, v[t]), __builtin_bit_cast(bf16x8, b[t]), acc, 0, 0, 0);
  }
#pragma unroll
  for (int r = 0; r < 4; ++r) acc[r] += dpp<0x128>(acc[r]);
  f32x2 o2 = {c < 8 ? acc[0] : acc[2], c < 8 ? acc[1] : acc[3]};
  o2 *= fw;
  *reinterpret_cast<f32x2*>(lds + wave * 128 + lane * 2) = o2;
  u32x2 gm;
  if (EARLYW) gm = __builtin_amdgcn_raw_buffer_load_b64(rw, mlo, mlb, 16);
  __syncthreads();
  if (wave != 0) return;
  DB1();
  if (AMP > 1) t4 = __builtin_amdgcn_s_memrealtime();
  old = __builtin_amdgcn_raw_ptr_buffer_atomic_add_i32(old, rw, ato, 0, 0);
  if (MLLATE == 2) __builtin_amdgcn_raw_buffer_store_b64(gml, rw, mo, mlm, 16);
  SB();
  f32x2 ow[WAVES];
#pragma unroll
  for (int w = 0; w < WAVES; ++w) ow[w] = *reinterpret_cast<const f32x2*>(reinterpret_cast<const char*>(lds) + vo8 + w * 512);
#pragma unroll
  for (int s2 = 1; s2 < WAVES; s2 *= 2)
#pragma unroll
    for (int w = 0; w < WAVES; w += 2 * s2) ow[w] += ow[w + s2];
  const unsigned oc0 = __float_as_uint(ow[0][0]) & KEEP, oc1 = __float_as_uint(ow[0][1]) & KEEP;
  __builtin_amdgcn_raw_buffer_store_b64(u32x2{oc0 | tg, oc1 | tg}, rw, vo8, mine, 16);
  if (MLLATE == 1) __builtin_amdgcn_raw_buffer_store_b64(gml, rw, mo, mlm, 16);
  SB();
  float wc;
  unsigned badw;
#define WEIGHTS() do { badw = ((gm[0] ^ tg) | (gm[1] ^ tg)) & (keepml >> 24); \
    const float mc = __uint_as_float(bfi(keepml, gm[0], __float_as_uint(mx))), lc = __uint_as_float(bfi(keepml, gm[1], __float_as_uint(lx))); \
    wc = __builtin_amdgcn_exp2f(mc - row_max16(mc)); wc *= __builtin_amdgcn_rcpf(row_sum16(wc * lc)); \
    asm volatile("" : "+v"(wc), "+v"(badw)); } while (0)
  if (EARLYW) WEIGHTS();
  SB();
  if (__builtin_amdgcn_readfirstlane(old) != SPLITS - 1) { DBEND(0u, 0u); AMPEND(0); return; }
  u32x2 gs[SPLITS];
  f32x2 o;
  unsigned bad, nsw = 1;
#define SWEEP() do { _Pragma("unroll") for (int s = 0; s < SPLITS; ++s) gs[s] = __builtin_amdgcn_raw_buffer_load_b64(rw, vo8, sb[s], 16); } while (0)
#define ACC(S) { asm volatile("" : "+v"(gs[S])); const float w_ = dpp<0x150 + S>(wc); \
    bad |= ((gs[S][0] ^ tg) | (gs[S][1] ^ tg)) & (ks[S] >> 24); \
    o = __builtin_elementwise_fma(f32x2{w_, w_}, f32x2{__uint_as_float(bfi(ks[S], gs[S][0], oc0)), __uint_as_float(bfi(ks[S], gs[S][1], oc1))}, o); \
    asm volatile("" : "+v"(o), "+v"(bad)); }
#define MERGE() do { const float wt = dpp<0x158>(wc); o = f32x2{wt * vtf0, wt * vtf1}; bad = badw; \
    ACC(0) ACC(1) ACC(2) ACC(3) ACC(4) ACC(5) ACC(6) ACC(7) } while (0)
  if (!EARLYW) gm = __builtin_amdgcn_raw_buffer_load_b64(rw, mlo, mlb, 16);
  SWEEP();
  __builtin_amdgcn_raw_buffer_store_b32(tag, rw, epo, 0, 16);
  SB();
  if (!EARLYW) WEIGHTS();
  MERGE();
  if (!__all(bad == 0)) {
#pragma nounroll
    for (unsigned spins = 0; spins < (1u << 20); ++spins) {
      asm volatile("" ::: "memory");
      gm = __builtin_amdgcn_raw_buffer_load_b64(rw, mlo, mlb, 16);
      SWEEP(); ++nsw;
      WEIGHTS();
      MERGE();
      if (__all(bad == 0)) break;
    }
  }
  {
    const float rcv = dpp<0xB1>((lane & 1) ? o[0] : o[1]);
    const bf16x2 pk = {(__bf16)((lane & 1) ? rcv : o[0]), (__bf16)((lane & 1) ? o[1] : rcv)};
    __builtin_amdgcn_raw_buffer_store_b32(__builtin_bit_cast(unsigned, pk), ro, op, 0, OAUX);
  }
  DBEND(nsw, 1u);
  AMPEND(1);
}

extern "C" void kernel_launch(void* const* d_in, const int* in_sizes, int n_in, void* d_out, int out_size, void* d_ws,
                              size_t ws_size, hipStream_t stream) {
  attn_fused<<<H * SPLITS, WAVES * 64, 0, stream>>>(d_in[1], H * C * D * 2, 0x00020000u, d_in[2], H * C * D * 2, 0x00020000u, (const u32x4*)d_in[0], (unsigned char*)d_ws,
                                                    (const u32x4*)d_in[3], (const unsigned short*)d_in[4],
                                                    (const unsigned short*)d_in[5], (__hip_bfloat16*)d_out);
}
```

```cpp
#include <hip/hip_runtime.h>
#include <hip/hip_bf16.h>
#include <stdint.h>

#define PRE 4
#ifndef NV1
#define NV1 2
#endif

namespace {

constexpr int H = 32, C = 4096, D = 128, SPLITS = 8, WAVES = 8;
constexpr int NT = C / SPLITS / WAVES / 4;
constexpr int KV_AUX = 3;
constexpr int SC1 = 16;
constexpr float SCALE = 0.088388347648318440f, LOG2E = 1.4426950408889634f;
constexpr float C2 = SCALE * LOG2E;

constexpr unsigned HSTR = 8192, REC = 512, O_ML = 4096, O_EP = 5120, O_CNT = 5248, WS_BYTES = H * HSTR;
constexpr unsigned KEEP = 0xffffff00u;

using bf16x2 = __attribute__((ext_vector_type(2))) __bf16;
using bf16x8 = __attribute__((ext_vector_type(8))) __bf16;
using u32x4 = __attribute__((ext_vector_type(4))) unsigned;
using u32x2 = __attribute__((ext_vector_type(2))) unsigned;
using i32x8 = __attribute__((ext_vector_type(8))) int;
using i32x4 = __attribute__((ext_vector_type(4))) int;
using f32x4 = __attribute__((ext_vector_type(4))) float;
using f32x2 = __attribute__((ext_vector_type(2))) float;
typedef __attribute__((address_space(1))) unsigned long long gu64;
typedef __attribute__((address_space(1))) u32x4 gu32x4;

__device__ __forceinline__ float dot2(unsigned k, unsigned q, float acc) {
  return __builtin_amdgcn_fdot2_f32_bf16(__builtin_bit_cast(bf16x2, k), __builtin_bit_cast(bf16x2, q), acc, false);
}
__device__ __forceinline__ float dot8(const u32x4& k, const u32x4& q) {
  return dot2(k[3], q[3], dot2(k[2], q[2], dot2(k[1], q[1], dot2(k[0], q[0], 0.f))));
}
template <int CTRL> __device__ __forceinline__ float dpp(float x) {
  return __builtin_bit_cast(float, __builtin_amdgcn_mov_dpp(__builtin_bit_cast(int, x), CTRL, 0xf, 0xf, true));
}
__device__ __forceinline__ float row_sum16(float x) {
  x += dpp<0x128>(x); x += dpp<0x124>(x); x += dpp<0x122>(x); x += dpp<0x121>(x);
  return x;
}
__device__ __forceinline__ float row_max16(float x) {
  asm volatile("s_nop 1\n\tv_max_f32_dpp %0, %0, %0 row_ror:8 row_mask:0xf bank_mask:0xf\n\ts_nop 1\n\t"
               "v_max_f32_dpp %0, %0, %0 row_ror:4 row_mask:0xf bank_mask:0xf\n\ts_nop 1\n\t"
               "v_max_f32_dpp %0, %0, %0 row_ror:2 row_mask:0xf bank_mask:0xf\n\ts_nop 1\n\t"
               "v_max_f32_dpp %0, %0, %0 row_ror:1 row_mask:0xf bank_mask:0xf" : "+v"(x));
  return x;
}
__device__ __forceinline__ float xrow_max(float x) {
  auto s = __builtin_amdgcn_permlane16_swap(__float_as_uint(x), __float_as_uint(x), false, false);
  x = fmaxf(__uint_as_float(s[0]), __uint_as_float(s[1]));
  auto t = __builtin_amdgcn_permlane32_swap(__float_as_uint(x), __float_as_uint(x), false, false);
  return fmaxf(__uint_as_float(t[0]), __uint_as_float(t[1]));
}
__device__ __forceinline__ float xrow_sum(float x) {
  auto s = __builtin_amdgcn_permlane16_swap(__float_as_uint(x), __float_as_uint(x), false, false);
  x = __uint_as_float(s[0]) + __uint_as_float(s[1]);
  auto t = __builtin_amdgcn_permlane32_swap(__float_as_uint(x), __float_as_uint(x), false, false);
  return __uint_as_float(t[0]) + __uint_as_float(t[1]);
}
__device__ __forceinline__ float bf2f(unsigned short h) { return __uint_as_float((unsigned)h << 16); }

}

__device__ i32x4 buf_load16(i32x4 rsrc, int voffset, int soffset, int aux) __asm("llvm.amdgcn.raw.buffer.load.v4i32");
#define SB() __builtin_amdgcn_sched_barrier(0)

__global__ void __launch_bounds__(WAVES * 64) tree_attn_decode(const void* K, unsigned kn, unsigned kf, const void* V, unsigned vn, unsigned vf,
                                                               const void* q, unsigned char* ws, const u32x4* __restrict__ kt,
                                                               const unsigned short* __restrict__ vt,
                                                               const unsigned short* __restrict__ mask, __hip_bfloat16* out) {
  __shared__ __attribute__((aligned(16))) float lds[WAVES * 128 + 2 * WAVES];
  i32x8 ka;
  unsigned long long qa;
  unsigned so, vo, head, tmp;
  asm volatile("s_load_dwordx8 %0, %6, 0x0\n\t"
               "s_load_dwordx2 %1, %6, 0x20\n\t"
               "v_readfirstlane_b32 %2, %8\n\t"
               "s_and_b32 %4, %7, 7\n\t"
               "s_bfe_u32 %5, %7, 0x20003\n\t"
               "s_lshl2_add_u32 %4, %4, %5\n\t"
               "s_lshr_b32 %5, %7, 5\n\t"
               "s_lshl3_add_u32 %5, %4, %5\n\t"
               "s_lshl_b32 %5, %5, 9\n\t"
               "s_add_u32 %2, %2, %5\n\t"
               "s_lshl_b32 %2, %2, 8\n\t"
               "v_and_b32 %3, 63, %8\n\t"
               "v_lshlrev_b32 %3, 4, %3\n\t"
               "s_waitcnt lgkmcnt(0)"
               : "=&s"(ka), "=&s"(qa), "=&s"(so), "=&v"(vo), "=&s"(head), "=&s"(tmp)
               : "s"(__builtin_amdgcn_kernarg_segment_ptr()), "s"(blockIdx.x), "v"(threadIdx.x)
               : "scc");
  const i32x4 rk = __builtin_shufflevector(ka, ka, 0, 1, 2, 3), rv = __builtin_shufflevector(ka, ka, 4, 5, 6, 7);
  u32x4 k[NT], v[NT];
#define LDX(r, t) __builtin_bit_cast(u32x4, buf_load16(r, vo + ((t) & 3) * 1024, so + ((t) >> 2) * 4096, KV_AUX))
  k[0] = LDX(rk, 0); SB();
  v[0] = LDX(rv, 0); SB();
#pragma unroll
  for (int t = 1; t < 4; ++t) { k[t] = LDX(rk, t); SB(); }
#pragma unroll
  for (int t = 1; t <= NV1; ++t) { v[t] = LDX(rv, t); SB(); }
  const unsigned split = blockIdx.x >> 5, lane = threadIdx.x & 63, wave = __builtin_amdgcn_readfirstlane(threadIdx.x >> 6);
  const unsigned c = lane & 15, g = lane >> 4, n = c & 7;
  const u32x4 qv = *(const gu32x4*)(qa + (head * 16 + c) * 16u);
#pragma unroll
  for (int t = 4; t < NT; ++t) k[t] = LDX(rk, t);
#pragma unroll
  for (int t = NV1 + 1; t < PRE; ++t) v[t] = LDX(rv, t);
  SB();

  float x[NT];
#pragma unroll
  for (int t = 0; t < NT; ++t) x[t] = row_sum16(dot8(k[t], qv)) * C2;
  float m = x[0];
#pragma unroll
  for (int t = 1; t < NT; ++t) m = fmaxf(m, x[t]);
  m = xrow_max(m);
  const unsigned lomask = c >= 8 ? 0xffff0000u : 0u;
  unsigned sel[4];
#pragma unroll
  for (int r = 0; r < 4; ++r) sel[r] = (n >> 1) == r ? ((n & 1) ? 0x03020c0cu : 0x0c0c0302u) : 0x0c0c0c0cu;
  float l = 0.f;
  u32x4 b[NT];
#pragma unroll
  for (int t = 0; t < NT; ++t) {
    const float p = __builtin_amdgcn_exp2f(x[t] - m);
    l += p;
    const unsigned s = __float_as_uint(p - __uint_as_float(__float_as_uint(p) & lomask));
#pragma unroll
    for (int r = 0; r < 4; ++r) b[t][r] = __builtin_amdgcn_perm(s, s, sel[r]);
    asm volatile("" : "+v"(b[t]));
  }
  l = xrow_sum(l);
  float* lm = lds + WAVES * 128;
  if (lane == 0) *reinterpret_cast<f32x2*>(lm + 2 * wave) = f32x2{m, l};
  SB();
  asm volatile("" : "+v"(vo));
#pragma unroll
  for (int t = PRE; t < NT; ++t) { v[t] = LDX(rv, t); SB(); }

  const unsigned d0 = 32 * g + 16 * (c >> 3) + n;
  const unsigned hb = head * HSTR;
  const auto rw = __builtin_amdgcn_make_buffer_rsrc((void*)ws, 0, wave == 0 ? WS_BYTES : 0u, 0x00020000);
  float mx = 0.f, vt0 = 0.f, vt1 = 0.f;
  unsigned tag = 0;
  if (wave == 0) {
    const unsigned epoch = __builtin_amdgcn_raw_buffer_load_b32(rw, 0, hb + O_EP, SC1);
    const u32x2 w0 = __builtin_amdgcn_raw_buffer_load_b64(rw, 0, hb + O_CNT, SC1);
    vt0 = bf2f(vt[head * D + d0]);
    vt1 = bf2f(vt[head * D + d0 + 8]);
    const float st = (row_sum16(dot8(kt[head * 16 + c], qv)) * SCALE + (1.0f - bf2f(mask[0])) * -1000000000.0f) * LOG2E;
    mx = c == 8 ? st : -INFINITY;
    tag = epoch + 1u;
    if (lane == 0 && w0[1] != tag) {
      unsigned long long e = ((unsigned long long)w0[1] << 32) | w0[0];
      __hip_atomic_compare_exchange_strong((gu64*)(ws + hb + O_CNT), &e, (unsigned long long)tag << 32, __ATOMIC_RELAXED,
                                           __ATOMIC_RELAXED, __HIP_MEMORY_SCOPE_AGENT);
    }
    asm volatile("s_waitcnt vmcnt(0)" : "+v"(mx), "+v"(vt0), "+v"(vt1), "+v"(tag) :: "memory");
  }
  SB();
  __syncthreads();

  const f32x2 ml = *reinterpret_cast<const f32x2*>(lm + 2 * n);
  const float MW = row_max16(ml[0]);
  const float L = __uint_as_float(__float_as_uint(row_sum16(__builtin_amdgcn_exp2f(ml[0] - MW) * ml[1]) * 0.5f) & KEEP);
  float fw = __builtin_amdgcn_exp2f(m - MW);
  unsigned tg = tag & 0xffu, vo8 = lane * 8u;
  __builtin_amdgcn_raw_buffer_store_b64(u32x2{__float_as_uint(MW), __float_as_uint(L) | tg}, rw, lane < 16 ? vo8 : WS_BYTES,
                                        hb + O_ML + split * 128, SC1);
  float lx = c == split ? L : 1.0f;
  mx = c == split ? MW : mx;
  unsigned km = (c < 8 && c != split) ? 0xffffffffu : 0u, kbm = n != split ? 0xffu : 0u;
  unsigned mlo = n != split ? n * 128u + (lane >> 3) * 8u : WS_BYTES;
  unsigned ato = lane == 0 ? hb + O_CNT : WS_BYTES, epo = lane == 0 ? hb + O_EP : WS_BYTES;
  unsigned op = (head * D + d0 + ((lane & 1) ? 7 : 0)) * 2u;
  int old = 1;
  unsigned ks[SPLITS], kb[SPLITS], sb[SPLITS];
#pragma unroll
  for (int s = 0; s < SPLITS; ++s) {
    ks[s] = s == split ? 0u : KEEP; kb[s] = s == split ? 0u : 0xffu; sb[s] = s == split ? WS_BYTES : hb + s * REC;
    asm volatile("" : "+s"(ks[s]), "+s"(kb[s]), "+s"(sb[s]));
  }
  unsigned mine = hb + split * REC, mlb = hb + O_ML;
  const auto ro = __builtin_amdgcn_make_buffer_rsrc((void*)out, 0, H * D * 2, 0x00020000);
  asm volatile("" : "+v"(fw), "+v"(lx), "+v"(mx), "+v"(km), "+v"(kbm), "+v"(vo8), "+v"(mlo), "+v"(ato), "+v"(epo), "+v"(tg), "+v"(old), "+v"(op));
  asm volatile("" : "+s"(mine), "+s"(mlb) : "s"(ro));
  SB();

  f32x4 acc = {0.f, 0.f, 0.f, 0.f};
#pragma unroll
  for (int t = 0; t < NT; ++t)
    acc = __builtin_amdgcn_mfma_f32_16x16x32_bf16(__builtin_bit_cast(bf16x8, v[t]), __builtin_bit_cast(bf16x8, b[t]), acc, 0, 0, 0);
#pragma unroll
  for (int r = 0; r < 4; ++r) acc[r] += dpp<0x128>(acc[r]);
  f32x2 o2 = {c < 8 ? acc[0] : acc[2], c < 8 ? acc[1] : acc[3]};
  o2 *= fw;
  *reinterpret_cast<f32x2*>(lds + wave * 128 + lane * 2) = o2;
  __syncthreads();
  if (wave != 0) return;

  old = __builtin_amdgcn_raw_ptr_buffer_atomic_add_i32(old, rw, ato, 0, 0);
  SB();
  f32x2 ow[WAVES];
#pragma unroll
  for (int w = 0; w < WAVES; ++w) ow[w] = *reinterpret_cast<const f32x2*>(reinterpret_cast<const char*>(lds) + vo8 + w * 512);
#pragma unroll
  for (int s = 1; s < WAVES; s *= 2)
#pragma unroll
    for (int w = 0; w < WAVES; w += 2 * s) ow[w] += ow[w + s];
  const unsigned oc0 = __float_as_uint(ow[0][0]) & KEEP, oc1 = __float_as_uint(ow[0][1]) & KEEP;
  __builtin_amdgcn_raw_buffer_store_b64(u32x2{oc0 | tg, oc1 | tg}, rw, vo8, mine, SC1);
  SB();
  if (__builtin_amdgcn_readfirstlane(old) != SPLITS - 1) {   return; }

  float o0, o1;
#pragma nounroll
  for (unsigned pass = 0; pass < (1u << 20); ++pass) {
    asm volatile("" ::: "memory");
    const u32x2 gm = __builtin_amdgcn_raw_buffer_load_b64(rw, mlo, mlb, SC1);
    u32x2 gs[SPLITS];
#pragma unroll
    for (int s = 0; s < SPLITS; ++s) gs[s] = __builtin_amdgcn_raw_buffer_load_b64(rw, vo8, sb[s], SC1);
    __builtin_amdgcn_raw_buffer_store_b32(tag, rw, epo, 0, SC1);
    SB();
    unsigned bad;
    float mc, lc;
    asm volatile("v_xor_b32 %2, %4, %9\n\tv_bfi_b32 %1, %5, %3, %7\n\tv_and_b32 %0, %2, %6\n\tv_bfi_b32 %2, %5, %2, %8"
                 : "=&v"(bad), "=&v"(mc), "=&v"(lc) : "v"(gm[0]), "v"(gm[1]), "v"(km), "v"(kbm), "v"(mx), "v"(lx), "v"(tg));
    float wc = __builtin_amdgcn_exp2f(mc - row_max16(mc));
    wc *= __builtin_amdgcn_rcpf(row_sum16(wc * lc));
    const float wt = dpp<0x158>(wc);
    o0 = wt * vt0;
    o1 = wt * vt1;
#define ACC(S) { unsigned t_, x0_, x1_;                                                                                            \
    asm volatile("v_bitop3_b32 %3, %6, %7, %13 bitop3:0x7e\n\tv_bfi_b32 %4, %8, %6, %10\n\tv_bfi_b32 %5, %8, %7, %11\n\t"          \
                 "v_and_or_b32 %2, %3, %9, %2\n\tv_fmac_f32_dpp %0, %12, %4 row_newbcast:" #S " row_mask:0xf bank_mask:0xf\n\t"    \
                 "v_fmac_f32_dpp %1, %12, %5 row_newbcast:" #S " row_mask:0xf bank_mask:0xf"                                       \
                 : "+v"(o0), "+v"(o1), "+v"(bad), "=&v"(t_), "=&v"(x0_), "=&v"(x1_)                                                \
                 : "v"(gs[S][0]), "v"(gs[S][1]), "s"(ks[S]), "s"(kb[S]), "v"(oc0), "v"(oc1), "v"(wc), "v"(tg)); }
    ACC(0) ACC(1) ACC(2) ACC(3) ACC(4) ACC(5) ACC(6) ACC(7)
#undef ACC
    if (__all(bad == 0)) break;
  }
  const float rcv = dpp<0xB1>((lane & 1) ? o0 : o1);
  const bf16x2 pk = {(__bf16)((lane & 1) ? rcv : o0), (__bf16)((lane & 1) ? o1 : rcv)};
  __builtin_amdgcn_raw_buffer_store_b32(__builtin_bit_cast(unsigned, pk), ro, op, 0, 0);
}

extern "C" void kernel_launch(void* const* d_in, const int* in_sizes, int n_in, void* d_out, int out_size, void* d_ws,
                              size_t ws_size, hipStream_t stream) {
  constexpr unsigned kv_bytes = H * C * D * 2, flags = 0x00020000u;
  tree_attn_decode<<<H * SPLITS, WAVES * 64, 0, stream>>>(d_in[1], kv_bytes, flags, d_in[2], kv_bytes, flags, d_in[0],
                                                          (unsigned char*)d_ws, (const u32x4*)d_in[3],
                                                          (const unsigned short*)d_in[4], (const unsigned short*)d_in[5],
                                                          (__hip_bfloat16*)d_out);
}
```

```cpp
#define PRE 3
#define NV1 2
#include <hip/hip_runtime.h>
#include <hip/hip_bf16.h>
#include <stdint.h>

#define RT(x) do { __builtin_amdgcn_sched_barrier(0); asm volatile("s_memrealtime %0\n\ts_waitcnt lgkmcnt(0)" : "=s"(x) :: "memory"); __builtin_amdgcn_sched_barrier(0); } while (0)

namespace {

constexpr int H = 32, C = 4096, D = 128, SPLITS = 8, WAVES = 8;
constexpr int NT = C / SPLITS / WAVES / 4;
constexpr int KV_AUX = 3;
constexpr int SC1 = 16;
constexpr float SCALE = 0.088388347648318440f, LOG2E = 1.4426950408889634f;
constexpr float C2 = SCALE * LOG2E;

constexpr unsigned HSTR = 8192, REC = 512, O_ML = 4096, O_EP = 5120, O_CNT = 5248, WS_BYTES = H * HSTR;
constexpr unsigned KEEP = 0xffffff00u;

using bf16x2 = __attribute__((ext_vector_type(2))) __bf16;
using bf16x8 = __attribute__((ext_vector_type(8))) __bf16;
using u32x4 = __attribute__((ext_vector_type(4))) unsigned;
using u32x2 = __attribute__((ext_vector_type(2))) unsigned;
using i32x8 = __attribute__((ext_vector_type(8))) int;
using i32x4 = __attribute__((ext_vector_type(4))) int;
using f32x4 = __attribute__((ext_vector_type(4))) float;
using f32x2 = __attribute__((ext_vector_type(2))) float;
typedef __attribute__((address_space(1))) unsigned long long gu64;
typedef __attribute__((address_space(1))) u32x4 gu32x4;

__device__ __forceinline__ float dot2(unsigned k, unsigned q, float acc) {
  return __builtin_amdgcn_fdot2_f32_bf16(__builtin_bit_cast(bf16x2, k), __builtin_bit_cast(bf16x2, q), acc, false);
}
__device__ __forceinline__ float dot8(const u32x4& k, const u32x4& q) {
  return dot2(k[3], q[3], dot2(k[2], q[2], dot2(k[1], q[1], dot2(k[0], q[0], 0.f))));
}
template <int CTRL> __device__ __forceinline__ float dpp(float x) {
  return __builtin_bit_cast(float, __builtin_amdgcn_mov_dpp(__builtin_bit_cast(int, x), CTRL, 0xf, 0xf, true));
}
__device__ __forceinline__ float row_sum16(float x) {
  x += dpp<0x128>(x); x += dpp<0x124>(x); x += dpp<0x122>(x); x += dpp<0x121>(x);
  return x;
}
__device__ __forceinline__ float row_max16(float x) {
  asm volatile("s_nop 1\n\tv_max_f32_dpp %0, %0, %0 row_ror:8 row_mask:0xf bank_mask:0xf\n\ts_nop 1\n\t"
               "v_max_f32_dpp %0, %0, %0 row_ror:4 row_mask:0xf bank_mask:0xf\n\ts_nop 1\n\t"
               "v_max_f32_dpp %0, %0, %0 row_ror:2 row_mask:0xf bank_mask:0xf\n\ts_nop 1\n\t"
               "v_max_f32_dpp %0, %0, %0 row_ror:1 row_mask:0xf bank_mask:0xf" : "+v"(x));
  return x;
}
__device__ __forceinline__ float xrow_max(float x) {
  auto s = __builtin_amdgcn_permlane16_swap(__float_as_uint(x), __float_as_uint(x), false, false);
  x = fmaxf(__uint_as_float(s[0]), __uint_as_float(s[1]));
  auto t = __builtin_amdgcn_permlane32_swap(__float_as_uint(x), __float_as_uint(x), false, false);
  return fmaxf(__uint_as_float(t[0]), __uint_as_float(t[1]));
}
__device__ __forceinline__ float xrow_sum(float x) {
  auto s = __builtin_amdgcn_permlane16_swap(__float_as_uint(x), __float_as_uint(x), false, false);
  x = __uint_as_float(s[0]) + __uint_as_float(s[1]);
  auto t = __builtin_amdgcn_permlane32_swap(__float_as_uint(x), __float_as_uint(x), false, false);
  return __uint_as_float(t[0]) + __uint_as_float(t[1]);
}
__device__ __forceinline__ float bf2f(unsigned short h) { return __uint_as_float((unsigned)h << 16); }

}

__device__ i32x4 buf_load16(i32x4 rsrc, int voffset, int soffset, int aux) __asm("llvm.amdgcn.raw.buffer.load.v4i32");
#define SB() __builtin_amdgcn_sched_barrier(0)

__global__ void __launch_bounds__(WAVES * 64) tree_attn_decode(const void* K, unsigned kn, unsigned kf, const void* V, unsigned vn, unsigned vf,
                                                               const void* q, unsigned char* ws, const u32x4* __restrict__ kt,
                                                               const unsigned short* __restrict__ vt,
                                                               const unsigned short* __restrict__ mask, __hip_bfloat16* out) {
  __shared__ __attribute__((aligned(16))) float lds[WAVES * 128 + 2 * WAVES];
  i32x8 ka;
  unsigned long long qa;
  unsigned so, vo, head, tmp;
  unsigned long long ta;
  asm volatile("s_memrealtime %0" : "=s"(ta));
  asm volatile("s_load_dwordx8 %0, %6, 0x0\n\t"
               "s_load_dwordx2 %1, %6, 0x20\n\t"
               "v_readfirstlane_b32 %2, %8\n\t"
               "s_and_b32 %4, %7, 7\n\t"
               "s_bfe_u32 %5, %7, 0x20003\n\t"
               "s_lshl2_add_u32 %4, %4, %5\n\t"
               "s_lshr_b32 %5, %7, 5\n\t"
               "s_lshl3_add_u32 %5, %4, %5\n\t"
               "s_lshl_b32 %5, %5, 9\n\t"
               "s_add_u32 %2, %2, %5\n\t"
               "s_lshl_b32 %2, %2, 8\n\t"
               "v_and_b32 %3, 63, %8\n\t"
               "v_lshlrev_b32 %3, 4, %3\n\t"
               "s_waitcnt lgkmcnt(0)"
               : "=&s"(ka), "=&s"(qa), "=&s"(so), "=&v"(vo), "=&s"(head), "=&s"(tmp)
               : "s"(__builtin_amdgcn_kernarg_segment_ptr()), "s"(blockIdx.x), "v"(threadIdx.x)
               : "scc");
  const i32x4 rk = __builtin_shufflevector(ka, ka, 0, 1, 2, 3), rv = __builtin_shufflevector(ka, ka, 4, 5, 6, 7);
  u32x4 k[NT], v[NT];
#define LDX(r, t) __builtin_bit_cast(u32x4, buf_load16(r, vo + ((t) & 3) * 1024, so + ((t) >> 2) * 4096, KV_AUX))
  k[0] = LDX(rk, 0); SB();
  v[0] = LDX(rv, 0); SB();
#pragma unroll
  for (int t = 1; t < 4; ++t) { k[t] = LDX(rk, t); SB(); }
#pragma unroll
  for (int t = 1; t <= NV1; ++t) { v[t] = LDX(rv, t); SB(); }
  const unsigned split = blockIdx.x >> 5, lane = threadIdx.x & 63, wave = __builtin_amdgcn_readfirstlane(threadIdx.x >> 6);
  const unsigned c = lane & 15, g = lane >> 4, n = c & 7;
  const u32x4 qv = *(const gu32x4*)(qa + (head * 16 + c) * 16u);
#pragma unroll
  for (int t = 4; t < NT; ++t) k[t] = LDX(rk, t);
#pragma unroll
  for (int t = NV1 + 1; t < PRE; ++t) v[t] = LDX(rv, t);
  SB();

  float x[NT];
#pragma unroll
  for (int t = 0; t < NT; ++t) x[t] = row_sum16(dot8(k[t], qv)) * C2;
  float m = x[0];
#pragma unroll
  for (int t = 1; t < NT; ++t) m = fmaxf(m, x[t]);
  m = xrow_max(m);
  const unsigned lomask = c >= 8 ? 0xffff0000u : 0u;
  unsigned sel[4];
#pragma unroll
  for (int r = 0; r < 4; ++r) sel[r] = (n >> 1) == r ? ((n & 1) ? 0x03020c0cu : 0x0c0c0302u) : 0x0c0c0c0cu;
  float l = 0.f;
  u32x4 b[NT];
#pragma unroll
  for (int t = 0; t < NT; ++t) {
    const float p = __builtin_amdgcn_exp2f(x[t] - m);
    l += p;
    const unsigned s = __float_as_uint(p - __uint_as_float(__float_as_uint(p) & lomask));
#pragma unroll
    for (int r = 0; r < 4; ++r) b[t][r] = __builtin_amdgcn_perm(s, s, sel[r]);
    asm volatile("" : "+v"(b[t]));
  }
  l = xrow_sum(l);
  float* lm = lds + WAVES * 128;
  if (lane == 0) *reinterpret_cast<f32x2*>(lm + 2 * wave) = f32x2{m, l};
  SB();
  asm volatile("" : "+v"(vo));
#pragma unroll
  for (int t = PRE; t < NT; ++t) { v[t] = LDX(rv, t); SB(); }

  const unsigned d0 = 32 * g + 16 * (c >> 3) + n;
  const unsigned hb = head * HSTR;
  const auto rw = __builtin_amdgcn_make_buffer_rsrc((void*)ws, 0, wave == 0 ? WS_BYTES : 0u, 0x00020000);
  float mx = 0.f, vt0 = 0.f, vt1 = 0.f;
  unsigned tag = 0;
  if (wave == 0) {
    const unsigned epoch = __builtin_amdgcn_raw_buffer_load_b32(rw, 0, hb + O_EP, SC1);
    const u32x2 w0 = __builtin_amdgcn_raw_buffer_load_b64(rw, 0, hb + O_CNT, SC1);
    vt0 = bf2f(vt[head * D + d0]);
    vt1 = bf2f(vt[head * D + d0 + 8]);
    const float st = (row_sum16(dot8(kt[head * 16 + c], qv)) * SCALE + (1.0f - bf2f(mask[0])) * -1000000000.0f) * LOG2E;
    mx = c == 8 ? st : -INFINITY;
    tag = epoch + 1u;
    if (lane == 0 && w0[1] != tag) {
      unsigned long long e = ((unsigned long long)w0[1] << 32) | w0[0];
      __hip_atomic_compare_exchange_strong((gu64*)(ws + hb + O_CNT), &e, (unsigned long long)tag << 32, __ATOMIC_RELAXED,
                                           __ATOMIC_RELAXED, __HIP_MEMORY_SCOPE_AGENT);
    }
    asm volatile("s_waitcnt vmcnt(0)" : "+v"(mx), "+v"(vt0), "+v"(vt1), "+v"(tag) :: "memory");
  }
  SB();
  __syncthreads();

  const f32x2 ml = *reinterpret_cast<const f32x2*>(lm + 2 * n);
  const float MW = row_max16(ml[0]);
  const float L = __uint_as_float(__float_as_uint(row_sum16(__builtin_amdgcn_exp2f(ml[0] - MW) * ml[1]) * 0.5f) & KEEP);
  float fw = __builtin_amdgcn_exp2f(m - MW);
  unsigned tg = tag & 0xffu, vo8 = lane * 8u;
  __builtin_amdgcn_raw_buffer_store_b64(u32x2{__float_as_uint(MW), __float_as_uint(L) | tg}, rw, lane < 16 ? vo8 : WS_BYTES,
                                        hb + O_ML + split * 128, SC1);
  float lx = c == split ? L : 1.0f;
  mx = c == split ? MW : mx;
  unsigned km = (c < 8 && c != split) ? 0xffffffffu : 0u, kbm = n != split ? 0xffu : 0u;
  unsigned mlo = n != split ? n * 128u + (lane >> 3) * 8u : WS_BYTES;
  unsigned ato = lane == 0 ? hb + O_CNT : WS_BYTES, epo = lane == 0 ? hb + O_EP : WS_BYTES;
  unsigned op = (head * D + d0 + ((lane & 1) ? 7 : 0)) * 2u;
  int old = 1;
  unsigned ks[SPLITS], kb[SPLITS], sb[SPLITS];
#pragma unroll
  for (int s = 0; s < SPLITS; ++s) {
    ks[s] = s == split ? 0u : KEEP; kb[s] = s == split ? 0u : 0xffu; sb[s] = s == split ? WS_BYTES : hb + s * REC;
    asm volatile("" : "+s"(ks[s]), "+s"(kb[s]), "+s"(sb[s]));
  }
  unsigned mine = hb + split * REC, mlb = hb + O_ML;
  const auto ro = __builtin_amdgcn_make_buffer_rsrc((void*)out, 0, H * D * 2, 0x00020000);
  asm volatile("" : "+v"(fw), "+v"(lx), "+v"(mx), "+v"(km), "+v"(kbm), "+v"(vo8), "+v"(mlo), "+v"(ato), "+v"(epo), "+v"(tg), "+v"(old), "+v"(op));
  asm volatile("" : "+s"(mine), "+s"(mlb) : "s"(ro));
  SB();

  f32x4 acc = {0.f, 0.f, 0.f, 0.f};
#pragma unroll
  for (int t = 0; t < NT; ++t)
    acc = __builtin_amdgcn_mfma_f32_16x16x32_bf16(__builtin_bit_cast(bf16x8, v[t]), __builtin_bit_cast(bf16x8, b[t]), acc, 0, 0, 0);
#pragma unroll
  for (int r = 0; r < 4; ++r) acc[r] += dpp<0x128>(acc[r]);
  f32x2 o2 = {c < 8 ? acc[0] : acc[2], c < 8 ? acc[1] : acc[3]};
  o2 *= fw;
  *reinterpret_cast<f32x2*>(lds + wave * 128 + lane * 2) = o2;
  __syncthreads();
  if (wave != 0) return;

  old = __builtin_amdgcn_raw_ptr_buffer_atomic_add_i32(old, rw, ato, 0, 0);
  SB();
  f32x2 ow[WAVES];
#pragma unroll
  for (int w = 0; w < WAVES; ++w) ow[w] = *reinterpret_cast<const f32x2*>(reinterpret_cast<const char*>(lds) + vo8 + w * 512);
#pragma unroll
  for (int s = 1; s < WAVES; s *= 2)
#pragma unroll
    for (int w = 0; w < WAVES; w += 2 * s) ow[w] += ow[w + s];
  const unsigned oc0 = __float_as_uint(ow[0][0]) & KEEP, oc1 = __float_as_uint(ow[0][1]) & KEEP;
  __builtin_amdgcn_raw_buffer_store_b64(u32x2{oc0 | tg, oc1 | tg}, rw, vo8, mine, SC1);
  SB();
  if (__builtin_amdgcn_readfirstlane(old) != SPLITS - 1) {   return; }

  float o0, o1;
#pragma nounroll
  for (unsigned pass = 0; pass < (1u << 20); ++pass) {
    asm volatile("" ::: "memory");
    const u32x2 gm = __builtin_amdgcn_raw_buffer_load_b64(rw, mlo, mlb, SC1);
    u32x2 gs[SPLITS];
#pragma unroll
    for (int s = 0; s < SPLITS; ++s) gs[s] = __builtin_amdgcn_raw_buffer_load_b64(rw, vo8, sb[s], SC1);
    __builtin_amdgcn_raw_buffer_store_b32(tag, rw, epo, 0, SC1);
    SB();
    unsigned bad;
    float mc, lc;
    asm volatile("v_xor_b32 %2, %4, %9\n\tv_bfi_b32 %1, %5, %3, %7\n\tv_and_b32 %0, %2, %6\n\tv_bfi_b32 %2, %5, %2, %8"
                 : "=&v"(bad), "=&v"(mc), "=&v"(lc) : "v"(gm[0]), "v"(gm[1]), "v"(km), "v"(kbm), "v"(mx), "v"(lx), "v"(tg));
    float wc = __builtin_amdgcn_exp2f(mc - row_max16(mc));
    wc *= __builtin_amdgcn_rcpf(row_sum16(wc * lc));
    const float wt = dpp<0x158>(wc);
    o0 = wt * vt0;
    o1 = wt * vt1;
#define ACC(S) { unsigned t_, x0_, x1_;                                                                                            \
    asm volatile("v_bitop3_b32 %3, %6, %7, %13 bitop3:0x7e\n\tv_bfi_b32 %4, %8, %6, %10\n\tv_bfi_b32 %5, %8, %7, %11\n\t"          \
                 "v_and_or_b32 %2, %3, %9, %2\n\tv_fmac_f32_dpp %0, %12, %4 row_newbcast:" #S " row_mask:0xf bank_mask:0xf\n\t"    \
                 "v_fmac_f32_dpp %1, %12, %5 row_newbcast:" #S " row_mask:0xf bank_mask:0xf"                                       \
                 : "+v"(o0), "+v"(o1), "+v"(bad), "=&v"(t_), "=&v"(x0_), "=&v"(x1_)                                                \
                 : "v"(gs[S][0]), "v"(gs[S][1]), "s"(ks[S]), "s"(kb[S]), "v"(oc0), "v"(oc1), "v"(wc), "v"(tg)); }
    ACC(0) ACC(1) ACC(2) ACC(3) ACC(4) ACC(5) ACC(6) ACC(7)
#undef ACC
    if (__all(bad == 0)) break;
  }
  const float rcv = dpp<0xB1>((lane & 1) ? o0 : o1);
  const bf16x2 pk = {(__bf16)((lane & 1) ? rcv : o0), (__bf16)((lane & 1) ? o1 : rcv)};
  __builtin_amdgcn_raw_buffer_store_b32(__builtin_bit_cast(unsigned, pk), ro, op, 0, 0);
  { unsigned long long te, now; RT(te); const unsigned long long tgt = te + (te - ta) * 4; do { RT(now); } while (now < tgt); }
}

extern "C" void kernel_launch(void* const* d_in, const int* in_sizes, int n_in, void* d_out, int out_size, void* d_ws,
                              size_t ws_size, hipStream_t stream) {
  constexpr unsigned kv_bytes = H * C * D * 2, flags = 0x00020000u;
  tree_attn_decode<<<H * SPLITS, WAVES * 64, 0, stream>>>(d_in[1], kv_bytes, flags, d_in[2], kv_bytes, flags, d_in[0],
                                                          (unsigned char*)d_ws, (const u32x4*)d_in[3],
                                                          (const unsigned short*)d_in[4], (const unsigned short*)d_in[5],
                                                          (__hip_bfloat16*)d_out);
}
```

```cpp
#include <hip/hip_runtime.h>
#include <hip/hip_bf16.h>
#include <stdint.h>


namespace {

constexpr int H = 32, C = 4096, D = 128, SPLITS = 8, WAVES = 8;
constexpr int NT = C / SPLITS / WAVES / 4;
constexpr int PRE = 3;
constexpr int KV_AUX = 3;
constexpr int SC1 = 16;
constexpr float SCALE = 0.088388347648318440f, LOG2E = 1.4426950408889634f;
constexpr float C2 = SCALE * LOG2E;

constexpr unsigned HSTR = 8192, REC = 512, O_ML = 4096, O_EP = 5120, O_CNT = 5248, WS_BYTES = H * HSTR;
constexpr unsigned KEEP = 0xffffff00u;

using bf16x2 = __attribute__((ext_vector_type(2))) __bf16;
using bf16x8 = __attribute__((ext_vector_type(8))) __bf16;
using u32x4 = __attribute__((ext_vector_type(4))) unsigned;
using u32x2 = __attribute__((ext_vector_type(2))) unsigned;
using i32x8 = __attribute__((ext_vector_type(8))) int;
using i32x4 = __attribute__((ext_vector_type(4))) int;
using f32x4 = __attribute__((ext_vector_type(4))) float;
using f32x2 = __attribute__((ext_vector_type(2))) float;
typedef __attribute__((address_space(1))) unsigned long long gu64;
typedef __attribute__((address_space(1))) u32x4 gu32x4;

__device__ __forceinline__ float dot2(unsigned k, unsigned q, float acc) {
  return __builtin_amdgcn_fdot2_f32_bf16(__builtin_bit_cast(bf16x2, k), __builtin_bit_cast(bf16x2, q), acc, false);
}
__device__ __forceinline__ float dot8(const u32x4& k, const u32x4& q) {
  return dot2(k[3], q[3], dot2(k[2], q[2], dot2(k[1], q[1], dot2(k[0], q[0], 0.f))));
}
template <int CTRL> __device__ __forceinline__ float dpp(float x) {
  return __builtin_bit_cast(float, __builtin_amdgcn_mov_dpp(__builtin_bit_cast(int, x), CTRL, 0xf, 0xf, true));
}
__device__ __forceinline__ float row_sum16(float x) {
  x += dpp<0x128>(x); x += dpp<0x124>(x); x += dpp<0x122>(x); x += dpp<0x121>(x);
  return x;
}
__device__ __forceinline__ float row_max16(float x) {
  asm volatile("s_nop 1\n\tv_max_f32_dpp %0, %0, %0 row_ror:8 row_mask:0xf bank_mask:0xf\n\ts_nop 1\n\t"
               "v_max_f32_dpp %0, %0, %0 row_ror:4 row_mask:0xf bank_mask:0xf\n\ts_nop 1\n\t"
               "v_max_f32_dpp %0, %0, %0 row_ror:2 row_mask:0xf bank_mask:0xf\n\ts_nop 1\n\t"
               "v_max_f32_dpp %0, %0, %0 row_ror:1 row_mask:0xf bank_mask:0xf" : "+v"(x));
  return x;
}
__device__ __forceinline__ float xrow_max(float x) {
  auto s = __builtin_amdgcn_permlane16_swap(__float_as_uint(x), __float_as_uint(x), false, false);
  x = fmaxf(__uint_as_float(s[0]), __uint_as_float(s[1]));
  auto t = __builtin_amdgcn_permlane32_swap(__float_as_uint(x), __float_as_uint(x), false, false);
  return fmaxf(__uint_as_float(t[0]), __uint_as_float(t[1]));
}
__device__ __forceinline__ float xrow_sum(float x) {
  auto s = __builtin_amdgcn_permlane16_swap(__float_as_uint(x), __float_as_uint(x), false, false);
  x = __uint_as_float(s[0]) + __uint_as_float(s[1]);
  auto t = __builtin_amdgcn_permlane32_swap(__float_as_uint(x), __float_as_uint(x), false, false);
  return __uint_as_float(t[0]) + __uint_as_float(t[1]);
}
__device__ __forceinline__ float bf2f(unsigned short h) { return __uint_as_float((unsigned)h << 16); }

}

__device__ i32x4 buf_load16(i32x4 rsrc, int voffset, int soffset, int aux) __asm("llvm.amdgcn.raw.buffer.load.v4i32");
#define SB() __builtin_amdgcn_sched_barrier(0)

__global__ void __launch_bounds__(WAVES * 64) tree_attn_decode(const void* K, unsigned kn, unsigned kf, const void* V, unsigned vn, unsigned vf,
                                                               const void* q, unsigned char* ws, const u32x4* __restrict__ kt,
                                                               const unsigned short* __restrict__ vt,
                                                               const unsigned short* __restrict__ mask, __hip_bfloat16* out) {
  __shared__ __attribute__((aligned(16))) float lds[WAVES * 128 + 2 * WAVES];
  i32x8 ka;
  unsigned long long qa;
  unsigned so, vo, head, tmp;
  asm volatile("s_load_dwordx8 %0, %6, 0x0\n\t"
               "s_load_dwordx2 %1, %6, 0x20\n\t"
               "v_readfirstlane_b32 %2, %8\n\t"
               "s_and_b32 %4, %7, 7\n\t"
               "s_bfe_u32 %5, %7, 0x20003\n\t"
               "s_lshl2_add_u32 %4, %4, %5\n\t"
               "s_lshr_b32 %5, %7, 5\n\t"
               "s_lshl3_add_u32 %5, %4, %5\n\t"
               "s_lshl_b32 %5, %5, 9\n\t"
               "s_add_u32 %2, %2, %5\n\t"
               "s_lshl_b32 %2, %2, 8\n\t"
               "v_and_b32 %3, 63, %8\n\t"
               "v_lshlrev_b32 %3, 4, %3\n\t"
               "s_waitcnt lgkmcnt(0)"
               : "=&s"(ka), "=&s"(qa), "=&s"(so), "=&v"(vo), "=&s"(head), "=&s"(tmp)
               : "s"(__builtin_amdgcn_kernarg_segment_ptr()), "s"(blockIdx.x), "v"(threadIdx.x)
               : "scc");
  const i32x4 rk = __builtin_shufflevector(ka, ka, 0, 1, 2, 3), rv = __builtin_shufflevector(ka, ka, 4, 5, 6, 7);
  u32x4 k[NT], v[NT];
#define LDX(r, t) __builtin_bit_cast(u32x4, buf_load16(r, vo + ((t) & 3) * 1024, so + ((t) >> 2) * 4096, KV_AUX))
  k[0] = LDX(rk, 0); SB();
  v[0] = LDX(rv, 0); SB();
#pragma unroll
  for (int t = 1; t < 4; ++t) { k[t] = LDX(rk, t); SB(); }
#pragma unroll
  for (int t = 1; t < PRE; ++t) { v[t] = LDX(rv, t); SB(); }
  const unsigned split = blockIdx.x >> 5, lane = threadIdx.x & 63, wave = __builtin_amdgcn_readfirstlane(threadIdx.x >> 6);
  const unsigned c = lane & 15, g = lane >> 4, n = c & 7;
  const u32x4 qv = *(const gu32x4*)(qa + (head * 16 + c) * 16u);
#pragma unroll
  for (int t = 4; t < NT; ++t) k[t] = LDX(rk, t);
  SB();

  float x[NT];
#pragma unroll
  for (int t = 0; t < NT; ++t) x[t] = row_sum16(dot8(k[t], qv)) * C2;
  float m = x[0];
#pragma unroll
  for (int t = 1; t < NT; ++t) m = fmaxf(m, x[t]);
  m = xrow_max(m);
  const unsigned lomask = c >= 8 ? 0xffff0000u : 0u;
  unsigned sel[4];
#pragma unroll
  for (int r = 0; r < 4; ++r) sel[r] = (n >> 1) == r ? ((n & 1) ? 0x03020c0cu : 0x0c0c0302u) : 0x0c0c0c0cu;
  float l = 0.f;
  u32x4 b[NT];
#pragma unroll
  for (int t = 0; t < NT; ++t) {
    const float p = __builtin_amdgcn_exp2f(x[t] - m);
    l += p;
    const unsigned s = __float_as_uint(p - __uint_as_float(__float_as_uint(p) & lomask));
#pragma unroll
    for (int r = 0; r < 4; ++r) b[t][r] = __builtin_amdgcn_perm(s, s, sel[r]);
    asm volatile("" : "+v"(b[t]));
  }
  l = xrow_sum(l);
  float* lm = lds + WAVES * 128;
  if (lane == 0) *reinterpret_cast<f32x2*>(lm + 2 * wave) = f32x2{m, l};
  SB();
  asm volatile("" : "+v"(vo));
#pragma unroll
  for (int t = PRE; t < NT; ++t) { v[t] = LDX(rv, t); SB(); }

  const unsigned d0 = 32 * g + 16 * (c >> 3) + n;
  const unsigned hb = head * HSTR;
  const auto rw = __builtin_amdgcn_make_buffer_rsrc((void*)ws, 0, wave == 0 ? WS_BYTES : 0u, 0x00020000);
  float mx = 0.f, vt0 = 0.f, vt1 = 0.f;
  unsigned tag = 0;
  if (wave == 0) {
    const unsigned epoch = __builtin_amdgcn_raw_buffer_load_b32(rw, 0, hb + O_EP, SC1);
    const u32x2 w0 = __builtin_amdgcn_raw_buffer_load_b64(rw, 0, hb + O_CNT, SC1);
    vt0 = bf2f(vt[head * D + d0]);
    vt1 = bf2f(vt[head * D + d0 + 8]);
    const float st = (row_sum16(dot8(kt[head * 16 + c], qv)) * SCALE + (1.0f - bf2f(mask[0])) * -1000000000.0f) * LOG2E;
    mx = c == 8 ? st : -INFINITY;
    tag = epoch + 1u;
    if (lane == 0 && w0[1] != tag) {
      unsigned long long e = ((unsigned long long)w0[1] << 32) | w0[0];
      __hip_atomic_compare_exchange_strong((gu64*)(ws + hb + O_CNT), &e, (unsigned long long)tag << 32, __ATOMIC_RELAXED,
                                           __ATOMIC_RELAXED, __HIP_MEMORY_SCOPE_AGENT);
    }
    asm volatile("s_waitcnt vmcnt(0)" : "+v"(mx), "+v"(vt0), "+v"(vt1), "+v"(tag) :: "memory");
  }
  SB();
  __syncthreads();

  const f32x2 ml = *reinterpret_cast<const f32x2*>(lm + 2 * n);
  const float MW = row_max16(ml[0]);
  const float L = __uint_as_float(__float_as_uint(row_sum16(__builtin_amdgcn_exp2f(ml[0] - MW) * ml[1]) * 0.5f) & KEEP);
  float fw = __builtin_amdgcn_exp2f(m - MW);
  unsigned tg = tag & 0xffu, vo8 = lane * 8u;
  __builtin_amdgcn_raw_buffer_store_b64(u32x2{__float_as_uint(MW), __float_as_uint(L) | tg}, rw, lane < 16 ? vo8 : WS_BYTES,
                                        hb + O_ML + split * 128, SC1);
  float lx = c == split ? L : 1.0f;
  mx = c == split ? MW : mx;
  unsigned km = (c < 8 && c != split) ? 0xffffffffu : 0u, kbm = n != split ? 0xffu : 0u;
  unsigned mlo = n != split ? n * 128u + (lane >> 3) * 8u : WS_BYTES;
  unsigned ato = lane == 0 ? hb + O_CNT : WS_BYTES, epo = lane == 0 ? hb + O_EP : WS_BYTES;
  unsigned op = (head * D + d0 + ((lane & 1) ? 7 : 0)) * 2u;
  int old = 1;
  unsigned ks[SPLITS], kb[SPLITS], sb[SPLITS];
#pragma unroll
  for (int s = 0; s < SPLITS; ++s) {
    ks[s] = s == split ? 0u : KEEP; kb[s] = s == split ? 0u : 0xffu; sb[s] = s == split ? WS_BYTES : hb + s * REC;
    asm volatile("" : "+s"(ks[s]), "+s"(kb[s]), "+s"(sb[s]));
  }
  unsigned mine = hb + split * REC, mlb = hb + O_ML;
  const auto ro = __builtin_amdgcn_make_buffer_rsrc((void*)out, 0, H * D * 2, 0x00020000);
  asm volatile("" : "+v"(fw), "+v"(lx), "+v"(mx), "+v"(km), "+v"(kbm), "+v"(vo8), "+v"(mlo), "+v"(ato), "+v"(epo), "+v"(tg), "+v"(old), "+v"(op));
  asm volatile("" : "+s"(mine), "+s"(mlb) : "s"(ro));
  SB();

  f32x4 acc = {0.f, 0.f, 0.f, 0.f};
#pragma unroll
  for (int t = 0; t < NT; ++t)
    acc = __builtin_amdgcn_mfma_f32_16x16x32_bf16(__builtin_bit_cast(bf16x8, v[t]), __builtin_bit_cast(bf16x8, b[t]), acc, 0, 0, 0);
#pragma unroll
  for (int r = 0; r < 4; ++r) acc[r] += dpp<0x128>(acc[r]);
  f32x2 o2 = {c < 8 ? acc[0] : acc[2], c < 8 ? acc[1] : acc[3]};
  o2 *= fw;
  *reinterpret_cast<f32x2*>(lds + wave * 128 + lane * 2) = o2;
  __syncthreads();
  if (wave != 0) return;

  old = __builtin_amdgcn_raw_ptr_buffer_atomic_add_i32(old, rw, ato, 0, 0);
  u32x2 gm = __builtin_amdgcn_raw_buffer_load_b64(rw, mlo, mlb, SC1);
  SB();
  f32x2 ow[WAVES];
#pragma unroll
  for (int w = 0; w < WAVES; ++w) ow[w] = *reinterpret_cast<const f32x2*>(reinterpret_cast<const char*>(lds) + vo8 + w * 512);
#pragma unroll
  for (int s = 1; s < WAVES; s *= 2)
#pragma unroll
    for (int w = 0; w < WAVES; w += 2 * s) ow[w] += ow[w + s];
  const unsigned oc0 = __float_as_uint(ow[0][0]) & KEEP, oc1 = __float_as_uint(ow[0][1]) & KEEP;
  __builtin_amdgcn_raw_buffer_store_b64(u32x2{oc0 | tg, oc1 | tg}, rw, vo8, mine, SC1);
  SB();
  if (__builtin_amdgcn_readfirstlane(old) != SPLITS - 1) return;

  float o0, o1;
#pragma nounroll
  for (unsigned pass = 0; pass < (1u << 20); ++pass) {
    asm volatile("" ::: "memory");
    u32x2 gs[SPLITS];
#pragma unroll
    for (int s = 0; s < SPLITS; ++s) gs[s] = __builtin_amdgcn_raw_buffer_load_b64(rw, vo8, sb[s], SC1);
    __builtin_amdgcn_raw_buffer_store_b32(tag, rw, epo, 0, SC1);
    SB();
    unsigned bad;
    float mc, lc;
    asm volatile("v_xor_b32 %2, %4, %9\n\tv_bfi_b32 %1, %5, %3, %7\n\tv_and_b32 %0, %2, %6\n\tv_bfi_b32 %2, %5, %2, %8"
                 : "=&v"(bad), "=&v"(mc), "=&v"(lc) : "v"(gm[0]), "v"(gm[1]), "v"(km), "v"(kbm), "v"(mx), "v"(lx), "v"(tg));
    float wc = __builtin_amdgcn_exp2f(mc - row_max16(mc));
    wc *= __builtin_amdgcn_rcpf(row_sum16(wc * lc));
    const float wt = dpp<0x158>(wc);
    o0 = wt * vt0;
    o1 = wt * vt1;
#define ACC(S) { unsigned t_, x0_, x1_;                                                                                            \
    asm volatile("v_bitop3_b32 %3, %6, %7, %13 bitop3:0x7e\n\tv_bfi_b32 %4, %8, %6, %10\n\tv_bfi_b32 %5, %8, %7, %11\n\t"          \
                 "v_and_or_b32 %2, %3, %9, %2\n\tv_fmac_f32_dpp %0, %12, %4 row_newbcast:" #S " row_mask:0xf bank_mask:0xf\n\t"    \
                 "v_fmac_f32_dpp %1, %12, %5 row_newbcast:" #S " row_mask:0xf bank_mask:0xf"                                       \
                 : "+v"(o0), "+v"(o1), "+v"(bad), "=&v"(t_), "=&v"(x0_), "=&v"(x1_)                                                \
                 : "v"(gs[S][0]), "v"(gs[S][1]), "s"(ks[S]), "s"(kb[S]), "v"(oc0), "v"(oc1), "v"(wc), "v"(tg)); }
    ACC(0) ACC(1) ACC(2) ACC(3) ACC(4) ACC(5) ACC(6) ACC(7)
#undef ACC
    if (__all(bad == 0)) break;
    gm = __builtin_amdgcn_raw_buffer_load_b64(rw, mlo, mlb, SC1);
    asm volatile("" : "+v"(gm));
  }
  const float rcv = dpp<0xB1>((lane & 1) ? o0 : o1);
  const bf16x2 pk = {(__bf16)((lane & 1) ? rcv : o0), (__bf16)((lane & 1) ? o1 : rcv)};
  __builtin_amdgcn_raw_buffer_store_b32(__builtin_bit_cast(unsigned, pk), ro, op, 0, 0);
}

extern "C" void kernel_launch(void* const* d_in, const int* in_sizes, int n_in, void* d_out, int out_size, void* d_ws,
                              size_t ws_size, hipStream_t stream) {
  constexpr unsigned kv_bytes = H * C * D * 2, flags = 0x00020000u;
  tree_attn_decode<<<H * SPLITS, WAVES * 64, 0, stream>>>(d_in[1], kv_bytes, flags, d_in[2], kv_bytes, flags, d_in[0],
                                                          (unsigned char*)d_ws, (const u32x4*)d_in[3],
                                                          (const unsigned short*)d_in[4], (const unsigned short*)d_in[5],
                                                          (__hip_bfloat16*)d_out);
}
```

```cpp
#include <hip/hip_runtime.h>
#include <hip/hip_bf16.h>
#include <stdint.h>


namespace {

constexpr int H = 32, C = 4096, D = 128, SPLITS = 8, WAVES = 8;
constexpr int NT = C / SPLITS / WAVES / 4;
constexpr int PRE = 3;
constexpr int KV_AUX = 3;
constexpr int SC1 = 16;
constexpr float SCALE = 0.088388347648318440f, LOG2E = 1.4426950408889634f;
constexpr float C2 = SCALE * LOG2E;

constexpr unsigned HSTR = 8192, REC = 512, O_ML = 4096, O_EP = 5120, O_CNT = 5248, WS_BYTES = H * HSTR;
constexpr unsigned KEEP = 0xffffff00u;

using bf16x2 = __attribute__((ext_vector_type(2))) __bf16;
using bf16x8 = __attribute__((ext_vector_type(8))) __bf16;
using u32x4 = __attribute__((ext_vector_type(4))) unsigned;
using u32x2 = __attribute__((ext_vector_type(2))) unsigned;
using i32x8 = __attribute__((ext_vector_type(8))) int;
using i32x4 = __attribute__((ext_vector_type(4))) int;
using f32x4 = __attribute__((ext_vector_type(4))) float;
using f32x2 = __attribute__((ext_vector_type(2))) float;
typedef __attribute__((address_space(1))) unsigned long long gu64;
typedef __attribute__((address_space(1))) u32x4 gu32x4;

__device__ __forceinline__ float dot2(unsigned k, unsigned q, float acc) {
  return __builtin_amdgcn_fdot2_f32_bf16(__builtin_bit_cast(bf16x2, k), __builtin_bit_cast(bf16x2, q), acc, false);
}
__device__ __forceinline__ float dot8(const u32x4& k, const u32x4& q) {
  return dot2(k[3], q[3], dot2(k[2], q[2], dot2(k[1], q[1], dot2(k[0], q[0], 0.f))));
}
template <int CTRL> __device__ __forceinline__ float dpp(float x) {
  return __builtin_bit_cast(float, __builtin_amdgcn_mov_dpp(__builtin_bit_cast(int, x), CTRL, 0xf, 0xf, true));
}
__device__ __forceinline__ float row_sum16(float x) {
  x += dpp<0x128>(x); x += dpp<0x124>(x); x += dpp<0x122>(x); x += dpp<0x121>(x);
  return x;
}
__device__ __forceinline__ float row_max16(float x) {
  asm volatile("s_nop 1\n\tv_max_f32_dpp %0, %0, %0 row_ror:8 row_mask:0xf bank_mask:0xf\n\ts_nop 1\n\t"
               "v_max_f32_dpp %0, %0, %0 row_ror:4 row_mask:0xf bank_mask:0xf\n\ts_nop 1\n\t"
               "v_max_f32_dpp %0, %0, %0 row_ror:2 row_mask:0xf bank_mask:0xf\n\ts_nop 1\n\t"
               "v_max_f32_dpp %0, %0, %0 row_ror:1 row_mask:0xf bank_mask:0xf" : "+v"(x));
  return x;
}
__device__ __forceinline__ float xrow_max(float x) {
  auto s = __builtin_amdgcn_permlane16_swap(__float_as_uint(x), __float_as_uint(x), false, false);
  x = fmaxf(__uint_as_float(s[0]), __uint_as_float(s[1]));
  auto t = __builtin_amdgcn_permlane32_swap(__float_as_uint(x), __float_as_uint(x), false, false);
  return fmaxf(__uint_as_float(t[0]), __uint_as_float(t[1]));
}
__device__ __forceinline__ float xrow_sum(float x) {
  auto s = __builtin_amdgcn_permlane16_swap(__float_as_uint(x), __float_as_uint(x), false, false);
  x = __uint_as_float(s[0]) + __uint_as_float(s[1]);
  auto t = __builtin_amdgcn_permlane32_swap(__float_as_uint(x), __float_as_uint(x), false, false);
  return __uint_as_float(t[0]) + __uint_as_float(t[1]);
}
__device__ __forceinline__ float bf2f(unsigned short h) { return __uint_as_float((unsigned)h << 16); }

}

__device__ i32x4 buf_load16(i32x4 rsrc, int voffset, int soffset, int aux) __asm("llvm.amdgcn.raw.buffer.load.v4i32");
#define SB() __builtin_amdgcn_sched_barrier(0)

__global__ void __launch_bounds__(768) tree_attn_decode(const void* K, unsigned kn, unsigned kf, const void* V, unsigned vn, unsigned vf,
                                                               const void* q, unsigned char* ws, const u32x4* __restrict__ kt,
                                                               const unsigned short* __restrict__ vt,
                                                               const unsigned short* __restrict__ mask, __hip_bfloat16* out) {
  __shared__ __attribute__((aligned(16))) float lds[WAVES * 128 + 2 * WAVES];
  i32x8 ka;
  unsigned long long qa;
  unsigned so, vo, head, tmp;
  asm volatile("s_load_dwordx8 %0, %6, 0x0\n\t"
               "s_load_dwordx2 %1, %6, 0x20\n\t"
               "v_readfirstlane_b32 %2, %8\n\t"
               "s_and_b32 %4, %7, 7\n\t"
               "s_cmpk_lt_u32 %2, 0x200\n\t"
               "s_cbranch_scc1 1\n\t"
               "s_endpgm\n\t"
               "s_bfe_u32 %5, %7, 0x20003\n\t"
               "s_lshl2_add_u32 %4, %4, %5\n\t"
               "s_lshr_b32 %5, %7, 5\n\t"
               "s_lshl3_add_u32 %5, %4, %5\n\t"
               "s_lshl_b32 %5, %5, 9\n\t"
               "s_add_u32 %2, %2, %5\n\t"
               "s_lshl_b32 %2, %2, 8\n\t"
               "v_and_b32 %3, 63, %8\n\t"
               "v_lshlrev_b32 %3, 4, %3\n\t"
               "s_waitcnt lgkmcnt(0)"
               : "=&s"(ka), "=&s"(qa), "=&s"(so), "=&v"(vo), "=&s"(head), "=&s"(tmp)
               : "s"(__builtin_amdgcn_kernarg_segment_ptr()), "s"(blockIdx.x), "v"(threadIdx.x)
               : "scc");
  const i32x4 rk = __builtin_shufflevector(ka, ka, 0, 1, 2, 3), rv = __builtin_shufflevector(ka, ka, 4, 5, 6, 7);
  u32x4 k[NT], v[NT];
#define LDX(r, t) __builtin_bit_cast(u32x4, buf_load16(r, vo + ((t) & 3) * 1024, so + ((t) >> 2) * 4096, KV_AUX))
  k[0] = LDX(rk, 0); SB();
  v[0] = LDX(rv, 0); SB();
#pragma unroll
  for (int t = 1; t < 4; ++t) { k[t] = LDX(rk, t); SB(); }
#pragma unroll
  for (int t = 1; t < PRE; ++t) { v[t] = LDX(rv, t); SB(); }
  const unsigned split = blockIdx.x >> 5, lane = threadIdx.x & 63, wave = __builtin_amdgcn_readfirstlane(threadIdx.x >> 6);
  const unsigned c = lane & 15, g = lane >> 4, n = c & 7;
  const u32x4 qv = *(const gu32x4*)(qa + (head * 16 + c) * 16u);
#pragma unroll
  for (int t = 4; t < NT; ++t) k[t] = LDX(rk, t);
  SB();

  float x[NT];
#pragma unroll
  for (int t = 0; t < NT; ++t) x[t] = row_sum16(dot8(k[t], qv)) * C2;
  float m = x[0];
#pragma unroll
  for (int t = 1; t < NT; ++t) m = fmaxf(m, x[t]);
  m = xrow_max(m);
  const unsigned lomask = c >= 8 ? 0xffff0000u : 0u;
  unsigned sel[4];
#pragma unroll
  for (int r = 0; r < 4; ++r) sel[r] = (n >> 1) == r ? ((n & 1) ? 0x03020c0cu : 0x0c0c0302u) : 0x0c0c0c0cu;
  float l = 0.f;
  u32x4 b[NT];
#pragma unroll
  for (int t = 0; t < NT; ++t) {
    const float p = __builtin_amdgcn_exp2f(x[t] - m);
    l += p;
    const unsigned s = __float_as_uint(p - __uint_as_float(__float_as_uint(p) & lomask));
#pragma unroll
    for (int r = 0; r < 4; ++r) b[t][r] = __builtin_amdgcn_perm(s, s, sel[r]);
    asm volatile("" : "+v"(b[t]));
  }
  l = xrow_sum(l);
  float* lm = lds + WAVES * 128;
  if (lane == 0) *reinterpret_cast<f32x2*>(lm + 2 * wave) = f32x2{m, l};
  SB();
  asm volatile("" : "+v"(vo));
#pragma unroll
  for (int t = PRE; t < NT; ++t) { v[t] = LDX(rv, t); SB(); }

  const unsigned d0 = 32 * g + 16 * (c >> 3) + n;
  const unsigned hb = head * HSTR;
  const auto rw = __builtin_amdgcn_make_buffer_rsrc((void*)ws, 0, wave == 0 ? WS_BYTES : 0u, 0x00020000);
  float mx = 0.f, vt0 = 0.f, vt1 = 0.f;
  unsigned tag = 0;
  if (wave == 0) {
    const unsigned epoch = __builtin_amdgcn_raw_buffer_load_b32(rw, 0, hb + O_EP, SC1);
    const u32x2 w0 = __builtin_amdgcn_raw_buffer_load_b64(rw, 0, hb + O_CNT, SC1);
    vt0 = bf2f(vt[head * D + d0]);
    vt1 = bf2f(vt[head * D + d0 + 8]);
    const float st = (row_sum16(dot8(kt[head * 16 + c], qv)) * SCALE + (1.0f - bf2f(mask[0])) * -1000000000.0f) * LOG2E;
    mx = c == 8 ? st : -INFINITY;
    tag = epoch + 1u;
    if (lane == 0 && w0[1] != tag) {
      unsigned long long e = ((unsigned long long)w0[1] << 32) | w0[0];
      __hip_atomic_compare_exchange_strong((gu64*)(ws + hb + O_CNT), &e, (unsigned long long)tag << 32, __ATOMIC_RELAXED,
                                           __ATOMIC_RELAXED, __HIP_MEMORY_SCOPE_AGENT);
    }
    asm volatile("s_waitcnt vmcnt(0)" : "+v"(mx), "+v"(vt0), "+v"(vt1), "+v"(tag) :: "memory");
  }
  SB();
  __syncthreads();

  const f32x2 ml = *reinterpret_cast<const f32x2*>(lm + 2 * n);
  const float MW = row_max16(ml[0]);
  const float L = __uint_as_float(__float_as_uint(row_sum16(__builtin_amdgcn_exp2f(ml[0] - MW) * ml[1]) * 0.5f) & KEEP);
  float fw = __builtin_amdgcn_exp2f(m - MW);
  unsigned tg = tag & 0xffu, vo8 = lane * 8u;
  __builtin_amdgcn_raw_buffer_store_b64(u32x2{__float_as_uint(MW), __float_as_uint(L) | tg}, rw, lane < 16 ? vo8 : WS_BYTES,
                                        hb + O_ML + split * 128, SC1);
  float lx = c == split ? L : 1.0f;
  mx = c == split ? MW : mx;
  unsigned km = (c < 8 && c != split) ? 0xffffffffu : 0u, kbm = n != split ? 0xffu : 0u;
  unsigned mlo = n != split ? n * 128u + (lane >> 3) * 8u : WS_BYTES;
  unsigned ato = lane == 0 ? hb + O_CNT : WS_BYTES, epo = lane == 0 ? hb + O_EP : WS_BYTES;
  unsigned op = (head * D + d0 + ((lane & 1) ? 7 : 0)) * 2u;
  int old = 1;
  unsigned ks[SPLITS], kb[SPLITS], sb[SPLITS];
#pragma unroll
  for (int s = 0; s < SPLITS; ++s) {
    ks[s] = s == split ? 0u : KEEP; kb[s] = s == split ? 0u : 0xffu; sb[s] = s == split ? WS_BYTES : hb + s * REC;
    asm volatile("" : "+s"(ks[s]), "+s"(kb[s]), "+s"(sb[s]));
  }
  unsigned mine = hb + split * REC, mlb = hb + O_ML;
  const auto ro = __builtin_amdgcn_make_buffer_rsrc((void*)out, 0, H * D * 2, 0x00020000);
  asm volatile("" : "+v"(fw), "+v"(lx), "+v"(mx), "+v"(km), "+v"(kbm), "+v"(vo8), "+v"(mlo), "+v"(ato), "+v"(epo), "+v"(tg), "+v"(old), "+v"(op));
  asm volatile("" : "+s"(mine), "+s"(mlb) : "s"(ro));
  SB();

  f32x4 acc = {0.f, 0.f, 0.f, 0.f};
#pragma unroll
  for (int t = 0; t < NT; ++t)
    acc = __builtin_amdgcn_mfma_f32_16x16x32_bf16(__builtin_bit_cast(bf16x8, v[t]), __builtin_bit_cast(bf16x8, b[t]), acc, 0, 0, 0);
#pragma unroll
  for (int r = 0; r < 4; ++r) acc[r] += dpp<0x128>(acc[r]);
  f32x2 o2 = {c < 8 ? acc[0] : acc[2], c < 8 ? acc[1] : acc[3]};
  o2 *= fw;
  *reinterpret_cast<f32x2*>(lds + wave * 128 + lane * 2) = o2;
  __syncthreads();
  if (wave != 0) return;

  old = __builtin_amdgcn_raw_ptr_buffer_atomic_add_i32(old, rw, ato, 0, 0);
  u32x2 gm = __builtin_amdgcn_raw_buffer_load_b64(rw, mlo, mlb, SC1);
  SB();
  f32x2 ow[WAVES];
#pragma unroll
  for (int w = 0; w < WAVES; ++w) ow[w] = *reinterpret_cast<const f32x2*>(reinterpret_cast<const char*>(lds) + vo8 + w * 512);
#pragma unroll
  for (int s = 1; s < WAVES; s *= 2)
#pragma unroll
    for (int w = 0; w < WAVES; w += 2 * s) ow[w] += ow[w + s];
  const unsigned oc0 = __float_as_uint(ow[0][0]) & KEEP, oc1 = __float_as_uint(ow[0][1]) & KEEP;
  __builtin_amdgcn_raw_buffer_store_b64(u32x2{oc0 | tg, oc1 | tg}, rw, vo8, mine, SC1);
  SB();
  if (__builtin_amdgcn_readfirstlane(old) != SPLITS - 1) return;

  float o0, o1;
#pragma nounroll
  for (unsigned pass = 0; pass < (1u << 20); ++pass) {
    asm volatile("" ::: "memory");
    u32x2 gs[SPLITS];
#pragma unroll
    for (int s = 0; s < SPLITS; ++s) gs[s] = __builtin_amdgcn_raw_buffer_load_b64(rw, vo8, sb[s], SC1);
    __builtin_amdgcn_raw_buffer_store_b32(tag, rw, epo, 0, SC1);
    SB();
    unsigned bad;
    float mc, lc;
    asm volatile("v_xor_b32 %2, %4, %9\n\tv_bfi_b32 %1, %5, %3, %7\n\tv_and_b32 %0, %2, %6\n\tv_bfi_b32 %2, %5, %2, %8"
                 : "=&v"(bad), "=&v"(mc), "=&v"(lc) : "v"(gm[0]), "v"(gm[1]), "v"(km), "v"(kbm), "v"(mx), "v"(lx), "v"(tg));
    float wc = __builtin_amdgcn_exp2f(mc - row_max16(mc));
    wc *= __builtin_amdgcn_rcpf(row_sum16(wc * lc));
    const float wt = dpp<0x158>(wc);
    o0 = wt * vt0;
    o1 = wt * vt1;
#define ACC(S) { unsigned t_, x0_, x1_;                                                                                            \
    asm volatile("v_bitop3_b32 %3, %6, %7, %13 bitop3:0x7e\n\tv_bfi_b32 %4, %8, %6, %10\n\tv_bfi_b32 %5, %8, %7, %11\n\t"          \
                 "v_and_or_b32 %2, %3, %9, %2\n\tv_fmac_f32_dpp %0, %12, %4 row_newbcast:" #S " row_mask:0xf bank_mask:0xf\n\t"    \
                 "v_fmac_f32_dpp %1, %12, %5 row_newbcast:" #S " row_mask:0xf bank_mask:0xf"                                       \
                 : "+v"(o0), "+v"(o1), "+v"(bad), "=&v"(t_), "=&v"(x0_), "=&v"(x1_)                                                \
                 : "v"(gs[S][0]), "v"(gs[S][1]), "s"(ks[S]), "s"(kb[S]), "v"(oc0), "v"(oc1), "v"(wc), "v"(tg)); }
    ACC(0) ACC(1) ACC(2) ACC(3) ACC(4) ACC(5) ACC(6) ACC(7)
#undef ACC
    if (__all(bad == 0)) break;
    gm = __builtin_amdgcn_raw_buffer_load_b64(rw, mlo, mlb, SC1);
    asm volatile("" : "+v"(gm));
  }
  const float rcv = dpp<0xB1>((lane & 1) ? o0 : o1);
  const bf16x2 pk = {(__bf16)((lane & 1) ? rcv : o0), (__bf16)((lane & 1) ? o1 : rcv)};
  __builtin_amdgcn_raw_buffer_store_b32(__builtin_bit_cast(unsigned, pk), ro, op, 0, 0);
}

extern "C" void kernel_launch(void* const* d_in, const int* in_sizes, int n_in, void* d_out, int out_size, void* d_ws,
                              size_t ws_size, hipStream_t stream) {
  constexpr unsigned kv_bytes = H * C * D * 2, flags = 0x00020000u;
  tree_attn_decode<<<H * SPLITS, 768, 0, stream>>>(d_in[1], kv_bytes, flags, d_in[2], kv_bytes, flags, d_in[0],
                                                          (unsigned char*)d_ws, (const u32x4*)d_in[3],
                                                          (const unsigned short*)d_in[4], (const unsigned short*)d_in[5],
                                                          (__hip_bfloat16*)d_out);
}
```

```cpp
#define PRE 16
#include <hip/hip_runtime.h>
#include <hip/hip_bf16.h>
#include <stdint.h>

#define SMPOS 16
#ifndef NV1
#define NV1 2
#endif

namespace {

constexpr int H = 32, C = 4096, D = 128, SPLITS = 8, WAVES = 4;
constexpr int NT = C / SPLITS / WAVES / 4;
constexpr int KV_AUX = 3;
constexpr int SC1 = 16;
constexpr float SCALE = 0.088388347648318440f, LOG2E = 1.4426950408889634f;
constexpr float C2 = SCALE * LOG2E;

constexpr unsigned HSTR = 8192, REC = 512, O_ML = 4096, O_EP = 5120, O_CNT = 5248, WS_BYTES = H * HSTR;
constexpr unsigned KEEP = 0xffffff00u;

using bf16x2 = __attribute__((ext_vector_type(2))) __bf16;
using bf16x8 = __attribute__((ext_vector_type(8))) __bf16;
using u32x4 = __attribute__((ext_vector_type(4))) unsigned;
using u32x2 = __attribute__((ext_vector_type(2))) unsigned;
using i32x8 = __attribute__((ext_vector_type(8))) int;
using u64x4 = __attribute__((ext_vector_type(4))) unsigned long long;
using u64x2 = __attribute__((ext_vector_type(2))) unsigned long long;
using i32x4 = __attribute__((ext_vector_type(4))) int;
using f32x4 = __attribute__((ext_vector_type(4))) float;
using f32x2 = __attribute__((ext_vector_type(2))) float;
typedef __attribute__((address_space(1))) unsigned long long gu64;
typedef __attribute__((address_space(1))) u32x4 gu32x4;

__device__ __forceinline__ float dot2(unsigned k, unsigned q, float acc) {
  return __builtin_amdgcn_fdot2_f32_bf16(__builtin_bit_cast(bf16x2, k), __builtin_bit_cast(bf16x2, q), acc, false);
}
__device__ __forceinline__ float dot8(const u32x4& k, const u32x4& q) {
  return dot2(k[3], q[3], dot2(k[2], q[2], dot2(k[1], q[1], dot2(k[0], q[0], 0.f))));
}
template <int CTRL> __device__ __forceinline__ float dpp(float x) {
  return __builtin_bit_cast(float, __builtin_amdgcn_mov_dpp(__builtin_bit_cast(int, x), CTRL, 0xf, 0xf, true));
}
__device__ __forceinline__ float row_sum16(float x) {
  x += dpp<0x128>(x); x += dpp<0x124>(x); x += dpp<0x122>(x); x += dpp<0x121>(x);
  return x;
}
__device__ __forceinline__ float row_max16(float x) {
  asm volatile("s_nop 1\n\tv_max_f32_dpp %0, %0, %0 row_ror:8 row_mask:0xf bank_mask:0xf\n\ts_nop 1\n\t"
               "v_max_f32_dpp %0, %0, %0 row_ror:4 row_mask:0xf bank_mask:0xf\n\ts_nop 1\n\t"
               "v_max_f32_dpp %0, %0, %0 row_ror:2 row_mask:0xf bank_mask:0xf\n\ts_nop 1\n\t"
               "v_max_f32_dpp %0, %0, %0 row_ror:1 row_mask:0xf bank_mask:0xf" : "+v"(x));
  return x;
}
__device__ __forceinline__ float xrow_max(float x) {
  auto s = __builtin_amdgcn_permlane16_swap(__float_as_uint(x), __float_as_uint(x), false, false);
  x = fmaxf(__uint_as_float(s[0]), __uint_as_float(s[1]));
  auto t = __builtin_amdgcn_permlane32_swap(__float_as_uint(x), __float_as_uint(x), false, false);
  return fmaxf(__uint_as_float(t[0]), __uint_as_float(t[1]));
}
__device__ __forceinline__ float xrow_sum(float x) {
  auto s = __builtin_amdgcn_permlane16_swap(__float_as_uint(x), __float_as_uint(x), false, false);
  x = __uint_as_float(s[0]) + __uint_as_float(s[1]);
  auto t = __builtin_amdgcn_permlane32_swap(__float_as_uint(x), __float_as_uint(x), false, false);
  return __uint_as_float(t[0]) + __uint_as_float(t[1]);
}
__device__ __forceinline__ float bf2f(unsigned short h) { return __uint_as_float((unsigned)h << 16); }

}

__device__ i32x4 buf_load16(i32x4 rsrc, int voffset, int soffset, int aux) __asm("llvm.amdgcn.raw.buffer.load.v4i32");
#define SB() __builtin_amdgcn_sched_barrier(0)

__global__ void __launch_bounds__(WAVES * 64) tree_attn_decode(const void* K_, unsigned kn, unsigned kf, const void* V_, unsigned vn, unsigned vf,
                                                               const void* q_, void* ws_, const void* kt_, const void* vt_,
                                                               const void* mask_, void* out_) {
  __shared__ __attribute__((aligned(16))) float lds[WAVES * 128 + 2 * WAVES];
  i32x8 ka;
  u64x4 kb_;
  u64x2 kc_;
  unsigned so, vo, head, tmp;
  asm volatile("s_load_dwordx8 %0, %6, 0x0\n\t"
               "s_load_dwordx8 %1, %6, 0x20\n\t"
               "v_readfirstlane_b32 %2, %8\n\t"
               "s_and_b32 %4, %7, 7\n\t"
               "s_bfe_u32 %5, %7, 0x20003\n\t"
               "s_lshl2_add_u32 %4, %4, %5\n\t"
               "s_lshr_b32 %5, %7, 5\n\t"
               "s_lshl3_add_u32 %5, %4, %5\n\t"
               "s_lshl_b32 %5, %5, 8\n\t"
               "s_add_u32 %2, %2, %5\n\t"
               "s_lshl_b32 %2, %2, 9\n\t"
               "v_and_b32 %3, 63, %8\n\t"
               "v_lshlrev_b32 %3, 4, %3\n\t"
               "s_waitcnt lgkmcnt(0)"
               : "=&s"(ka), "=&s"(kb_), "=&s"(so), "=&v"(vo), "=&s"(head), "=&s"(tmp)
               : "s"(__builtin_amdgcn_kernarg_segment_ptr()), "s"(blockIdx.x), "v"(threadIdx.x)
               : "scc");
  const i32x4 rk = __builtin_shufflevector(ka, ka, 0, 1, 2, 3), rv = __builtin_shufflevector(ka, ka, 4, 5, 6, 7);
  u32x4 k[NT], v[NT];
#define LDX(r, t) __builtin_bit_cast(u32x4, buf_load16(r, vo + ((t) & 3) * 1024, so + ((t) >> 2) * 4096, KV_AUX))
  k[0] = LDX(rk, 0); SB();
  v[0] = LDX(rv, 0); SB();
#pragma unroll
  for (int t = 1; t < 4; ++t) { k[t] = LDX(rk, t); SB(); }
#pragma unroll
  for (int t = 1; t <= NV1; ++t) { v[t] = LDX(rv, t); SB(); }
  asm volatile("s_load_dwordx4 %0, %1, 0x40" : "=s"(kc_) : "s"(__builtin_amdgcn_kernarg_segment_ptr()));
  SB();
  const unsigned split = blockIdx.x >> 5, lane = threadIdx.x & 63, wave = __builtin_amdgcn_readfirstlane(threadIdx.x >> 6);
  const unsigned c = lane & 15, g = lane >> 4, n = c & 7;
  const u32x4 qv = *(const gu32x4*)(kb_[0] + (head * 16 + c) * 16u);
#pragma unroll
  for (int t = 4; t < SMPOS; ++t) k[t] = LDX(rk, t);
  SB();
  asm volatile("s_waitcnt lgkmcnt(0)" : "+s"(kc_));
  unsigned char* const ws = (unsigned char*)kb_[1];
  const void *const kt = (const void*)kb_[2], *const vt = (const void*)kb_[3], *const mask = (const void*)kc_[0];
  void* const out = (void*)kc_[1];
  const unsigned d0 = 32 * g + 16 * (c >> 3) + n;
  const unsigned hb = head * HSTR, nsm = wave == 0 ? H * D * 2 + 16 : 0u;
  const auto rw = __builtin_amdgcn_make_buffer_rsrc((void*)ws, 0, wave == 0 ? WS_BYTES : 0u, 0x00020000);
  const unsigned epoch = __builtin_amdgcn_raw_buffer_load_b32(rw, 0, hb + O_EP, SC1);
  const u32x2 w0 = __builtin_amdgcn_raw_buffer_load_b64(rw, 0, hb + O_CNT, SC1);
  const u32x4 ktv = __builtin_amdgcn_raw_buffer_load_b128(__builtin_amdgcn_make_buffer_rsrc((void*)kt, 0, nsm, 0x00020000), c * 16, head * 256, 0);
  const auto rvt = __builtin_amdgcn_make_buffer_rsrc((void*)vt, 0, nsm, 0x00020000);
  const unsigned short vtb0 = __builtin_amdgcn_raw_buffer_load_b16(rvt, d0 * 2, head * 256, 0);
  const unsigned short vtb1 = __builtin_amdgcn_raw_buffer_load_b16(rvt, d0 * 2 + 16, head * 256, 0);
  const unsigned short mk = __builtin_amdgcn_raw_buffer_load_b16(__builtin_amdgcn_make_buffer_rsrc((void*)mask, 0, nsm ? 16 : 0, 0x00020000), 0, 0, 0);
  SB();
#pragma unroll
  for (int t = SMPOS; t < NT; ++t) k[t] = LDX(rk, t);
#pragma unroll
  for (int t = NV1 + 1; t < PRE; ++t) v[t] = LDX(rv, t);
  SB();

  float x[NT];
#pragma unroll
  for (int t = 0; t < NT; ++t) x[t] = row_sum16(dot8(k[t], qv)) * C2;
  float m = x[0];
#pragma unroll
  for (int t = 1; t < NT; ++t) m = fmaxf(m, x[t]);
  m = xrow_max(m);
  const unsigned lomask = c >= 8 ? 0xffff0000u : 0u;
  unsigned sel[4];
#pragma unroll
  for (int r = 0; r < 4; ++r) sel[r] = (n >> 1) == r ? ((n & 1) ? 0x03020c0cu : 0x0c0c0302u) : 0x0c0c0c0cu;
  float l = 0.f;
  unsigned ps[NT];
#pragma unroll
  for (int t = 0; t < NT; ++t) {
    const float p = __builtin_amdgcn_exp2f(x[t] - m);
    l += p;
    ps[t] = __float_as_uint(p - __uint_as_float(__float_as_uint(p) & lomask));
    asm volatile("" : "+v"(ps[t]));
  }
  asm volatile("" : "+v"(sel[0]), "+v"(sel[1]), "+v"(sel[2]), "+v"(sel[3]));
  l = xrow_sum(l);
  float* lm = lds + WAVES * 128;
  if (lane == 0) *reinterpret_cast<f32x2*>(lm + 2 * wave) = f32x2{m, l};
  float vt0 = bf2f(vtb0), vt1 = bf2f(vtb1);
  const float st = (row_sum16(dot8(ktv, qv)) * SCALE + (1.0f - bf2f(mk)) * -1000000000.0f) * LOG2E;
  float mx = c == 8 ? st : -INFINITY;
  unsigned tag = epoch + 1u;
  unsigned long long e = ((unsigned long long)w0[1] << 32) | w0[0];
  asm volatile("" : "+v"(mx), "+v"(vt0), "+v"(vt1), "+v"(tag));
  if (threadIdx.x == 0 && w0[1] != tag)
    __hip_atomic_compare_exchange_strong((gu64*)(ws + hb + O_CNT), &e, (unsigned long long)tag << 32, __ATOMIC_RELAXED,
                                         __ATOMIC_RELAXED, __HIP_MEMORY_SCOPE_AGENT);
  SB();
  asm volatile("" : "+v"(vo));
#pragma unroll
  for (int t = PRE; t < NT; ++t) { v[t] = LDX(rv, t); SB(); }

  SB();
  __syncthreads();

  const f32x2 ml = *reinterpret_cast<const f32x2*>(lm + 2 * (c & (WAVES - 1)));
  const float MW = row_max16(ml[0]);
  const float L = __uint_as_float(__float_as_uint(row_sum16(__builtin_amdgcn_exp2f(ml[0] - MW) * ml[1]) * (WAVES / 16.0f)) & KEEP);
  float fw = __builtin_amdgcn_exp2f(m - MW);
  unsigned tg = tag & 0xffu, vo8 = lane * 8u;
  __builtin_amdgcn_raw_buffer_store_b64(u32x2{__float_as_uint(MW), __float_as_uint(L) | tg}, rw, lane < 16 ? vo8 : WS_BYTES,
                                        hb + O_ML + split * 128, SC1);
  float lx = c == split ? L : 1.0f;
  mx = c == split ? MW : mx;
  unsigned km = (c < 8 && c != split) ? 0xffffffffu : 0u, kbm = n != split ? 0xffu : 0u;
  unsigned mlo = n != split ? n * 128u + (lane >> 3) * 8u : WS_BYTES;
  unsigned ato = lane == 0 ? hb + O_CNT : WS_BYTES, epo = lane == 0 ? hb + O_EP : WS_BYTES;
  unsigned op = (head * D + d0 + ((lane & 1) ? 7 : 0)) * 2u;
  int old = 1;
  unsigned ks[SPLITS], kb[SPLITS], sb[SPLITS];
#pragma unroll
  for (int s = 0; s < SPLITS; ++s) {
    ks[s] = s == split ? 0u : KEEP; kb[s] = s == split ? 0u : 0xffu; sb[s] = s == split ? WS_BYTES : hb + s * REC;
    asm volatile("" : "+s"(ks[s]), "+s"(kb[s]), "+s"(sb[s]));
  }
  unsigned mine = hb + split * REC, mlb = hb + O_ML;
  const auto ro = __builtin_amdgcn_make_buffer_rsrc((void*)out, 0, H * D * 2, 0x00020000);
  asm volatile("" : "+v"(fw), "+v"(lx), "+v"(mx), "+v"(km), "+v"(kbm), "+v"(vo8), "+v"(mlo), "+v"(ato), "+v"(epo), "+v"(tg), "+v"(old), "+v"(op));
  asm volatile("" : "+s"(mine), "+s"(mlb) : "s"(ro));
  SB();

  f32x4 acc = {0.f, 0.f, 0.f, 0.f};
#pragma unroll
  for (int t = 0; t < NT; ++t) {
    u32x4 b;
#pragma unroll
    for (int r = 0; r < 4; ++r) b[r] = __builtin_amdgcn_perm(ps[t], ps[t], sel[r]);
    asm volatile("" : "+v"(b));
    SB();
    asm volatile("s_nop 1\n\tv_mfma_f32_16x16x32_bf16 %0, %1, %2, %0" : "+v"(acc) : "v"(v[t]), "v"(b));
    SB();
  }
  asm volatile("s_nop 7\n\ts_nop 0" : "+v"(acc));
#pragma unroll
  for (int r = 0; r < 4; ++r) acc[r] += dpp<0x128>(acc[r]);
  f32x2 o2 = {c < 8 ? acc[0] : acc[2], c < 8 ? acc[1] : acc[3]};
  o2 *= fw;
  *reinterpret_cast<f32x2*>(lds + wave * 128 + lane * 2) = o2;
  asm volatile("" :: "v"(e));
  __syncthreads();
  if (wave != 0) return;

  old = __builtin_amdgcn_raw_ptr_buffer_atomic_add_i32(old, rw, ato, 0, 0);
  u32x2 gm = __builtin_amdgcn_raw_buffer_load_b64(rw, mlo, mlb, SC1);
  SB();
  f32x2 ow[WAVES];
#pragma unroll
  for (int w = 0; w < WAVES; ++w) ow[w] = *reinterpret_cast<const f32x2*>(reinterpret_cast<const char*>(lds) + vo8 + w * 512);
#pragma unroll
  for (int s = 1; s < WAVES; s *= 2)
#pragma unroll
    for (int w = 0; w < WAVES; w += 2 * s) ow[w] += ow[w + s];
  const unsigned oc0 = __float_as_uint(ow[0][0]) & KEEP, oc1 = __float_as_uint(ow[0][1]) & KEEP;
  __builtin_amdgcn_raw_buffer_store_b64(u32x2{oc0 | tg, oc1 | tg}, rw, vo8, mine, SC1);
  SB();
  if (__builtin_amdgcn_readfirstlane(old) != SPLITS - 1) {   return; }

  float o0, o1;
#pragma nounroll
  for (unsigned pass = 0; pass < (1u << 20); ++pass) {
    asm volatile("" ::: "memory");
    u32x2 gs[SPLITS];
#pragma unroll
    for (int s = 0; s < SPLITS; ++s) gs[s] = __builtin_amdgcn_raw_buffer_load_b64(rw, vo8, sb[s], SC1);
    __builtin_amdgcn_raw_buffer_store_b32(tag, rw, epo, 0, SC1);
    SB();
    unsigned bad;
    float mc, lc;
    asm volatile("v_xor_b32 %2, %4, %9\n\tv_bfi_b32 %1, %5, %3, %7\n\tv_and_b32 %0, %2, %6\n\tv_bfi_b32 %2, %5, %2, %8"
                 : "=&v"(bad), "=&v"(mc), "=&v"(lc) : "v"(gm[0]), "v"(gm[1]), "v"(km), "v"(kbm), "v"(mx), "v"(lx), "v"(tg));
    float wc = __builtin_amdgcn_exp2f(mc - row_max16(mc));
    wc *= __builtin_amdgcn_rcpf(row_sum16(wc * lc));
    const float wt = dpp<0x158>(wc);
    o0 = wt * vt0;
    o1 = wt * vt1;
#define ACC(S) { unsigned t_, x0_, x1_;                                                                                            \
    asm volatile("v_bitop3_b32 %3, %6, %7, %13 bitop3:0x7e\n\tv_bfi_b32 %4, %8, %6, %10\n\tv_bfi_b32 %5, %8, %7, %11\n\t"          \
                 "v_and_or_b32 %2, %3, %9, %2\n\tv_fmac_f32_dpp %0, %12, %4 row_newbcast:" #S " row_mask:0xf bank_mask:0xf\n\t"    \
                 "v_fmac_f32_dpp %1, %12, %5 row_newbcast:" #S " row_mask:0xf bank_mask:0xf"                                       \
                 : "+v"(o0), "+v"(o1), "+v"(bad), "=&v"(t_), "=&v"(x0_), "=&v"(x1_)                                                \
                 : "v"(gs[S][0]), "v"(gs[S][1]), "s"(ks[S]), "s"(kb[S]), "v"(oc0), "v"(oc1), "v"(wc), "v"(tg)); }
    ACC(0) ACC(1) ACC(2) ACC(3) ACC(4) ACC(5) ACC(6) ACC(7)
#undef ACC
    if (__all(bad == 0)) break;
    gm = __builtin_amdgcn_raw_buffer_load_b64(rw, mlo, mlb, SC1);
    asm volatile("" : "+v"(gm));
  }
  const float rcv = dpp<0xB1>((lane & 1) ? o0 : o1);
  const bf16x2 pk = {(__bf16)((lane & 1) ? rcv : o0), (__bf16)((lane & 1) ? o1 : rcv)};
  __builtin_amdgcn_raw_buffer_store_b32(__builtin_bit_cast(unsigned, pk), ro, op, 0, 0);
}

extern "C" void kernel_launch(void* const* d_in, const int* in_sizes, int n_in, void* d_out, int out_size, void* d_ws,
                              size_t ws_size, hipStream_t stream) {
  constexpr unsigned kv_bytes = H * C * D * 2, flags = 0x00020000u;
  tree_attn_decode<<<H * SPLITS, WAVES * 64, 0, stream>>>(d_in[1], kv_bytes, flags, d_in[2], kv_bytes, flags, d_in[0],
                                                          d_ws, d_in[3],
                                                          d_in[4], d_in[5],
                                                          d_out);
}
```

```cpp
#define PATS "gG01234wWabc"
#include <hip/hip_runtime.h>
#include <hip/hip_bf16.h>
#include <stdint.h>

#define PRE 16
#define SMPOS 16
#ifndef NV1
#define NV1 2
#endif
#define RT(x) do { __builtin_amdgcn_sched_barrier(0); asm volatile("s_memrealtime %0\n\ts_waitcnt lgkmcnt(0)" : "=s"(x) :: "memory"); __builtin_amdgcn_sched_barrier(0); } while (0)

namespace {

constexpr int H = 32, C = 4096, D = 128, SPLITS = 8, WAVES = 4;
constexpr int NT = C / SPLITS / WAVES / 4;
constexpr int pos(char c) { const char* p = PATS; int i = 0; while (p[i] != c) ++i; return 8 + i; }
constexpr int chunk(int j) { return ((j & 1) << pos('2')) + ((j >> 1 & 1) << pos('3')) + ((j >> 2 & 1) << pos('4')); }
constexpr int KV_AUX = 3;
constexpr int SC1 = 16;
constexpr float SCALE = 0.088388347648318440f, LOG2E = 1.4426950408889634f;
constexpr float C2 = SCALE * LOG2E;

constexpr unsigned HSTR = 8192, REC = 512, O_ML = 4096, O_EP = 5120, O_CNT = 5248, WS_BYTES = H * HSTR;
constexpr unsigned KEEP = 0xffffff00u;

using bf16x2 = __attribute__((ext_vector_type(2))) __bf16;
using bf16x8 = __attribute__((ext_vector_type(8))) __bf16;
using u32x4 = __attribute__((ext_vector_type(4))) unsigned;
using u32x2 = __attribute__((ext_vector_type(2))) unsigned;
using i32x8 = __attribute__((ext_vector_type(8))) int;
using u64x4 = __attribute__((ext_vector_type(4))) unsigned long long;
using u64x2 = __attribute__((ext_vector_type(2))) unsigned long long;
using i32x4 = __attribute__((ext_vector_type(4))) int;
using f32x4 = __attribute__((ext_vector_type(4))) float;
using f32x2 = __attribute__((ext_vector_type(2))) float;
typedef __attribute__((address_space(1))) unsigned long long gu64;
typedef __attribute__((address_space(1))) u32x4 gu32x4;

__device__ __forceinline__ float dot2(unsigned k, unsigned q, float acc) {
  return __builtin_amdgcn_fdot2_f32_bf16(__builtin_bit_cast(bf16x2, k), __builtin_bit_cast(bf16x2, q), acc, false);
}
__device__ __forceinline__ float dot8(const u32x4& k, const u32x4& q) {
  return dot2(k[3], q[3], dot2(k[2], q[2], dot2(k[1], q[1], dot2(k[0], q[0], 0.f))));
}
template <int CTRL> __device__ __forceinline__ float dpp(float x) {
  return __builtin_bit_cast(float, __builtin_amdgcn_mov_dpp(__builtin_bit_cast(int, x), CTRL, 0xf, 0xf, true));
}
__device__ __forceinline__ float row_sum16(float x) {
  x += dpp<0x128>(x); x += dpp<0x124>(x); x += dpp<0x122>(x); x += dpp<0x121>(x);
  return x;
}
__device__ __forceinline__ float row_max16(float x) {
  asm volatile("s_nop 1\n\tv_max_f32_dpp %0, %0, %0 row_ror:8 row_mask:0xf bank_mask:0xf\n\ts_nop 1\n\t"
               "v_max_f32_dpp %0, %0, %0 row_ror:4 row_mask:0xf bank_mask:0xf\n\ts_nop 1\n\t"
               "v_max_f32_dpp %0, %0, %0 row_ror:2 row_mask:0xf bank_mask:0xf\n\ts_nop 1\n\t"
               "v_max_f32_dpp %0, %0, %0 row_ror:1 row_mask:0xf bank_mask:0xf" : "+v"(x));
  return x;
}
__device__ __forceinline__ unsigned row_bcast(unsigned x, int i) {
  switch (i) {
#define CASE(I) case I: return __builtin_amdgcn_mov_dpp(x, 0x150 + I, 0xf, 0xf, true);
    CASE(0) CASE(1) CASE(2) CASE(3) CASE(4) CASE(5) CASE(6) CASE(7) CASE(8) CASE(9) CASE(10) CASE(11) CASE(12) CASE(13) CASE(14)
#undef CASE
  }
  return __builtin_amdgcn_mov_dpp(x, 0x15f, 0xf, 0xf, true);
}
__device__ __forceinline__ float transpose_sum16(float (&d)[16], unsigned c) {
#define A1(X, Y, CTRL, M) "v_add_f32_dpp " X ", " Y ", " Y " " CTRL " row_mask:0xf bank_mask:" M "\n\t"
  asm volatile("s_nop 2\n\t"
               A1("%0", "%0", "row_ror:8", "0x3") A1("%1", "%1", "row_ror:8", "0x3") A1("%2", "%2", "row_ror:8", "0x3") A1("%3", "%3", "row_ror:8", "0x3")
               A1("%4", "%4", "row_ror:8", "0x3") A1("%5", "%5", "row_ror:8", "0x3") A1("%6", "%6", "row_ror:8", "0x3") A1("%7", "%7", "row_ror:8", "0x3")
               A1("%0", "%8", "row_ror:8", "0xc") A1("%1", "%9", "row_ror:8", "0xc") A1("%2", "%10", "row_ror:8", "0xc") A1("%3", "%11", "row_ror:8", "0xc")
               A1("%4", "%12", "row_ror:8", "0xc") A1("%5", "%13", "row_ror:8", "0xc") A1("%6", "%14", "row_ror:8", "0xc") A1("%7", "%15", "row_ror:8", "0xc")
               A1("%0", "%0", "row_half_mirror", "0x5") A1("%1", "%1", "row_half_mirror", "0x5")
               A1("%2", "%2", "row_half_mirror", "0x5") A1("%3", "%3", "row_half_mirror", "0x5")
               A1("%0", "%4", "row_half_mirror", "0xa") A1("%1", "%5", "row_half_mirror", "0xa")
               A1("%2", "%6", "row_half_mirror", "0xa") A1("%3", "%7", "row_half_mirror", "0xa")
               "s_nop 1"
               : "+v"(d[0]), "+v"(d[1]), "+v"(d[2]), "+v"(d[3]), "+v"(d[4]), "+v"(d[5]), "+v"(d[6]), "+v"(d[7])
               : "v"(d[8]), "v"(d[9]), "v"(d[10]), "v"(d[11]), "v"(d[12]), "v"(d[13]), "v"(d[14]), "v"(d[15]));
#undef A1
  const float a0 = d[0] + dpp<0x4E>(d[0]), a1 = d[1] + dpp<0x4E>(d[1]), a2 = d[2] + dpp<0x4E>(d[2]), a3 = d[3] + dpp<0x4E>(d[3]);
  const float e0 = (c & 2) ? a2 : a0, e1 = (c & 2) ? a3 : a1;
  const float f0 = e0 + dpp<0xB1>(e0), f1 = e1 + dpp<0xB1>(e1);
  return (c & 1) ? f1 : f0;
}
__device__ __forceinline__ float xrow_max(float x) {
  auto s = __builtin_amdgcn_permlane16_swap(__float_as_uint(x), __float_as_uint(x), false, false);
  x = fmaxf(__uint_as_float(s[0]), __uint_as_float(s[1]));
  auto t = __builtin_amdgcn_permlane32_swap(__float_as_uint(x), __float_as_uint(x), false, false);
  return fmaxf(__uint_as_float(t[0]), __uint_as_float(t[1]));
}
__device__ __forceinline__ float xrow_sum(float x) {
  auto s = __builtin_amdgcn_permlane16_swap(__float_as_uint(x), __float_as_uint(x), false, false);
  x = __uint_as_float(s[0]) + __uint_as_float(s[1]);
  auto t = __builtin_amdgcn_permlane32_swap(__float_as_uint(x), __float_as_uint(x), false, false);
  return __uint_as_float(t[0]) + __uint_as_float(t[1]);
}
__device__ __forceinline__ float bf2f(unsigned short h) { return __uint_as_float((unsigned)h << 16); }

}

__device__ i32x4 buf_load16(i32x4 rsrc, int voffset, int soffset, int aux) __asm("llvm.amdgcn.raw.buffer.load.v4i32");
#define SB() __builtin_amdgcn_sched_barrier(0)

__global__ void __launch_bounds__(WAVES * 64) tree_attn_decode(const void* K_, unsigned kn, unsigned kf, const void* V_, unsigned vn, unsigned vf,
                                                               const void* q_, void* ws_, const void* kt_, const void* vt_,
                                                               const void* mask_, void* out_) {
  __shared__ __attribute__((aligned(16))) float lds[WAVES * 128 + 2 * WAVES];
  i32x8 ka;
  u64x4 kb_;
  u64x2 kc_;
  unsigned so, vo, head, tmp, tmp2;
  unsigned long long ta;
  asm volatile("s_memrealtime %0" : "=s"(ta));
  asm volatile("s_load_dwordx8 %0, %7, 0x0\n\t"
               "s_load_dwordx8 %1, %7, 0x20\n\t"
               "v_readfirstlane_b32 %2, %9\n\t"
               "s_and_b32 %4, %8, 7\n\t"
               "s_bfe_u32 %5, %8, 0x20003\n\t"
               "s_lshl2_add_u32 %4, %4, %5\n\t"
               "s_lshl_b32 %5, %4, 20\n\t"
               "s_bitcmp1_b32 %2, 6\n\t" "s_cselect_b32 %6, %10, 0\n\t" "s_or_b32 %5, %5, %6\n\t"
               "s_bitcmp1_b32 %2, 7\n\t" "s_cselect_b32 %6, %11, 0\n\t" "s_or_b32 %5, %5, %6\n\t"
               "s_bitcmp1_b32 %8, 5\n\t" "s_cselect_b32 %6, %12, 0\n\t" "s_or_b32 %5, %5, %6\n\t"
               "s_bitcmp1_b32 %8, 6\n\t" "s_cselect_b32 %6, %13, 0\n\t" "s_or_b32 %5, %5, %6\n\t"
               "s_bitcmp1_b32 %8, 7\n\t" "s_cselect_b32 %6, %14, 0\n\t" "s_or_b32 %2, %5, %6\n\t"
               "v_and_b32 %3, 63, %9\n\t"
               "v_lshlrev_b32 %3, 4, %3\n\t"
               "s_waitcnt lgkmcnt(0)"
               : "=&s"(ka), "=&s"(kb_), "=&s"(so), "=&v"(vo), "=&s"(head), "=&s"(tmp), "=&s"(tmp2)
               : "s"(__builtin_amdgcn_kernarg_segment_ptr()), "s"(blockIdx.x), "v"(threadIdx.x), "i"(1 << pos('w')), "i"(1 << pos('W')), "i"(1 << pos('a')), "i"(1 << pos('b')), "i"(1 << pos('c'))
               : "scc");
  const i32x4 rk = __builtin_shufflevector(ka, ka, 0, 1, 2, 3), rv = __builtin_shufflevector(ka, ka, 4, 5, 6, 7);
  u32x4 k[NT], v[NT];
#define LDX(r, t) __builtin_bit_cast(u32x4, buf_load16(r, vo + ((t) & 3) * 1024, so + chunk((t) >> 2), KV_AUX))
  k[0] = LDX(rk, 0); SB();
  v[0] = LDX(rv, 0); SB();
#pragma unroll
  for (int t = 1; t < 4; ++t) { k[t] = LDX(rk, t); SB(); }
#pragma unroll
  for (int t = 1; t <= NV1; ++t) { v[t] = LDX(rv, t); SB(); }
  asm volatile("s_load_dwordx4 %0, %1, 0x40" : "=s"(kc_) : "s"(__builtin_amdgcn_kernarg_segment_ptr()));
  SB();
  const unsigned split = blockIdx.x >> 5, lane = threadIdx.x & 63, wave = __builtin_amdgcn_readfirstlane(threadIdx.x >> 6);
  const unsigned c = lane & 15, g = lane >> 4, n = c & 7;
  const u32x4 qv = *(const gu32x4*)(kb_[0] + (head * 16 + c) * 16u);
#pragma unroll
  for (int t = 4; t < SMPOS; ++t) k[t] = LDX(rk, t);
  SB();
  asm volatile("s_waitcnt lgkmcnt(0)" : "+s"(kc_));
  unsigned char* const ws = (unsigned char*)kb_[1];
  const void *const kt = (const void*)kb_[2], *const vt = (const void*)kb_[3], *const mask = (const void*)kc_[0];
  void* const out = (void*)kc_[1];
  const unsigned d0 = 32 * g + 16 * (c >> 3) + n;
  const unsigned hb = head * HSTR, nsm = wave == 0 ? H * D * 2 + 16 : 0u;
  const auto rw = __builtin_amdgcn_make_buffer_rsrc((void*)ws, 0, wave == 0 ? WS_BYTES : 0u, 0x00020000);
  const unsigned epoch = __builtin_amdgcn_raw_buffer_load_b32(rw, 0, hb + O_EP, SC1);
  const u32x2 w0 = __builtin_amdgcn_raw_buffer_load_b64(rw, 0, hb + O_CNT, SC1);
  const u32x4 ktv = __builtin_amdgcn_raw_buffer_load_b128(__builtin_amdgcn_make_buffer_rsrc((void*)kt, 0, nsm, 0x00020000), c * 16, head * 256, 0);
  const auto rvt = __builtin_amdgcn_make_buffer_rsrc((void*)vt, 0, nsm, 0x00020000);
  const unsigned short vtb0 = __builtin_amdgcn_raw_buffer_load_b16(rvt, d0 * 2, head * 256, 0);
  const unsigned short vtb1 = __builtin_amdgcn_raw_buffer_load_b16(rvt, d0 * 2 + 16, head * 256, 0);
  const unsigned short mk = __builtin_amdgcn_raw_buffer_load_b16(__builtin_amdgcn_make_buffer_rsrc((void*)mask, 0, nsm ? 16 : 0, 0x00020000), 0, 0, 0);
  SB();
#pragma unroll
  for (int t = SMPOS; t < NT; ++t) k[t] = LDX(rk, t);
#pragma unroll
  for (int t = NV1 + 1; t < PRE; ++t) v[t] = LDX(rv, t);
  SB();

  float xs[2];
#pragma unroll
  for (int h = 0; h < 2; ++h) {
    float d[16];
#pragma unroll
    for (int t = 0; t < 16; ++t) d[t] = dot8(k[16 * h + t], qv);
    xs[h] = transpose_sum16(d, c) * C2;
  }
  const float m = xrow_max(row_max16(fmaxf(xs[0], xs[1])));
  unsigned pk[2];
  float l = 0.f;
#pragma unroll
  for (int h = 0; h < 2; ++h) {
    const float p = __builtin_amdgcn_exp2f(xs[h] - m);
    l += p;
    const unsigned hi = __float_as_uint(p) & 0xffff0000u;
    pk[h] = hi | (__float_as_uint(p - __uint_as_float(hi)) >> 16);
  }
  l = row_sum16(l);
  unsigned sel[4];
#pragma unroll
  for (int r = 0; r < 4; ++r) sel[r] = (n >> 1) == r ? ((c < 8 ? 0x0302u : 0x0100u) << (16 * (n & 1))) | (0x0c0cu << (16 * (~n & 1))) : 0x0c0c0c0cu;
  asm volatile("" : "+v"(pk[0]), "+v"(pk[1]));
  asm volatile("" : "+v"(sel[0]), "+v"(sel[1]), "+v"(sel[2]), "+v"(sel[3]));
  l = xrow_sum(l);
  float* lm = lds + WAVES * 128;
  if (lane == 0) *reinterpret_cast<f32x2*>(lm + 2 * wave) = f32x2{m, l};
  float vt0 = bf2f(vtb0), vt1 = bf2f(vtb1);
  const float st = (row_sum16(dot8(ktv, qv)) * SCALE + (1.0f - bf2f(mk)) * -1000000000.0f) * LOG2E;
  float mx = c == 8 ? st : -INFINITY;
  unsigned tag = epoch + 1u;
  unsigned long long e = ((unsigned long long)w0[1] << 32) | w0[0];
  asm volatile("" : "+v"(mx), "+v"(vt0), "+v"(vt1), "+v"(tag));
  if (threadIdx.x == 0 && w0[1] != tag)
    __hip_atomic_compare_exchange_strong((gu64*)(ws + hb + O_CNT), &e, (unsigned long long)tag << 32, __ATOMIC_RELAXED,
                                         __ATOMIC_RELAXED, __HIP_MEMORY_SCOPE_AGENT);
  SB();
  asm volatile("" : "+v"(vo));
#pragma unroll
  for (int t = PRE; t < NT; ++t) { v[t] = LDX(rv, t); SB(); }

  SB();
  __syncthreads();

  const f32x2 ml = *reinterpret_cast<const f32x2*>(lm + 2 * (c & (WAVES - 1)));
  const float MW = row_max16(ml[0]);
  const float L = __uint_as_float(__float_as_uint(row_sum16(__builtin_amdgcn_exp2f(ml[0] - MW) * ml[1]) * (WAVES / 16.0f)) & KEEP);
  float fw = __builtin_amdgcn_exp2f(m - MW);
  unsigned tg = tag & 0xffu, vo8 = lane * 8u;
  __builtin_amdgcn_raw_buffer_store_b64(u32x2{__float_as_uint(MW), __float_as_uint(L) | tg}, rw, lane < 16 ? vo8 : WS_BYTES,
                                        hb + O_ML + split * 128, SC1);
  float lx = c == split ? L : 1.0f;
  mx = c == split ? MW : mx;
  unsigned km = (c < 8 && c != split) ? 0xffffffffu : 0u, kbm = n != split ? 0xffu : 0u;
  unsigned mlo = n != split ? n * 128u + (lane >> 3) * 8u : WS_BYTES;
  unsigned ato = lane == 0 ? hb + O_CNT : WS_BYTES, epo = lane == 0 ? hb + O_EP : WS_BYTES;
  unsigned op = (head * D + d0 + ((lane & 1) ? 7 : 0)) * 2u;
  int old = 1;
  unsigned ks[SPLITS], kb[SPLITS], sb[SPLITS];
#pragma unroll
  for (int s = 0; s < SPLITS; ++s) {
    ks[s] = s == split ? 0u : KEEP; kb[s] = s == split ? 0u : 0xffu; sb[s] = s == split ? WS_BYTES : hb + s * REC;
    asm volatile("" : "+s"(ks[s]), "+s"(kb[s]), "+s"(sb[s]));
  }
  unsigned mine = hb + split * REC, mlb = hb + O_ML;
  const auto ro = __builtin_amdgcn_make_buffer_rsrc((void*)out, 0, H * D * 2, 0x00020000);
  asm volatile("" : "+v"(fw), "+v"(lx), "+v"(mx), "+v"(km), "+v"(kbm), "+v"(vo8), "+v"(mlo), "+v"(ato), "+v"(epo), "+v"(tg), "+v"(old), "+v"(op));
  asm volatile("" : "+s"(mine), "+s"(mlb) : "s"(ro));
  SB();

  f32x4 acc = {0.f, 0.f, 0.f, 0.f};
#pragma unroll
  for (int t = 0; t < NT; ++t) {
    u32x4 b;
    const unsigned bc = row_bcast(pk[t >> 4], t & 15);
#pragma unroll
    for (int r = 0; r < 4; ++r) b[r] = __builtin_amdgcn_perm(bc, bc, sel[r]);
    asm volatile("" : "+v"(b));
    SB();
    asm volatile("s_nop 1\n\tv_mfma_f32_16x16x32_bf16 %0, %1, %2, %0" : "+v"(acc) : "v"(v[t]), "v"(b));
    SB();
  }
  asm volatile("s_nop 7\n\ts_nop 0" : "+v"(acc));
#pragma unroll
  for (int r = 0; r < 4; ++r) acc[r] += dpp<0x128>(acc[r]);
  f32x2 o2 = {c < 8 ? acc[0] : acc[2], c < 8 ? acc[1] : acc[3]};
  o2 *= fw;
  *reinterpret_cast<f32x2*>(lds + wave * 128 + lane * 2) = o2;
  asm volatile("" :: "v"(e));
  __syncthreads();
  if (wave != 0) return;

  old = __builtin_amdgcn_raw_ptr_buffer_atomic_add_i32(old, rw, ato, 0, 0);
  u32x2 gm = __builtin_amdgcn_raw_buffer_load_b64(rw, mlo, mlb, SC1);
  SB();
  f32x2 ow[WAVES];
#pragma unroll
  for (int w = 0; w < WAVES; ++w) ow[w] = *reinterpret_cast<const f32x2*>(reinterpret_cast<const char*>(lds) + vo8 + w * 512);
#pragma unroll
  for (int s = 1; s < WAVES; s *= 2)
#pragma unroll
    for (int w = 0; w < WAVES; w += 2 * s) ow[w] += ow[w + s];
  const unsigned oc0 = __float_as_uint(ow[0][0]) & KEEP, oc1 = __float_as_uint(ow[0][1]) & KEEP;
  __builtin_amdgcn_raw_buffer_store_b64(u32x2{oc0 | tg, oc1 | tg}, rw, vo8, mine, SC1);
  SB();
  if (__builtin_amdgcn_readfirstlane(old) != SPLITS - 1) {   return; }

  float o0, o1;
#pragma nounroll
  for (unsigned pass = 0; pass < (1u << 20); ++pass) {
    asm volatile("" ::: "memory");
    u32x2 gs[SPLITS];
#pragma unroll
    for (int s = 0; s < SPLITS; ++s) gs[s] = __builtin_amdgcn_raw_buffer_load_b64(rw, vo8, sb[s], SC1);
    __builtin_amdgcn_raw_buffer_store_b32(tag, rw, epo, 0, SC1);
    SB();
    unsigned bad;
    float mc, lc;
    asm volatile("v_xor_b32 %2, %4, %9\n\tv_bfi_b32 %1, %5, %3, %7\n\tv_and_b32 %0, %2, %6\n\tv_bfi_b32 %2, %5, %2, %8"
                 : "=&v"(bad), "=&v"(mc), "=&v"(lc) : "v"(gm[0]), "v"(gm[1]), "v"(km), "v"(kbm), "v"(mx), "v"(lx), "v"(tg));
    float wc = __builtin_amdgcn_exp2f(mc - row_max16(mc));
    wc *= __builtin_amdgcn_rcpf(row_sum16(wc * lc));
    const float wt = dpp<0x158>(wc);
    o0 = wt * vt0;
    o1 = wt * vt1;
#define ACC(S) { unsigned t_, x0_, x1_;                                                                                            \
    asm volatile("v_bitop3_b32 %3, %6, %7, %13 bitop3:0x7e\n\tv_bfi_b32 %4, %8, %6, %10\n\tv_bfi_b32 %5, %8, %7, %11\n\t"          \
                 "v_and_or_b32 %2, %3, %9, %2\n\tv_fmac_f32_dpp %0, %12, %4 row_newbcast:" #S " row_mask:0xf bank_mask:0xf\n\t"    \
                 "v_fmac_f32_dpp %1, %12, %5 row_newbcast:" #S " row_mask:0xf bank_mask:0xf"                                       \
                 : "+v"(o0), "+v"(o1), "+v"(bad), "=&v"(t_), "=&v"(x0_), "=&v"(x1_)                                                \
                 : "v"(gs[S][0]), "v"(gs[S][1]), "s"(ks[S]), "s"(kb[S]), "v"(oc0), "v"(oc1), "v"(wc), "v"(tg)); }
    ACC(0) ACC(1) ACC(2) ACC(3) ACC(4) ACC(5) ACC(6) ACC(7)
#undef ACC
    if (__all(bad == 0)) break;
    gm = __builtin_amdgcn_raw_buffer_load_b64(rw, mlo, mlb, SC1);
    asm volatile("" : "+v"(gm));
  }
  const float rcv = dpp<0xB1>((lane & 1) ? o0 : o1);
  const bf16x2 res = {(__bf16)((lane & 1) ? rcv : o0), (__bf16)((lane & 1) ? o1 : rcv)};
  __builtin_amdgcn_raw_buffer_store_b32(__builtin_bit_cast(unsigned, res), ro, op, 0, 0);
  { unsigned long long te, now; RT(te); const unsigned long long tgt = te + (te - ta) * 4; do { RT(now); } while (now < tgt); }
}

extern "C" void kernel_launch(void* const* d_in, const int* in_sizes, int n_in, void* d_out, int out_size, void* d_ws,
                              size_t ws_size, hipStream_t stream) {
  constexpr unsigned kv_bytes = H * C * D * 2, flags = 0x00020000u;
  tree_attn_decode<<<H * SPLITS, WAVES * 64, 0, stream>>>(d_in[1], kv_bytes, flags, d_in[2], kv_bytes, flags, d_in[0],
                                                          d_ws, d_in[3],
                                                          d_in[4], d_in[5],
                                                          d_out);
}
```

```cpp
#define PRE 16
#include <hip/hip_runtime.h>
#include <hip/hip_bf16.h>
#include <stdint.h>

#define SMPOS 16
#ifndef NV1
#define NV1 2
#endif
#define RT(x) do { __builtin_amdgcn_sched_barrier(0); asm volatile("s_memrealtime %0\n\ts_waitcnt lgkmcnt(0)" : "=s"(x) :: "memory"); __builtin_amdgcn_sched_barrier(0); } while (0)

namespace {

constexpr int H = 32, C = 4096, D = 128, SPLITS = 8, WAVES = 4;
constexpr int NT = C / SPLITS / WAVES / 4;
constexpr int KV_AUX = 3;
constexpr int SC1 = 16;
constexpr float SCALE = 0.088388347648318440f, LOG2E = 1.4426950408889634f;
constexpr float C2 = SCALE * LOG2E;

constexpr unsigned HSTR = 8192, REC = 512, O_ML = 4096, O_EP = 5120, O_CNT = 5248, WS_BYTES = H * HSTR;
constexpr unsigned KEEP = 0xffffff00u;

using bf16x2 = __attribute__((ext_vector_type(2))) __bf16;
using bf16x8 = __attribute__((ext_vector_type(8))) __bf16;
using u32x4 = __attribute__((ext_vector_type(4))) unsigned;
using u32x2 = __attribute__((ext_vector_type(2))) unsigned;
using i32x8 = __attribute__((ext_vector_type(8))) int;
using u64x4 = __attribute__((ext_vector_type(4))) unsigned long long;
using u64x2 = __attribute__((ext_vector_type(2))) unsigned long long;
using i32x4 = __attribute__((ext_vector_type(4))) int;
using f32x4 = __attribute__((ext_vector_type(4))) float;
using f32x2 = __attribute__((ext_vector_type(2))) float;
typedef __attribute__((address_space(1))) unsigned long long gu64;
typedef __attribute__((address_space(1))) u32x4 gu32x4;

__device__ __forceinline__ float dot2(unsigned k, unsigned q, float acc) {
  return __builtin_amdgcn_fdot2_f32_bf16(__builtin_bit_cast(bf16x2, k), __builtin_bit_cast(bf16x2, q), acc, false);
}
__device__ __forceinline__ float dot8(const u32x4& k, const u32x4& q) {
  return dot2(k[3], q[3], dot2(k[2], q[2], dot2(k[1], q[1], dot2(k[0], q[0], 0.f))));
}
template <int CTRL> __device__ __forceinline__ float dpp(float x) {
  return __builtin_bit_cast(float, __builtin_amdgcn_mov_dpp(__builtin_bit_cast(int, x), CTRL, 0xf, 0xf, true));
}
__device__ __forceinline__ float row_sum16(float x) {
  x += dpp<0x128>(x); x += dpp<0x124>(x); x += dpp<0x122>(x); x += dpp<0x121>(x);
  return x;
}
__device__ __forceinline__ float row_max16(float x) {
  asm volatile("s_nop 1\n\tv_max_f32_dpp %0, %0, %0 row_ror:8 row_mask:0xf bank_mask:0xf\n\ts_nop 1\n\t"
               "v_max_f32_dpp %0, %0, %0 row_ror:4 row_mask:0xf bank_mask:0xf\n\ts_nop 1\n\t"
               "v_max_f32_dpp %0, %0, %0 row_ror:2 row_mask:0xf bank_mask:0xf\n\ts_nop 1\n\t"
               "v_max_f32_dpp %0, %0, %0 row_ror:1 row_mask:0xf bank_mask:0xf" : "+v"(x));
  return x;
}
__device__ __forceinline__ unsigned row_bcast(unsigned x, int i) {
  switch (i) {
#define CASE(I) case I: return __builtin_amdgcn_mov_dpp(x, 0x150 + I, 0xf, 0xf, true);
    CASE(0) CASE(1) CASE(2) CASE(3) CASE(4) CASE(5) CASE(6) CASE(7) CASE(8) CASE(9) CASE(10) CASE(11) CASE(12) CASE(13) CASE(14)
#undef CASE
  }
  return __builtin_amdgcn_mov_dpp(x, 0x15f, 0xf, 0xf, true);
}
__device__ __forceinline__ float transpose_sum16(float (&d)[16], unsigned c) {
#define A1(X, Y, CTRL, M) "v_add_f32_dpp " X ", " Y ", " Y " " CTRL " row_mask:0xf bank_mask:" M "\n\t"
  asm volatile("s_nop 2\n\t"
               A1("%0", "%0", "row_ror:8", "0x3") A1("%1", "%1", "row_ror:8", "0x3") A1("%2", "%2", "row_ror:8", "0x3") A1("%3", "%3", "row_ror:8", "0x3")
               A1("%4", "%4", "row_ror:8", "0x3") A1("%5", "%5", "row_ror:8", "0x3") A1("%6", "%6", "row_ror:8", "0x3") A1("%7", "%7", "row_ror:8", "0x3")
               A1("%0", "%8", "row_ror:8", "0xc") A1("%1", "%9", "row_ror:8", "0xc") A1("%2", "%10", "row_ror:8", "0xc") A1("%3", "%11", "row_ror:8", "0xc")
               A1("%4", "%12", "row_ror:8", "0xc") A1("%5", "%13", "row_ror:8", "0xc") A1("%6", "%14", "row_ror:8", "0xc") A1("%7", "%15", "row_ror:8", "0xc")
               A1("%0", "%0", "row_half_mirror", "0x5") A1("%1", "%1", "row_half_mirror", "0x5")
               A1("%2", "%2", "row_half_mirror", "0x5") A1("%3", "%3", "row_half_mirror", "0x5")
               A1("%0", "%4", "row_half_mirror", "0xa") A1("%1", "%5", "row_half_mirror", "0xa")
               A1("%2", "%6", "row_half_mirror", "0xa") A1("%3", "%7", "row_half_mirror", "0xa")
               "s_nop 1"
               : "+v"(d[0]), "+v"(d[1]), "+v"(d[2]), "+v"(d[3]), "+v"(d[4]), "+v"(d[5]), "+v"(d[6]), "+v"(d[7])
               : "v"(d[8]), "v"(d[9]), "v"(d[10]), "v"(d[11]), "v"(d[12]), "v"(d[13]), "v"(d[14]), "v"(d[15]));
#undef A1
  const float a0 = d[0] + dpp<0x4E>(d[0]), a1 = d[1] + dpp<0x4E>(d[1]), a2 = d[2] + dpp<0x4E>(d[2]), a3 = d[3] + dpp<0x4E>(d[3]);
  const float e0 = (c & 2) ? a2 : a0, e1 = (c & 2) ? a3 : a1;
  const float f0 = e0 + dpp<0xB1>(e0), f1 = e1 + dpp<0xB1>(e1);
  return (c & 1) ? f1 : f0;
}
__device__ __forceinline__ float xrow_max(float x) {
  auto s = __builtin_amdgcn_permlane16_swap(__float_as_uint(x), __float_as_uint(x), false, false);
  x = fmaxf(__uint_as_float(s[0]), __uint_as_float(s[1]));
  auto t = __builtin_amdgcn_permlane32_swap(__float_as_uint(x), __float_as_uint(x), false, false);
  return fmaxf(__uint_as_float(t[0]), __uint_as_float(t[1]));
}
__device__ __forceinline__ float xrow_sum(float x) {
  auto s = __builtin_amdgcn_permlane16_swap(__float_as_uint(x), __float_as_uint(x), false, false);
  x = __uint_as_float(s[0]) + __uint_as_float(s[1]);
  auto t = __builtin_amdgcn_permlane32_swap(__float_as_uint(x), __float_as_uint(x), false, false);
  return __uint_as_float(t[0]) + __uint_as_float(t[1]);
}
__device__ __forceinline__ float bf2f(unsigned short h) { return __uint_as_float((unsigned)h << 16); }

}

__device__ i32x4 buf_load16(i32x4 rsrc, int voffset, int soffset, int aux) __asm("llvm.amdgcn.raw.buffer.load.v4i32");
#define SB() __builtin_amdgcn_sched_barrier(0)

__global__ void __launch_bounds__(WAVES * 64) tree_attn_decode(const void* K_, unsigned kn, unsigned kf, const void* V_, unsigned vn, unsigned vf,
                                                               const void* q_, void* ws_, const void* kt_, const void* vt_,
                                                               const void* mask_, void* out_) {
  __shared__ __attribute__((aligned(16))) float lds[WAVES * 128 + 2 * WAVES];
  i32x8 ka;
  u64x4 kb_;
  u64x2 kc_;
  unsigned so, vo, head, tmp;
  unsigned long long ta;
  asm volatile("s_memrealtime %0" : "=s"(ta));
  asm volatile("s_load_dwordx8 %0, %6, 0x0\n\t"
               "s_load_dwordx8 %1, %6, 0x20\n\t"
               "v_readfirstlane_b32 %2, %8\n\t"
               "s_and_b32 %4, %7, 7\n\t"
               "s_bfe_u32 %5, %7, 0x20003\n\t"
               "s_lshl2_add_u32 %4, %4, %5\n\t"
               "s_lshr_b32 %5, %7, 5\n\t"
               "s_lshl3_add_u32 %5, %4, %5\n\t"
               "s_lshl_b32 %5, %5, 8\n\t"
               "s_add_u32 %2, %2, %5\n\t"
               "s_lshl_b32 %2, %2, 9\n\t"
               "v_and_b32 %3, 63, %8\n\t"
               "v_lshlrev_b32 %3, 4, %3\n\t"
               "s_waitcnt lgkmcnt(0)"
               : "=&s"(ka), "=&s"(kb_), "=&s"(so), "=&v"(vo), "=&s"(head), "=&s"(tmp)
               : "s"(__builtin_amdgcn_kernarg_segment_ptr()), "s"(blockIdx.x), "v"(threadIdx.x)
               : "scc");
  const i32x4 rk = __builtin_shufflevector(ka, ka, 0, 1, 2, 3), rv = __builtin_shufflevector(ka, ka, 4, 5, 6, 7);
  u32x4 k[NT], v[NT];
#define LDX(r, t) __builtin_bit_cast(u32x4, buf_load16(r, vo + ((t) & 3) * 1024, so + ((t) >> 2) * 4096, KV_AUX))
  k[0] = LDX(rk, 0); SB();
  v[0] = LDX(rv, 0); SB();
#pragma unroll
  for (int t = 1; t < 4; ++t) { k[t] = LDX(rk, t); SB(); }
#pragma unroll
  for (int t = 1; t <= NV1; ++t) { v[t] = LDX(rv, t); SB(); }
  asm volatile("s_load_dwordx4 %0, %1, 0x40" : "=s"(kc_) : "s"(__builtin_amdgcn_kernarg_segment_ptr()));
  SB();
  const unsigned split = blockIdx.x >> 5, lane = threadIdx.x & 63, wave = __builtin_amdgcn_readfirstlane(threadIdx.x >> 6);
  const unsigned c = lane & 15, g = lane >> 4, n = c & 7;
  const u32x4 qv = *(const gu32x4*)(kb_[0] + (head * 16 + c) * 16u);
#pragma unroll
  for (int t = 4; t < SMPOS; ++t) k[t] = LDX(rk, t);
  SB();
  asm volatile("s_waitcnt lgkmcnt(0)" : "+s"(kc_));
  unsigned char* const ws = (unsigned char*)kb_[1];
  const void *const kt = (const void*)kb_[2], *const vt = (const void*)kb_[3], *const mask = (const void*)kc_[0];
  void* const out = (void*)kc_[1];
  const unsigned d0 = 32 * g + 16 * (c >> 3) + n;
  const unsigned hb = head * HSTR, nsm = wave == 0 ? H * D * 2 + 16 : 0u;
  const auto rw = __builtin_amdgcn_make_buffer_rsrc((void*)ws, 0, wave == 0 ? WS_BYTES : 0u, 0x00020000);
  const unsigned epoch = __builtin_amdgcn_raw_buffer_load_b32(rw, 0, hb + O_EP, SC1);
  const u32x2 w0 = __builtin_amdgcn_raw_buffer_load_b64(rw, 0, hb + O_CNT, SC1);
  const u32x4 ktv = __builtin_amdgcn_raw_buffer_load_b128(__builtin_amdgcn_make_buffer_rsrc((void*)kt, 0, nsm, 0x00020000), c * 16, head * 256, 0);
  const auto rvt = __builtin_amdgcn_make_buffer_rsrc((void*)vt, 0, nsm, 0x00020000);
  const unsigned short vtb0 = __builtin_amdgcn_raw_buffer_load_b16(rvt, d0 * 2, head * 256, 0);
  const unsigned short vtb1 = __builtin_amdgcn_raw_buffer_load_b16(rvt, d0 * 2 + 16, head * 256, 0);
  const unsigned short mk = __builtin_amdgcn_raw_buffer_load_b16(__builtin_amdgcn_make_buffer_rsrc((void*)mask, 0, nsm ? 16 : 0, 0x00020000), 0, 0, 0);
  SB();
#pragma unroll
  for (int t = SMPOS; t < NT; ++t) k[t] = LDX(rk, t);
#pragma unroll
  for (int t = NV1 + 1; t < PRE; ++t) v[t] = LDX(rv, t);
  SB();

  float xs[2];
#pragma unroll
  for (int h = 0; h < 2; ++h) {
    float d[16];
#pragma unroll
    for (int t = 0; t < 16; ++t) d[t] = dot8(k[16 * h + t], qv);
    xs[h] = transpose_sum16(d, c) * C2;
  }
  const float m = xrow_max(row_max16(fmaxf(xs[0], xs[1])));
  unsigned pk[2];
  float l = 0.f;
#pragma unroll
  for (int h = 0; h < 2; ++h) {
    const float p = __builtin_amdgcn_exp2f(xs[h] - m);
    l += p;
    const unsigned hi = __float_as_uint(p) & 0xffff0000u;
    pk[h] = hi | (__float_as_uint(p - __uint_as_float(hi)) >> 16);
  }
  l = row_sum16(l);
  unsigned sel[4];
#pragma unroll
  for (int r = 0; r < 4; ++r) sel[r] = (n >> 1) == r ? ((c < 8 ? 0x0302u : 0x0100u) << (16 * (n & 1))) | (0x0c0cu << (16 * (~n & 1))) : 0x0c0c0c0cu;
  asm volatile("" : "+v"(pk[0]), "+v"(pk[1]));
  asm volatile("" : "+v"(sel[0]), "+v"(sel[1]), "+v"(sel[2]), "+v"(sel[3]));
  l = xrow_sum(l);
  float* lm = lds + WAVES * 128;
  if (lane == 0) *reinterpret_cast<f32x2*>(lm + 2 * wave) = f32x2{m, l};
  float vt0 = bf2f(vtb0), vt1 = bf2f(vtb1);
  const float st = (row_sum16(dot8(ktv, qv)) * SCALE + (1.0f - bf2f(mk)) * -1000000000.0f) * LOG2E;
  float mx = c == 8 ? st : -INFINITY;
  unsigned tag = epoch + 1u;
  unsigned long long e = ((unsigned long long)w0[1] << 32) | w0[0];
  asm volatile("" : "+v"(mx), "+v"(vt0), "+v"(vt1), "+v"(tag));
  if (threadIdx.x == 0 && w0[1] != tag)
    __hip_atomic_compare_exchange_strong((gu64*)(ws + hb + O_CNT), &e, (unsigned long long)tag << 32, __ATOMIC_RELAXED,
                                         __ATOMIC_RELAXED, __HIP_MEMORY_SCOPE_AGENT);
  SB();
  asm volatile("" : "+v"(vo));
#pragma unroll
  for (int t = PRE; t < NT; ++t) { v[t] = LDX(rv, t); SB(); }

  SB();
  __syncthreads();

  const f32x2 ml = *reinterpret_cast<const f32x2*>(lm + 2 * (c & (WAVES - 1)));
  const float MW = row_max16(ml[0]);
  const float L = __uint_as_float(__float_as_uint(row_sum16(__builtin_amdgcn_exp2f(ml[0] - MW) * ml[1]) * (WAVES / 16.0f)) & KEEP);
  float fw = __builtin_amdgcn_exp2f(m - MW);
  unsigned tg = tag & 0xffu, vo8 = lane * 8u;
  __builtin_amdgcn_raw_buffer_store_b64(u32x2{__float_as_uint(MW), __float_as_uint(L) | tg}, rw, lane < 16 ? vo8 : WS_BYTES,
                                        hb + O_ML + split * 128, SC1);
  float lx = c == split ? L : 1.0f;
  mx = c == split ? MW : mx;
  unsigned km = (c < 8 && c != split) ? 0xffffffffu : 0u, kbm = n != split ? 0xffu : 0u;
  unsigned mlo = n != split ? n * 128u + (lane >> 3) * 8u : WS_BYTES;
  unsigned ato = lane == 0 ? hb + O_CNT : WS_BYTES, epo = lane == 0 ? hb + O_EP : WS_BYTES;
  unsigned op = (head * D + d0 + ((lane & 1) ? 7 : 0)) * 2u;
  int old = 1;
  unsigned ks[SPLITS], kb[SPLITS], sb[SPLITS];
#pragma unroll
  for (int s = 0; s < SPLITS; ++s) {
    ks[s] = s == split ? 0u : KEEP; kb[s] = s == split ? 0u : 0xffu; sb[s] = s == split ? WS_BYTES : hb + s * REC;
    asm volatile("" : "+s"(ks[s]), "+s"(kb[s]), "+s"(sb[s]));
  }
  unsigned mine = hb + split * REC, mlb = hb + O_ML;
  const auto ro = __builtin_amdgcn_make_buffer_rsrc((void*)out, 0, H * D * 2, 0x00020000);
  asm volatile("" : "+v"(fw), "+v"(lx), "+v"(mx), "+v"(km), "+v"(kbm), "+v"(vo8), "+v"(mlo), "+v"(ato), "+v"(epo), "+v"(tg), "+v"(old), "+v"(op));
  asm volatile("" : "+s"(mine), "+s"(mlb) : "s"(ro));
  SB();

  f32x4 acc = {0.f, 0.f, 0.f, 0.f};
#pragma unroll
  for (int t = 0; t < NT; ++t) {
    u32x4 b;
    const unsigned bc = row_bcast(pk[t >> 4], t & 15);
#pragma unroll
    for (int r = 0; r < 4; ++r) b[r] = __builtin_amdgcn_perm(bc, bc, sel[r]);
    asm volatile("" : "+v"(b));
    SB();
    asm volatile("s_nop 1\n\tv_mfma_f32_16x16x32_bf16 %0, %1, %2, %0" : "+v"(acc) : "v"(v[t]), "v"(b));
    SB();
  }
  asm volatile("s_nop 7\n\ts_nop 0" : "+v"(acc));
#pragma unroll
  for (int r = 0; r < 4; ++r) acc[r] += dpp<0x128>(acc[r]);
  f32x2 o2 = {c < 8 ? acc[0] : acc[2], c < 8 ? acc[1] : acc[3]};
  o2 *= fw;
  *reinterpret_cast<f32x2*>(lds + wave * 128 + lane * 2) = o2;
  asm volatile("" :: "v"(e));
  __syncthreads();
  if (wave != 0) return;

  old = __builtin_amdgcn_raw_ptr_buffer_atomic_add_i32(old, rw, ato, 0, 0);
  u32x2 gm = __builtin_amdgcn_raw_buffer_load_b64(rw, mlo, mlb, SC1);
  SB();
  f32x2 ow[WAVES];
#pragma unroll
  for (int w = 0; w < WAVES; ++w) ow[w] = *reinterpret_cast<const f32x2*>(reinterpret_cast<const char*>(lds) + vo8 + w * 512);
#pragma unroll
  for (int s = 1; s < WAVES; s *= 2)
#pragma unroll
    for (int w = 0; w < WAVES; w += 2 * s) ow[w] += ow[w + s];
  const unsigned oc0 = __float_as_uint(ow[0][0]) & KEEP, oc1 = __float_as_uint(ow[0][1]) & KEEP;
  __builtin_amdgcn_raw_buffer_store_b64(u32x2{oc0 | tg, oc1 | tg}, rw, vo8, mine, SC1);
  SB();
  if (__builtin_amdgcn_readfirstlane(old) != SPLITS - 1) {   return; }

  float o0, o1;
#pragma nounroll
  for (unsigned pass = 0; pass < (1u << 20); ++pass) {
    asm volatile("" ::: "memory");
    u32x2 gs[SPLITS];
#pragma unroll
    for (int s = 0; s < SPLITS; ++s) gs[s] = __builtin_amdgcn_raw_buffer_load_b64(rw, vo8, sb[s], SC1);
    __builtin_amdgcn_raw_buffer_store_b32(tag, rw, epo, 0, SC1);
    SB();
    unsigned bad;
    float mc, lc;
    asm volatile("v_xor_b32 %2, %4, %9\n\tv_bfi_b32 %1, %5, %3, %7\n\tv_and_b32 %0, %2, %6\n\tv_bfi_b32 %2, %5, %2, %8"
                 : "=&v"(bad), "=&v"(mc), "=&v"(lc) : "v"(gm[0]), "v"(gm[1]), "v"(km), "v"(kbm), "v"(mx), "v"(lx), "v"(tg));
    float wc = __builtin_amdgcn_exp2f(mc - row_max16(mc));
    wc *= __builtin_amdgcn_rcpf(row_sum16(wc * lc));
    const float wt = dpp<0x158>(wc);
    o0 = wt * vt0;
    o1 = wt * vt1;
#define ACC(S) { unsigned t_, x0_, x1_;                                                                                            \
    asm volatile("v_bitop3_b32 %3, %6, %7, %13 bitop3:0x7e\n\tv_bfi_b32 %4, %8, %6, %10\n\tv_bfi_b32 %5, %8, %7, %11\n\t"          \
                 "v_and_or_b32 %2, %3, %9, %2\n\tv_fmac_f32_dpp %0, %12, %4 row_newbcast:" #S " row_mask:0xf bank_mask:0xf\n\t"    \
                 "v_fmac_f32_dpp %1, %12, %5 row_newbcast:" #S " row_mask:0xf bank_mask:0xf"                                       \
                 : "+v"(o0), "+v"(o1), "+v"(bad), "=&v"(t_), "=&v"(x0_), "=&v"(x1_)                                                \
                 : "v"(gs[S][0]), "v"(gs[S][1]), "s"(ks[S]), "s"(kb[S]), "v"(oc0), "v"(oc1), "v"(wc), "v"(tg)); }
    ACC(0) ACC(1) ACC(2) ACC(3) ACC(4) ACC(5) ACC(6) ACC(7)
#undef ACC
    if (__all(bad == 0)) break;
    gm = __builtin_amdgcn_raw_buffer_load_b64(rw, mlo, mlb, SC1);
    asm volatile("" : "+v"(gm));
  }
  const float rcv = dpp<0xB1>((lane & 1) ? o0 : o1);
  const bf16x2 res = {(__bf16)((lane & 1) ? rcv : o0), (__bf16)((lane & 1) ? o1 : rcv)};
  __builtin_amdgcn_raw_buffer_store_b32(__builtin_bit_cast(unsigned, res), ro, op, 0, 0);
  { unsigned long long te, now; RT(te); const unsigned long long tgt = te + (te - ta) * 4; do { RT(now); } while (now < tgt); }
}

extern "C" void kernel_launch(void* const* d_in, const int* in_sizes, int n_in, void* d_out, int out_size, void* d_ws,
                              size_t ws_size, hipStream_t stream) {
  constexpr unsigned kv_bytes = H * C * D * 2, flags = 0x00020000u;
  tree_attn_decode<<<H * SPLITS, WAVES * 64, 0, stream>>>(d_in[1], kv_bytes, flags, d_in[2], kv_bytes, flags, d_in[0],
                                                          d_ws, d_in[3],
                                                          d_in[4], d_in[5],
                                                          d_out);
}
```

```cpp
#include <hip/hip_runtime.h>
#include <hip/hip_bf16.h>
#include <stdint.h>

namespace {

constexpr int H = 32, C = 4096, D = 128, SPLITS = 8, WAVES = 4;
constexpr int NT = C / SPLITS / WAVES / 4;
constexpr int PRE = 16;
constexpr int KV_AUX = 3;
constexpr int SC1 = 16;
constexpr float SCALE = 0.088388347648318440f, LOG2E = 1.4426950408889634f;
constexpr float C2 = SCALE * LOG2E;

constexpr unsigned HSTR = 8192, REC = 512, O_ML = 4096, O_EP = 5120, O_CNT = 5248, WS_BYTES = H * HSTR;
constexpr unsigned KEEP = 0xffffff00u;

using bf16x2 = __attribute__((ext_vector_type(2))) __bf16;
using bf16x8 = __attribute__((ext_vector_type(8))) __bf16;
using u32x4 = __attribute__((ext_vector_type(4))) unsigned;
using u32x2 = __attribute__((ext_vector_type(2))) unsigned;
using i32x8 = __attribute__((ext_vector_type(8))) int;
using u64x4 = __attribute__((ext_vector_type(4))) unsigned long long;
using u64x2 = __attribute__((ext_vector_type(2))) unsigned long long;
using i32x4 = __attribute__((ext_vector_type(4))) int;
using f32x4 = __attribute__((ext_vector_type(4))) float;
using f32x2 = __attribute__((ext_vector_type(2))) float;
typedef __attribute__((address_space(1))) unsigned long long gu64;
typedef __attribute__((address_space(1))) u32x4 gu32x4;

__device__ __forceinline__ float dot2(unsigned k, unsigned q, float acc) {
  return __builtin_amdgcn_fdot2_f32_bf16(__builtin_bit_cast(bf16x2, k), __builtin_bit_cast(bf16x2, q), acc, false);
}
__device__ __forceinline__ float dot8(const u32x4& k, const u32x4& q) {
  return dot2(k[3], q[3], dot2(k[2], q[2], dot2(k[1], q[1], dot2(k[0], q[0], 0.f))));
}
template <int CTRL> __device__ __forceinline__ float dpp(float x) {
  return __builtin_bit_cast(float, __builtin_amdgcn_mov_dpp(__builtin_bit_cast(int, x), CTRL, 0xf, 0xf, true));
}
__device__ __forceinline__ float row_sum16(float x) {
  x += dpp<0x128>(x); x += dpp<0x124>(x); x += dpp<0x122>(x); x += dpp<0x121>(x);
  return x;
}
__device__ __forceinline__ float row_max16(float x) {
  asm volatile("s_nop 1\n\tv_max_f32_dpp %0, %0, %0 row_ror:8 row_mask:0xf bank_mask:0xf\n\ts_nop 1\n\t"
               "v_max_f32_dpp %0, %0, %0 row_ror:4 row_mask:0xf bank_mask:0xf\n\ts_nop 1\n\t"
               "v_max_f32_dpp %0, %0, %0 row_ror:2 row_mask:0xf bank_mask:0xf\n\ts_nop 1\n\t"
               "v_max_f32_dpp %0, %0, %0 row_ror:1 row_mask:0xf bank_mask:0xf" : "+v"(x));
  return x;
}
__device__ __forceinline__ unsigned row_bcast(unsigned x, int i) {
  switch (i) {
#define CASE(I) case I: return __builtin_amdgcn_mov_dpp(x, 0x150 + I, 0xf, 0xf, true);
    CASE(0) CASE(1) CASE(2) CASE(3) CASE(4) CASE(5) CASE(6) CASE(7) CASE(8) CASE(9) CASE(10) CASE(11) CASE(12) CASE(13) CASE(14)
#undef CASE
  }
  return __builtin_amdgcn_mov_dpp(x, 0x15f, 0xf, 0xf, true);
}
__device__ __forceinline__ float transpose_sum16(float (&d)[16], unsigned c) {
#define A1(X, Y, CTRL, M) "v_add_f32_dpp " X ", " Y ", " Y " " CTRL " row_mask:0xf bank_mask:" M "\n\t"
  asm volatile("s_nop 2\n\t"
               A1("%0", "%0", "row_ror:8", "0x3") A1("%1", "%1", "row_ror:8", "0x3") A1("%2", "%2", "row_ror:8", "0x3") A1("%3", "%3", "row_ror:8", "0x3")
               A1("%4", "%4", "row_ror:8", "0x3") A1("%5", "%5", "row_ror:8", "0x3") A1("%6", "%6", "row_ror:8", "0x3") A1("%7", "%7", "row_ror:8", "0x3")
               A1("%0", "%8", "row_ror:8", "0xc") A1("%1", "%9", "row_ror:8", "0xc") A1("%2", "%10", "row_ror:8", "0xc") A1("%3", "%11", "row_ror:8", "0xc")
               A1("%4", "%12", "row_ror:8", "0xc") A1("%5", "%13", "row_ror:8", "0xc") A1("%6", "%14", "row_ror:8", "0xc") A1("%7", "%15", "row_ror:8", "0xc")
               A1("%0", "%0", "row_half_mirror", "0x5") A1("%1", "%1", "row_half_mirror", "0x5")
               A1("%2", "%2", "row_half_mirror", "0x5") A1("%3", "%3", "row_half_mirror", "0x5")
               A1("%0", "%4", "row_half_mirror", "0xa") A1("%1", "%5", "row_half_mirror", "0xa")
               A1("%2", "%6", "row_half_mirror", "0xa") A1("%3", "%7", "row_half_mirror", "0xa")
               "s_nop 1"
               : "+v"(d[0]), "+v"(d[1]), "+v"(d[2]), "+v"(d[3]), "+v"(d[4]), "+v"(d[5]), "+v"(d[6]), "+v"(d[7])
               : "v"(d[8]), "v"(d[9]), "v"(d[10]), "v"(d[11]), "v"(d[12]), "v"(d[13]), "v"(d[14]), "v"(d[15]));
#undef A1
  const float a0 = d[0] + dpp<0x4E>(d[0]), a1 = d[1] + dpp<0x4E>(d[1]), a2 = d[2] + dpp<0x4E>(d[2]), a3 = d[3] + dpp<0x4E>(d[3]);
  const float e0 = (c & 2) ? a2 : a0, e1 = (c & 2) ? a3 : a1;
  const float f0 = e0 + dpp<0xB1>(e0), f1 = e1 + dpp<0xB1>(e1);
  return (c & 1) ? f1 : f0;
}
__device__ __forceinline__ float xrow_max(float x) {
  auto s = __builtin_amdgcn_permlane16_swap(__float_as_uint(x), __float_as_uint(x), false, false);
  x = fmaxf(__uint_as_float(s[0]), __uint_as_float(s[1]));
  auto t = __builtin_amdgcn_permlane32_swap(__float_as_uint(x), __float_as_uint(x), false, false);
  return fmaxf(__uint_as_float(t[0]), __uint_as_float(t[1]));
}
__device__ __forceinline__ float xrow_sum(float x) {
  auto s = __builtin_amdgcn_permlane16_swap(__float_as_uint(x), __float_as_uint(x), false, false);
  x = __uint_as_float(s[0]) + __uint_as_float(s[1]);
  auto t = __builtin_amdgcn_permlane32_swap(__float_as_uint(x), __float_as_uint(x), false, false);
  return __uint_as_float(t[0]) + __uint_as_float(t[1]);
}
__device__ __forceinline__ float bf2f(unsigned short h) { return __uint_as_float((unsigned)h << 16); }

}

__device__ i32x4 buf_load16(i32x4 rsrc, int voffset, int soffset, int aux) __asm("llvm.amdgcn.raw.buffer.load.v4i32");
#define SB() __builtin_amdgcn_sched_barrier(0)

__global__ void __launch_bounds__(WAVES * 64) tree_attn_decode(const void* K_, unsigned kn, unsigned kf, const void* V_, unsigned vn, unsigned vf,
                                                               const void* q_, void* ws_, const void* kt_, const void* vt_,
                                                               const void* mask_, void* out_) {
  __shared__ __attribute__((aligned(16))) float lds[WAVES * 128 + 2 * WAVES];
  i32x8 ka;
  u64x4 kb_;
  u64x2 kc_;
  unsigned so, vo, head, tmp;
  asm volatile("s_load_dwordx8 %0, %6, 0x0\n\t"
               "s_load_dwordx8 %1, %6, 0x20\n\t"
               "v_readfirstlane_b32 %2, %8\n\t"
               "s_and_b32 %4, %7, 7\n\t"
               "s_bfe_u32 %5, %7, 0x20003\n\t"
               "s_lshl2_add_u32 %4, %4, %5\n\t"
               "s_lshr_b32 %5, %7, 5\n\t"
               "s_lshl3_add_u32 %5, %4, %5\n\t"
               "s_lshl_b32 %5, %5, 8\n\t"
               "s_add_u32 %2, %2, %5\n\t"
               "s_lshl_b32 %2, %2, 9\n\t"
               "v_and_b32 %3, 63, %8\n\t"
               "v_lshlrev_b32 %3, 4, %3\n\t"
               "s_waitcnt lgkmcnt(0)"
               : "=&s"(ka), "=&s"(kb_), "=&s"(so), "=&v"(vo), "=&s"(head), "=&s"(tmp)
               : "s"(__builtin_amdgcn_kernarg_segment_ptr()), "s"(blockIdx.x), "v"(threadIdx.x)
               : "scc");
  const i32x4 rk = __builtin_shufflevector(ka, ka, 0, 1, 2, 3), rv = __builtin_shufflevector(ka, ka, 4, 5, 6, 7);
  u32x4 k[NT], v[NT];
#define LDX(r, t) __builtin_bit_cast(u32x4, buf_load16(r, vo + ((t) & 3) * 1024, so + ((t) >> 2) * 4096, KV_AUX))
  k[0] = LDX(rk, 0); SB();
  v[0] = LDX(rv, 0); SB();
#pragma unroll
  for (int t = 1; t < 4; ++t) { k[t] = LDX(rk, t); SB(); }
#pragma unroll
  for (int t = 1; t <= 2; ++t) { v[t] = LDX(rv, t); SB(); }
  asm volatile("s_load_dwordx4 %0, %1, 0x40" : "=s"(kc_) : "s"(__builtin_amdgcn_kernarg_segment_ptr()));
  SB();
  const unsigned split = blockIdx.x >> 5, lane = threadIdx.x & 63, wave = __builtin_amdgcn_readfirstlane(threadIdx.x >> 6);
  const unsigned c = lane & 15, g = lane >> 4, n = c & 7;
  const u32x4 qv = *(const gu32x4*)(kb_[0] + (head * 16 + c) * 16u);
#pragma unroll
  for (int t = 4; t < 16; ++t) k[t] = LDX(rk, t);
  SB();
  asm volatile("s_waitcnt lgkmcnt(0)" : "+s"(kc_));
  unsigned char* const ws = (unsigned char*)kb_[1];
  const void *const kt = (const void*)kb_[2], *const vt = (const void*)kb_[3], *const mask = (const void*)kc_[0];
  void* const out = (void*)kc_[1];
  const unsigned d0 = 32 * g + 16 * (c >> 3) + n;
  const unsigned hb = head * HSTR, nsm = wave == 0 ? H * D * 2 + 16 : 0u;
  const auto rw = __builtin_amdgcn_make_buffer_rsrc((void*)ws, 0, wave == 0 ? WS_BYTES : 0u, 0x00020000);
  const unsigned epoch = __builtin_amdgcn_raw_buffer_load_b32(rw, 0, hb + O_EP, SC1);
  const u32x2 w0 = __builtin_amdgcn_raw_buffer_load_b64(rw, 0, hb + O_CNT, SC1);
  const u32x4 ktv = __builtin_amdgcn_raw_buffer_load_b128(__builtin_amdgcn_make_buffer_rsrc((void*)kt, 0, nsm, 0x00020000), c * 16, head * 256, 0);
  const auto rvt = __builtin_amdgcn_make_buffer_rsrc((void*)vt, 0, nsm, 0x00020000);
  const unsigned short vtb0 = __builtin_amdgcn_raw_buffer_load_b16(rvt, d0 * 2, head * 256, 0);
  const unsigned short vtb1 = __builtin_amdgcn_raw_buffer_load_b16(rvt, d0 * 2 + 16, head * 256, 0);
  const unsigned short mk = __builtin_amdgcn_raw_buffer_load_b16(__builtin_amdgcn_make_buffer_rsrc((void*)mask, 0, nsm ? 16 : 0, 0x00020000), 0, 0, 0);
  SB();
#pragma unroll
  for (int t = 16; t < NT; ++t) k[t] = LDX(rk, t);
#pragma unroll
  for (int t = 2 + 1; t < PRE; ++t) v[t] = LDX(rv, t);
  SB();

  float xs[2];
#pragma unroll
  for (int h = 0; h < 2; ++h) {
    float d[16];
#pragma unroll
    for (int t = 0; t < 16; ++t) d[t] = dot8(k[16 * h + t], qv);
    xs[h] = transpose_sum16(d, c) * C2;
  }
  const float m = xrow_max(row_max16(fmaxf(xs[0], xs[1])));
  unsigned pk[2];
  float l = 0.f;
#pragma unroll
  for (int h = 0; h < 2; ++h) {
    const float p = __builtin_amdgcn_exp2f(xs[h] - m);
    l += p;
    const unsigned hi = __float_as_uint(p) & 0xffff0000u;
    pk[h] = hi | (__float_as_uint(p - __uint_as_float(hi)) >> 16);
  }
  l = row_sum16(l);
  unsigned sel[4];
#pragma unroll
  for (int r = 0; r < 4; ++r) sel[r] = (n >> 1) == r ? ((c < 8 ? 0x0302u : 0x0100u) << (16 * (n & 1))) | (0x0c0cu << (16 * (~n & 1))) : 0x0c0c0c0cu;
  asm volatile("" : "+v"(pk[0]), "+v"(pk[1]));
  asm volatile("" : "+v"(sel[0]), "+v"(sel[1]), "+v"(sel[2]), "+v"(sel[3]));
  l = xrow_sum(l);
  float* lm = lds + WAVES * 128;
  if (lane == 0) *reinterpret_cast<f32x2*>(lm + 2 * wave) = f32x2{m, l};
  float vt0 = bf2f(vtb0), vt1 = bf2f(vtb1);
  const float st = (row_sum16(dot8(ktv, qv)) * SCALE + (1.0f - bf2f(mk)) * -1000000000.0f) * LOG2E;
  float mx = c == 8 ? st : -INFINITY;
  unsigned tag = epoch + 1u;
  unsigned long long e = ((unsigned long long)w0[1] << 32) | w0[0];
  asm volatile("" : "+v"(mx), "+v"(vt0), "+v"(vt1), "+v"(tag));
  if (threadIdx.x == 0 && w0[1] != tag)
    __hip_atomic_compare_exchange_strong((gu64*)(ws + hb + O_CNT), &e, (unsigned long long)tag << 32, __ATOMIC_RELAXED,
                                         __ATOMIC_RELAXED, __HIP_MEMORY_SCOPE_AGENT);
  SB();
  asm volatile("" : "+v"(vo));
#pragma unroll
  for (int t = PRE; t < NT; ++t) { v[t] = LDX(rv, t); SB(); }

  SB();
  __syncthreads();

  const f32x2 ml = *reinterpret_cast<const f32x2*>(lm + 2 * (c & (WAVES - 1)));
  const float MW = row_max16(ml[0]);
  const float L = __uint_as_float(__float_as_uint(row_sum16(__builtin_amdgcn_exp2f(ml[0] - MW) * ml[1]) * (WAVES / 16.0f)) & KEEP);
  float fw = __builtin_amdgcn_exp2f(m - MW);
  unsigned tg = tag & 0xffu, vo8 = lane * 8u;
  __builtin_amdgcn_raw_buffer_store_b64(u32x2{__float_as_uint(MW), __float_as_uint(L) | tg}, rw, lane < 16 ? vo8 : WS_BYTES,
                                        hb + O_ML + split * 128, SC1);
  float lx = c == split ? L : 1.0f;
  mx = c == split ? MW : mx;
  unsigned km = (c < 8 && c != split) ? 0xffffffffu : 0u, kbm = n != split ? 0xffu : 0u;
  unsigned mlo = n != split ? n * 128u + (lane >> 3) * 8u : WS_BYTES;
  unsigned ato = lane == 0 ? hb + O_CNT : WS_BYTES, epo = lane == 0 ? hb + O_EP : WS_BYTES;
  unsigned op = (head * D + d0 + ((lane & 1) ? 7 : 0)) * 2u;
  int old = 1;
  unsigned ks[SPLITS], kb[SPLITS], sb[SPLITS];
#pragma unroll
  for (int s = 0; s < SPLITS; ++s) {
    ks[s] = s == split ? 0u : KEEP; kb[s] = s == split ? 0u : 0xffu; sb[s] = s == split ? WS_BYTES : hb + s * REC;
    asm volatile("" : "+s"(ks[s]), "+s"(kb[s]), "+s"(sb[s]));
  }
  unsigned mine = hb + split * REC, mlb = hb + O_ML;
  const auto ro = __builtin_amdgcn_make_buffer_rsrc((void*)out, 0, H * D * 2, 0x00020000);
  asm volatile("" : "+v"(fw), "+v"(lx), "+v"(mx), "+v"(km), "+v"(kbm), "+v"(vo8), "+v"(mlo), "+v"(ato), "+v"(epo), "+v"(tg), "+v"(old), "+v"(op));
  asm volatile("" : "+s"(mine), "+s"(mlb) : "s"(ro));
  SB();

  f32x4 acc = {0.f, 0.f, 0.f, 0.f};
#pragma unroll
  for (int t = 0; t < NT; ++t) {
    u32x4 b;
    const unsigned bc = row_bcast(pk[t >> 4], t & 15);
#pragma unroll
    for (int r = 0; r < 4; ++r) b[r] = __builtin_amdgcn_perm(bc, bc, sel[r]);
    asm volatile("" : "+v"(b));
    SB();
    asm volatile("s_nop 1\n\tv_mfma_f32_16x16x32_bf16 %0, %1, %2, %0" : "+v"(acc) : "v"(v[t]), "v"(b));
    SB();
  }
  asm volatile("s_nop 7\n\ts_nop 0" : "+v"(acc));
#pragma unroll
  for (int r = 0; r < 4; ++r) acc[r] += dpp<0x128>(acc[r]);
  f32x2 o2 = {c < 8 ? acc[0] : acc[2], c < 8 ? acc[1] : acc[3]};
  o2 *= fw;
  *reinterpret_cast<f32x2*>(lds + wave * 128 + lane * 2) = o2;
  asm volatile("" :: "v"(e));
  __syncthreads();
  if (wave != 0) return;

  old = __builtin_amdgcn_raw_ptr_buffer_atomic_add_i32(old, rw, ato, 0, 0);
  u32x2 gm = __builtin_amdgcn_raw_buffer_load_b64(rw, mlo, mlb, SC1);
  SB();
  f32x2 ow[WAVES];
#pragma unroll
  for (int w = 0; w < WAVES; ++w) ow[w] = *reinterpret_cast<const f32x2*>(reinterpret_cast<const char*>(lds) + vo8 + w * 512);
#pragma unroll
  for (int s = 1; s < WAVES; s *= 2)
#pragma unroll
    for (int w = 0; w < WAVES; w += 2 * s) ow[w] += ow[w + s];
  const unsigned oc0 = __float_as_uint(ow[0][0]) & KEEP, oc1 = __float_as_uint(ow[0][1]) & KEEP;
  __builtin_amdgcn_raw_buffer_store_b64(u32x2{oc0 | tg, oc1 | tg}, rw, vo8, mine, SC1);
  SB();
  if (__builtin_amdgcn_readfirstlane(old) != SPLITS - 1) return;

  float o0, o1;
#pragma nounroll
  for (unsigned pass = 0; pass < (1u << 20); ++pass) {
    asm volatile("" ::: "memory");
    u32x2 gs[SPLITS];
#pragma unroll
    for (int s = 0; s < SPLITS; ++s) gs[s] = __builtin_amdgcn_raw_buffer_load_b64(rw, vo8, sb[s], SC1);
    __builtin_amdgcn_raw_buffer_store_b32(tag, rw, epo, 0, SC1);
    SB();
    unsigned bad;
    float mc, lc;
    asm volatile("v_xor_b32 %2, %4, %9\n\tv_bfi_b32 %1, %5, %3, %7\n\tv_and_b32 %0, %2, %6\n\tv_bfi_b32 %2, %5, %2, %8"
                 : "=&v"(bad), "=&v"(mc), "=&v"(lc) : "v"(gm[0]), "v"(gm[1]), "v"(km), "v"(kbm), "v"(mx), "v"(lx), "v"(tg));
    float wc = __builtin_amdgcn_exp2f(mc - row_max16(mc));
    wc *= __builtin_amdgcn_rcpf(row_sum16(wc * lc));
    const float wt = dpp<0x158>(wc);
    o0 = wt * vt0;
    o1 = wt * vt1;
#define ACC(S) { unsigned t_, x0_, x1_;                                                                                            \
    asm volatile("v_bitop3_b32 %3, %6, %7, %13 bitop3:0x7e\n\tv_bfi_b32 %4, %8, %6, %10\n\tv_bfi_b32 %5, %8, %7, %11\n\t"          \
                 "v_and_or_b32 %2, %3, %9, %2\n\tv_fmac_f32_dpp %0, %12, %4 row_newbcast:" #S " row_mask:0xf bank_mask:0xf\n\t"    \
                 "v_fmac_f32_dpp %1, %12, %5 row_newbcast:" #S " row_mask:0xf bank_mask:0xf"                                       \
                 : "+v"(o0), "+v"(o1), "+v"(bad), "=&v"(t_), "=&v"(x0_), "=&v"(x1_)                                                \
                 : "v"(gs[S][0]), "v"(gs[S][1]), "s"(ks[S]), "s"(kb[S]), "v"(oc0), "v"(oc1), "v"(wc), "v"(tg)); }
    ACC(0) ACC(1) ACC(2) ACC(3) ACC(4) ACC(5) ACC(6) ACC(7)
#undef ACC
    if (__all(bad == 0)) break;
    gm = __builtin_amdgcn_raw_buffer_load_b64(rw, mlo, mlb, SC1);
    asm volatile("" : "+v"(gm));
  }
  const float rcv = dpp<0xB1>((lane & 1) ? o0 : o1);
  const bf16x2 res = {(__bf16)((lane & 1) ? rcv : o0), (__bf16)((lane & 1) ? o1 : rcv)};
  __builtin_amdgcn_raw_buffer_store_b32(__builtin_bit_cast(unsigned, res), ro, op, 0, 0);
}

extern "C" void kernel_launch(void* const* d_in, const int* in_sizes, int n_in, void* d_out, int out_size, void* d_ws,
                              size_t ws_size, hipStream_t stream) {
  constexpr unsigned kv_bytes = H * C * D * 2, flags = 0x00020000u;
  tree_attn_decode<<<H * SPLITS, WAVES * 64, 0, stream>>>(d_in[1], kv_bytes, flags, d_in[2], kv_bytes, flags, d_in[0],
                                                          d_ws, d_in[3],
                                                          d_in[4], d_in[5],
                                                          d_out);
}
```
